```python
import torch

def _softmax(x, axis=-1):
    return torch.softmax(x, dim=axis)
def _gelu(x):
    return torch.nn.functional.gelu(x, approximate="tanh")
def _silu(x):
    return torch.nn.functional.silu(x)
def _sigmoid(x):
    return torch.sigmoid(x)
def _layernorm(x, axis=-1, eps=1e-5):
    m = x.mean(dim=axis, keepdim=True)
    v = x.var(dim=axis, keepdim=True, unbiased=False)
    return (x - m) / torch.sqrt(v + eps)
def _scatter_add(tgt, idx, src, axis=0):
    return tgt.clone().index_add_(axis, idx, src)
def _segment_sum(src, seg, n):
    out = torch.zeros((n,) + src.shape[1:], dtype=src.dtype)
    return out.index_add_(0, seg, src)
def _pad(x, pw):
    flat = tuple(v for pair in reversed(pw) for v in pair)
    return torch.nn.functional.pad(x, flat)

kernel_name = 'recurrent_scan__linear_kernel_hedgehog__dense_activation_mish__prompt'

def setup_inputs():

    _w = (lambda a: (lambda t: t.to(torch.bfloat16) if t.is_floating_point() else t)(torch.as_tensor(a)))
    def _A_setup():
        B, S, D, H = 1, 2048, 640, 32
        Dh = D // H
        return {
            "x": _w((lambda *s: torch.randn(*s, dtype=torch.bfloat16) * 0.05)(B, S, D)),
            "wqkv": _w((lambda *s: torch.randn(*s, dtype=torch.bfloat16) * 0.4)(D, 3 * D)),
            "w_phi": _w((lambda *s: torch.randn(*s, dtype=torch.bfloat16) * 0.4)(H, Dh, Dh) * (Dh ** -0.5)),
            "wo": _w((lambda *s: torch.randn(*s, dtype=torch.bfloat16) * 0.2)(D, D)),
        }
    _A_in = _A_setup()

    def _B_setup():
        B, S, D, F = 1, 2048, 640, 1728
        return {
            "x": _w((lambda *s: torch.randn(*s, dtype=torch.bfloat16) * 0.05)(B, S, D)),
            "w1": _w((lambda *s: torch.randn(*s, dtype=torch.bfloat16) * 0.4)(D, F) * (D ** -0.5)),
            "b1": _w((lambda *s: torch.randn(*s, dtype=torch.bfloat16) * 0.4)(F)),
            "w2": _w((lambda *s: torch.randn(*s, dtype=torch.bfloat16) * 0.1)(F, D) * (F ** -0.5)),
        }
    _B_in = _B_setup()

    return {
        "A_x": _A_in["x"],
        "A_wqkv": _A_in["wqkv"],
        "A_w_phi": _A_in["w_phi"],
        "A_wo": _A_in["wo"],
        "B_x": _B_in["x"],
        "B_w1": _B_in["w1"],
        "B_b1": _B_in["b1"],
        "B_w2": _B_in["w2"],
    }

def reference(A_x, A_wqkv, A_w_phi, A_wo, B_x, B_w1, B_b1, B_w2):
    def _A_ref(x, wqkv, w_phi, wo):
        B, S, D = x.shape
        H = 32; Dh = D // H
        qkv = (lambda x, *d: x.permute(*d))((x @ wqkv).reshape(B, S, 3, H, Dh), 2, 0, 3, 1, 4)
        q, k, v = qkv[0], qkv[1], qkv[2]
        pq = torch.einsum("bhsd,hdf->bhsf", q, w_phi)
        pk = torch.einsum("bhsd,hdf->bhsf", k, w_phi)
        phi_q = torch.concatenate([_softmax(pq, axis=-1), _softmax(-pq, axis=-1)], axis=-1)
        phi_k = torch.concatenate([_softmax(pk, axis=-1), _softmax(-pk, axis=-1)], axis=-1)
        kv = torch.einsum("bhsf,bhsd->bhfd", phi_k, v)
        z  = torch.einsum("bhsf->bhf", phi_k)
        num = torch.einsum("bhqf,bhfd->bhqd", phi_q, kv)
        den = torch.einsum("bhqf,bhf->bhq", phi_q, z)[..., None] + 1e-6
        o = num / den
        return (lambda x, *d: x.permute(*d))(o, 0, 2, 1, 3).reshape(B, S, D) @ wo

    def _B_ref(x, w1, b1, w2):
        h = torch.einsum("bsd,df->bsf", x, w1) + b1
        sp = torch.log1p(torch.exp(-torch.abs(h))) + torch.where(h > 0, h, torch.zeros_like(h))
        act = h * torch.tanh(sp)
        return torch.einsum("bsf,fd->bsd", act, w2)

    x = A_x
    xn = _layernorm(x, axis=-1)
    s = _A_ref(xn, A_wqkv, A_w_phi, A_wo)
    g = _silu(_B_ref(xn, B_w1, B_b1, B_w2))
    return x + s * g
```

```cpp
#include <hip/hip_runtime.h>
#include <hip/hip_bf16.h>
#include <stdint.h>

namespace naive {
constexpr int S = 2048, D = 640, H = 32, DH = 20, F = 1728;
__device__ __forceinline__ float bf2f(uint16_t v) { return __uint_as_float((uint32_t)v << 16); }
__device__ __forceinline__ uint16_t f2bf(float f) { __hip_bfloat16 b = __float2bfloat16(f); return *(uint16_t*)&b; }

__global__ void ln_k(const uint16_t* x, float* xn) {
  int row = blockIdx.x; int t = threadIdx.x;
  float v[10]; float s = 0;
  for (int i = 0; i < 10; i++) { v[i] = bf2f(x[row * D + t + 64 * i]); s += v[i]; }
  for (int o = 32; o; o >>= 1) s += __shfl_xor(s, o);
  float m = s / D; float q = 0;
  for (int i = 0; i < 10; i++) { float d = v[i] - m; q += d * d; }
  for (int o = 32; o; o >>= 1) q += __shfl_xor(q, o);
  float r = 1.0f / sqrtf(q / D + 1e-5f);
  for (int i = 0; i < 10; i++) xn[row * D + t + 64 * i] = (v[i] - m) * r;
}
__global__ void gemm_k(const float* A, const uint16_t* B, float* C, int M, int N, int K) {
  __shared__ float As[16][17], Bs[16][17];
  int tx = threadIdx.x, ty = threadIdx.y;
  int row = blockIdx.y * 16 + ty, col = blockIdx.x * 16 + tx;
  float acc = 0;
  for (int k0 = 0; k0 < K; k0 += 16) {
    As[ty][tx] = A[row * K + k0 + tx];
    Bs[ty][tx] = bf2f(B[(k0 + ty) * N + col]);
    __syncthreads();
    for (int k = 0; k < 16; k++) acc += As[ty][k] * Bs[k][tx];
    __syncthreads();
  }
  C[row * N + col] = acc;
}
__global__ void mish_k(float* h, const uint16_t* b1) {
  int i = blockIdx.x * 256 + threadIdx.x;
  if (i >= S * F) return;
  float v = h[i] + bf2f(b1[i % F]);
  float sp = log1pf(expf(-fabsf(v))) + (v > 0 ? v : 0.f);
  h[i] = v * tanhf(sp);
}
__global__ void phi_k(const float* qkv, const uint16_t* wphi, float* phi, int which) {
  int i = blockIdx.x * 256 + threadIdx.x;
  int tok = i / H, h = i % H;
  const float* q = qkv + tok * 3 * D + which * D + h * DH;
  float p[DH];
  for (int f = 0; f < DH; f++) { float a = 0; for (int d = 0; d < DH; d++) a += q[d] * bf2f(wphi[(h * DH + d) * DH + f]); p[f] = a; }
  float mx = p[0], mn = p[0];
  for (int f = 1; f < DH; f++) { mx = fmaxf(mx, p[f]); mn = fminf(mn, p[f]); }
  float s1 = 0, s2 = 0; float e1[DH], e2[DH];
  for (int f = 0; f < DH; f++) { e1[f] = expf(p[f] - mx); e2[f] = expf(mn - p[f]); s1 += e1[f]; s2 += e2[f]; }
  float* o = phi + (size_t)i * 40;
  for (int f = 0; f < DH; f++) { o[f] = e1[f] / s1; o[20 + f] = e2[f] / s2; }
}
__global__ void kv_k(const float* phik, const float* qkv, float* kv) {
  int h = blockIdx.x; int t = threadIdx.x;
  if (t >= 840) return;
  int f = t / 21, d = t % 21;
  float a = 0;
  for (int s = 0; s < S; s++) {
    float v = d < 20 ? qkv[s * 3 * D + 2 * D + h * DH + d] : 1.0f;
    a += phik[((size_t)s * H + h) * 40 + f] * v;
  }
  kv[h * 840 + t] = a;
}
__global__ void o_k(const float* phiq, const float* kv, float* o) {
  int i = blockIdx.x * 256 + threadIdx.x;
  int tok = i / H, h = i % H;
  const float* p = phiq + (size_t)i * 40;
  float den = 0;
  for (int f = 0; f < 40; f++) den += p[f] * kv[h * 840 + f * 21 + 20];
  den += 1e-6f;
  for (int d = 0; d < DH; d++) { float a = 0; for (int f = 0; f < 40; f++) a += p[f] * kv[h * 840 + f * 21 + d]; o[tok * D + h * DH + d] = a / den; }
}
__global__ void fin_k(const uint16_t* x, const float* s, const float* y, uint16_t* out, float* outf) {
  int i = blockIdx.x * 256 + threadIdx.x;
  float yy = y[i];
  float g = yy / (1.0f + expf(-yy));
  float r = bf2f(x[i]) + s[i] * g;
  out[i] = f2bf(r);
  if (outf) outf[i] = r;
}
struct Bufs { float *xn, *qkv, *h, *phiq, *phik, *kv, *o, *s, *y; };
static Bufs carve(void* ws) {
  float* p = (float*)ws; Bufs b;
  b.xn = p; p += S * D; b.qkv = p; p += S * 3 * D; b.h = p; p += S * F; b.phiq = p; p += S * H * 40; b.phik = p; p += S * H * 40;
  b.kv = p; p += H * 840; b.o = p; p += S * D; b.s = p; p += S * D; b.y = p; p += S * D;
  return b;
}
static void run(void* const* d_in, void* d_out, void* d_ws, hipStream_t stream, float* outf = nullptr) {
  const uint16_t* x = (const uint16_t*)d_in[0]; const uint16_t* wqkv = (const uint16_t*)d_in[1];
  const uint16_t* wphi = (const uint16_t*)d_in[2]; const uint16_t* wo = (const uint16_t*)d_in[3];
  const uint16_t* w1 = (const uint16_t*)d_in[5]; const uint16_t* b1 = (const uint16_t*)d_in[6]; const uint16_t* w2 = (const uint16_t*)d_in[7];
  Bufs b = carve(d_ws);
  ln_k<<<S, 64, 0, stream>>>(x, b.xn);
  gemm_k<<<dim3(3 * D / 16, S / 16), dim3(16, 16), 0, stream>>>(b.xn, wqkv, b.qkv, S, 3 * D, D);
  gemm_k<<<dim3(F / 16, S / 16), dim3(16, 16), 0, stream>>>(b.xn, w1, b.h, S, F, D);
  mish_k<<<(S * F + 255) / 256, 256, 0, stream>>>(b.h, b1);
  phi_k<<<S * H / 256, 256, 0, stream>>>(b.qkv, wphi, b.phiq, 0);
  phi_k<<<S * H / 256, 256, 0, stream>>>(b.qkv, wphi, b.phik, 1);
  kv_k<<<H, 896, 0, stream>>>(b.phik, b.qkv, b.kv);
  o_k<<<S * H / 256, 256, 0, stream>>>(b.phiq, b.kv, b.o);
  gemm_k<<<dim3(D / 16, S / 16), dim3(16, 16), 0, stream>>>(b.o, wo, b.s, S, D, D);
  gemm_k<<<dim3(D / 16, S / 16), dim3(16, 16), 0, stream>>>(b.h, w2, b.y, S, D, F);
  fin_k<<<S * D / 256, 256, 0, stream>>>(x, b.s, b.y, (uint16_t*)d_out, outf);
}
}

extern "C" void kernel_launch(void* const* d_in, const int* in_sizes, int n_in, void* d_out, int out_size, void* d_ws, size_t ws_size, hipStream_t stream) {
  naive::run(d_in, d_out, d_ws, stream);
}
```

```cpp
#include <hip/hip_runtime.h>
#include <stdint.h>

typedef unsigned short u16;
typedef __attribute__((ext_vector_type(2))) __bf16 bf16x2;
typedef __attribute__((ext_vector_type(8))) __bf16 bf16x8;
typedef __attribute__((ext_vector_type(4))) short s16x4;
typedef __attribute__((ext_vector_type(2))) float f32x2;
typedef __attribute__((ext_vector_type(4))) float f32x4;
typedef __attribute__((ext_vector_type(2))) unsigned u32x2;
typedef __attribute__((ext_vector_type(4))) unsigned u32x4;
#define LDSP __attribute__((address_space(3)))
#define DEVI __device__ __forceinline__

constexpr unsigned OFF_ACT = 0;
constexpr unsigned OFF_WT = OFF_ACT + 128u * 54u * 1024u;
constexpr unsigned OFF_OT = OFF_WT + 40u * 74u * 1024u;
constexpr unsigned OFF_QT = OFF_OT + 128u * 20u * 1024u;
constexpr unsigned OFF_KVP = OFF_QT + 32u * 2048u * 48u;
constexpr unsigned WS_END = OFF_KVP + 32u * 16u * 3360u;

constexpr float L2E = 1.4426950408889634f;

DEVI unsigned pk2(float a, float b) {
  bf16x2 v = {(__bf16)a, (__bf16)b};
  return __builtin_bit_cast(unsigned, v);
}
DEVI float bflo(unsigned u) { return __uint_as_float(u << 16); }
DEVI float bfhi(unsigned u) { return __uint_as_float(u & 0xffff0000u); }
DEVI f32x4 mfma(u32x4 a, u32x4 b, f32x4 c) {
  return __builtin_amdgcn_mfma_f32_16x16x32_bf16(__builtin_bit_cast(bf16x8, a), __builtin_bit_cast(bf16x8, b), c, 0, 0, 0);
}
DEVI u32x2 trread(LDSP char* p) {
  return __builtin_bit_cast(u32x2, __builtin_amdgcn_ds_read_tr16_b64_v4i16((LDSP s16x4*)p));
}
DEVI u32x4 trread2(LDSP char* p0, LDSP char* p1) {
  u32x2 a = trread(p0), b = trread(p1);
  return u32x4{a[0], a[1], b[0], b[1]};
}
DEVI float dot2(unsigned a, unsigned b, float c) {
  return __builtin_amdgcn_fdot2_f32_bf16(__builtin_bit_cast(bf16x2, a), __builtin_bit_cast(bf16x2, b), c, false);
}
DEVI float dot_ones(u32x4 a, float c) {
  c = dot2(a[0], 0x3f803f80u, c); c = dot2(a[1], 0x3f803f80u, c);
  c = dot2(a[2], 0x3f803f80u, c); c = dot2(a[3], 0x3f803f80u, c);
  return c;
}
DEVI float dot_self(u32x4 a, float c) {
  c = dot2(a[0], a[0], c); c = dot2(a[1], a[1], c); c = dot2(a[2], a[2], c); c = dot2(a[3], a[3], c);
  return c;
}
DEVI float red_g(float v) {
  v += __shfl_xor(v, 16);
  v += __shfl_xor(v, 32);
  return v;
}
DEVI void glds16(unsigned voff, const void* sbase, unsigned lds_dst) {
  unsigned keep;
  asm volatile("s_mov_b32 %0, m0\n\ts_mov_b32 m0, %3\n\ts_nop 0\n\tglobal_load_lds_dwordx4 %1, %2\n\ts_mov_b32 m0, %0"
               : "=&s"(keep) : "v"(voff), "s"(sbase), "s"(lds_dst) : "memory");
}
#define WAIT_VM(n) asm volatile("s_waitcnt vmcnt(" #n ")" ::: "memory")
#define WAIT_LGKM0() asm volatile("s_waitcnt lgkmcnt(0)" ::: "memory")

DEVI void feature_map(const f32x4 (&p)[5], unsigned (&o)[20]) {
  float mx = p[0][0], mn = p[0][0];
#pragma unroll
  for (int t = 0; t < 5; ++t)
#pragma unroll
    for (int r = 0; r < 4; ++r) { mx = fmaxf(mx, p[t][r]); mn = fminf(mn, p[t][r]); }
  const float a = -mx * L2E, b = mn * L2E;
  float e1[20], e2[20], s1 = 0.f, s2 = 0.f;
#pragma unroll
  for (int t = 0; t < 5; ++t)
#pragma unroll
    for (int r = 0; r < 4; ++r) {
      e1[4 * t + r] = __builtin_amdgcn_exp2f(fmaf(p[t][r], L2E, a));
      e2[4 * t + r] = __builtin_amdgcn_exp2f(fmaf(p[t][r], -L2E, b));
      s1 += e1[4 * t + r]; s2 += e2[4 * t + r];
    }
  const float r1 = __builtin_amdgcn_rcpf(s1), r2 = __builtin_amdgcn_rcpf(s2);
#pragma unroll
  for (int i = 0; i < 10; ++i) {
    o[i] = pk2(e1[2 * i] * r1, e1[2 * i + 1] * r1);
    o[10 + i] = pk2(e2[2 * i] * r2, e2[2 * i + 1] * r2);
  }
}
DEVI void head_proj(LDSP char* wl, const u32x4 (&b)[3], f32x4 (&p)[5]) {
#pragma unroll
  for (int t = 0; t < 5; ++t) {
    u32x4 a0 = trread2(wl + 0 * 40 + t * 8, wl + 4 * 40 + t * 8);
    u32x4 a1 = trread2(wl + 8 * 40 + t * 8, wl + 12 * 40 + t * 8);
    u32x2 h2 = trread(wl + 16 * 40 + t * 8);
    u32x4 a2 = {h2[0], h2[1], 0u, 0u};
    f32x4 c = {0.f, 0.f, 0.f, 0.f};
    c = mfma(a0, b[0], c);
    c = mfma(a1, b[1], c);
    c = mfma(a2, b[2], c);
    p[t] = c;
  }
}

constexpr unsigned XS_BYTES = 16384, WSUB = 10240, STAGE = XS_BYTES + 3 * WSUB;
constexpr unsigned K1_WPHI = 3 * STAGE;
constexpr unsigned K1_ZERO = K1_WPHI + 3200;
constexpr unsigned K1_STAT = K1_ZERO + 768;
constexpr unsigned K1_CS = K1_STAT + 1024;
constexpr unsigned K1_LDS = K1_CS + 960;
constexpr unsigned PK_PITCH = 352, V_PITCH = 224, K1_VIMG = 128 * PK_PITCH;

__global__ __launch_bounds__(768, 3) void hedgehog_k1(const u16* __restrict__ x, const u16* __restrict__ wqkv,
                                                      const u16* __restrict__ wphi, const u16* __restrict__ wo,
                                                      const u16* __restrict__ w1, const u16* __restrict__ b1,
                                                      const u16* __restrict__ w2, char* __restrict__ ws) {
  extern __shared__ __attribute__((aligned(16))) char smem[];
  LDSP char* lds = (LDSP char*)smem;
  const unsigned ldsbase = (unsigned)(size_t)lds;
  const int tid = threadIdx.x, lane = tid & 63;
  const int wave = __builtin_amdgcn_readfirstlane(tid >> 6);
  const int mi = wave & 3, nj = wave >> 2;
  const int g = lane >> 4, m = lane & 15, q4 = m >> 2, p4 = lane & 3;
  const int b = blockIdx.x, xcd = b & 7, jx = b >> 3;
  const int rb = 4 * (xcd >> 1) + (jx & 3);
  const int cgi = jx >> 2;
  const bool atype = cgi < 4;
  const int grp = 4 * (xcd & 1) + (cgi & 3);
  const u16* W = atype ? wqkv : w1;
  const int ldw = atype ? 1920 : 1728;
  const int c0 = atype ? 80 * grp : (grp < 6 ? 224 * grp : 1344 + 192 * (grp - 6));
  const int sub = atype ? 640 : (grp < 6 ? 80 : 64);
  const bool five = atype || (grp < 6 && nj < 2);

  unsigned voff[4];
  const char* sptr[4];
  unsigned sstr[4];
#pragma unroll
  for (int i = 0; i < 4; ++i) {
    const int s = wave + 12 * i;
    if (s < 16) {
      const int c = 64 * s + lane, row = c >> 3, ch = (c & 7) ^ ((row >> 1) & 7);
      voff[i] = row * 1280 + ch * 16;
      sptr[i] = (const char*)x + (size_t)rb * 128 * 1280;
      sstr[i] = 128;
    } else {
      const int s2 = (s < 46 ? s : 45) - 16, sj = s2 / 10, ss = s2 - 10 * sj;
      const int c = 64 * ss + lane, rho = c / 10, cc = c - 10 * rho;
      const int k = (rho & 0x33) | ((rho & 4) << 1) | ((rho & 8) >> 1);
      int col = c0 + sub * sj + 8 * cc;
      col = col > ldw - 8 ? ldw - 8 : col;
      voff[i] = (k * ldw + col) * 2;
      sptr[i] = (const char*)W;
      sstr[i] = 128 * ldw;
    }
  }
  const bool slot4 = wave < 10;
  auto issue = [&](unsigned stage_off) {
    const unsigned dst = ldsbase + stage_off + wave * 1024;
#pragma unroll
    for (int i = 0; i < 3; ++i) { glds16(voff[i], sptr[i], dst + i * 12288); sptr[i] += sstr[i]; }
    if (slot4) { glds16(voff[3], sptr[3], dst + 3 * 12288); sptr[3] += sstr[3]; }
  };
  issue(0);

  const int gw = b * 12 + wave;
  {
    const int sid = gw < 2560 ? gw : 2559;
    *(u32x4*)(ws + OFF_OT + (size_t)sid * 1024 + lane * 16) = u32x4{~0u, ~0u, ~0u, ~0u};
  }
  const int fid = gw < 2960 ? gw : 2959;
  u16 rt_v[8];
  unsigned rt_dst;
  {
    const u16* src; int ct, lo, hi;
    if (fid < 2160) {
      ct = fid / 54; const int ks = fid - 54 * ct;
      src = w2; lo = 32 * ks + 4 * g; hi = lo + 16;
      rt_dst = OFF_WT + (ct * 74 + ks) * 1024;
    } else {
      const int f2 = fid - 2160; ct = f2 / 20; const int ks = f2 - 20 * ct;
      const int e = 4 * ks + g, i5 = e / 5, r5 = e - 5 * i5;
      src = wo;
      lo = 40 * i5 + (r5 == 4 ? 16 : (r5 >> 1) * 20 + (r5 & 1) * 8);
      hi = r5 == 4 ? lo + 20 : lo + 4;
      rt_dst = OFF_WT + (ct * 74 + 54 + ks) * 1024;
    }
    const u16* pl = src + lo * 640 + 16 * ct + m;
    const u16* ph = src + hi * 640 + 16 * ct + m;
#pragma unroll
    for (int j = 0; j < 4; ++j) { rt_v[j] = pl[j * 640]; rt_v[4 + j] = ph[j * 640]; }
  }
  u32x2 bias[5];
  u32x4 wphi_v = {0u, 0u, 0u, 0u};
  if (atype) {
#pragma unroll
    for (int ct = 0; ct < 5; ++ct) bias[ct] = u32x2{0u, 0u};
    if (tid < 200) wphi_v = *(const u32x4*)(wphi + grp * 1600 + tid * 8);
  } else {
#pragma unroll
    for (int ct = 0; ct < 5; ++ct) {
      int col = c0 + sub * nj + 16 * ct + 4 * g;
      col = col > 1724 ? 1724 : col;
      bias[ct] = *(const u32x2*)(b1 + col);
    }
  }
  issue(STAGE);

  f32x4 acc[2][5];
#pragma unroll
  for (int r = 0; r < 2; ++r)
#pragma unroll
    for (int c = 0; c < 5; ++c) acc[r][c] = f32x4{0.f, 0.f, 0.f, 0.f};
  float st1 = 0.f, st2 = 0.f, csa = 0.f, csb = 0.f;
  const unsigned xa0 = (mi * 32 + m) * 128 + ((g ^ (m >> 1)) << 4);
  const unsigned xa1 = xa0 ^ 64;
  const unsigned pstr = atype ? 40 : 8, cstr = atype ? 8 : 32;
  const unsigned wa = XS_BYTES + nj * WSUB + (16 * (g >> 1) + 4 * (g & 1) + q4) * 160 + p4 * pstr;
  unsigned cur = 0, nxt = 2 * STAGE;
  for (int t = 0; t < 10; ++t) {
    if (t < 9) { if (slot4) WAIT_VM(4); else WAIT_VM(3); } else WAIT_VM(0);
    WAIT_LGKM0();
    __builtin_amdgcn_s_barrier();
    if (t < 8) issue(nxt);
    LDSP char* sx = lds + cur;
    u32x4 xf[2][2], wf[2][5];
    xf[0][0] = *(LDSP u32x4*)(sx + xa0);
    xf[0][1] = *(LDSP u32x4*)(sx + xa0 + 2048);
#pragma unroll
    for (int c = 0; c < 4; ++c) wf[0][c] = trread2(sx + wa + c * cstr, sx + wa + c * cstr + 1280);
    xf[1][0] = *(LDSP u32x4*)(sx + xa1);
    xf[1][1] = *(LDSP u32x4*)(sx + xa1 + 2048);
#pragma unroll
    for (int c = 0; c < 4; ++c) wf[1][c] = trread2(sx + wa + c * cstr + 5120, sx + wa + c * cstr + 6400);
#pragma unroll
    for (int ks = 0; ks < 2; ++ks)
#pragma unroll
      for (int c = 0; c < 4; ++c) {
        acc[0][c] = mfma(wf[ks][c], xf[ks][0], acc[0][c]);
        acc[1][c] = mfma(wf[ks][c], xf[ks][1], acc[1][c]);
      }
    if (five) {
      wf[0][4] = trread2(sx + wa + 4 * cstr, sx + wa + 4 * cstr + 1280);
      wf[1][4] = trread2(sx + wa + 4 * cstr + 5120, sx + wa + 4 * cstr + 6400);
#pragma unroll
      for (int ks = 0; ks < 2; ++ks) {
        acc[0][4] = mfma(wf[ks][4], xf[ks][0], acc[0][4]);
        acc[1][4] = mfma(wf[ks][4], xf[ks][1], acc[1][4]);
      }
      if (mi == 0) { csb = dot_ones(wf[0][4], csb); csb = dot_ones(wf[1][4], csb); }
    }
    if (mi == 0) { csa = dot_ones(wf[0][0], csa); csa = dot_ones(wf[1][0], csa); }
    else if (mi == 1) { csa = dot_ones(wf[0][1], csa); csa = dot_ones(wf[1][1], csa); }
    else if (mi == 2) { csa = dot_ones(wf[0][2], csa); csa = dot_ones(wf[1][2], csa); }
    else { csa = dot_ones(wf[0][3], csa); csa = dot_ones(wf[1][3], csa); }
    if (nj == 0) {
      st1 = dot_ones(xf[0][0], st1); st2 = dot_self(xf[0][0], st2);
      st1 = dot_ones(xf[1][0], st1); st2 = dot_self(xf[1][0], st2);
    } else if (nj == 1) {
      st1 = dot_ones(xf[0][1], st1); st2 = dot_self(xf[0][1], st2);
      st1 = dot_ones(xf[1][1], st1); st2 = dot_self(xf[1][1], st2);
    }
    nxt = cur;
    cur = cur == 2 * STAGE ? 0 : cur + STAGE;
  }

  csa = red_g(csa);
  csb = red_g(csb);
  st1 = red_g(st1);
  st2 = red_g(st2);
  if (g == 0) {
    *(LDSP float*)(lds + K1_CS + (nj * 80 + mi * 16 + m) * 4) = csa;
    if (mi == 0) *(LDSP float*)(lds + K1_CS + (nj * 80 + 64 + m) * 4) = csb;
    if (nj < 2) {
      const float mean = st1 * (1.0f / 640.0f);
      const float var = fmaxf(st2 * (1.0f / 640.0f) - mean * mean, 0.f);
      float rs = __builtin_amdgcn_rsqf(var + 1e-5f);
      if (!atype) rs *= L2E;
      *(LDSP f32x2*)(lds + K1_STAT + (mi * 32 + nj * 16 + m) * 8) = f32x2{rs, -mean * rs};
    }
  }
  if (tid < 200) *(LDSP u32x4*)(lds + K1_WPHI + tid * 16) = wphi_v;
  else if (tid < 248) *(LDSP u32x4*)(lds + K1_ZERO + (tid - 200) * 16) = u32x4{0u, 0u, 0u, 0u};
  {
    u32x4 v;
#pragma unroll
    for (int j = 0; j < 4; ++j) v[j] = (unsigned)rt_v[2 * j] | ((unsigned)rt_v[2 * j + 1] << 16);
    *(u32x4*)(ws + rt_dst + lane * 16) = v;
  }
  __syncthreads();

  f32x2 stt[2];
  stt[0] = *(LDSP f32x2*)(lds + K1_STAT + (mi * 32 + m) * 8);
  stt[1] = *(LDSP f32x2*)(lds + K1_STAT + (mi * 32 + 16 + m) * 8);
  f32x4 cs[5];
#pragma unroll
  for (int c = 0; c < 5; ++c) cs[c] = *(LDSP f32x4*)(lds + K1_CS + (nj * 80 + c * 16 + 4 * g) * 4);

  if (!atype) {
    const int T0 = (c0 + sub * nj) >> 4;
    const int odd = T0 & 1;
#pragma unroll
    for (int r = 0; r < 2; ++r) {
      u32x2 pk[5];
#pragma unroll
      for (int c = 0; c < 5; ++c) {
        float res[4];
#pragma unroll
        for (int e = 0; e < 4; ++e) {
          const unsigned bw = bias[c][e >> 1];
          const float bl = ((e & 1) ? bfhi(bw) : bflo(bw)) * L2E;
          const float hl = fmaf(acc[r][c][e], stt[r][0], fmaf(stt[r][1], cs[c][e], bl));
          const float ex = __builtin_amdgcn_exp2f(fminf(hl, 40.f));
          const float n = ex * (ex + 2.f);
          const float rc = __builtin_amdgcn_rcpf(fmaf(n, L2E, 2.f * L2E));
          res[e] = hl * (n * rc);
        }
        pk[c] = u32x2{pk2(res[0], res[1]), pk2(res[2], res[3])};
      }
      char* base = ws + OFF_ACT + ((size_t)((rb * 8 + mi * 2 + r) * 54 + (T0 >> 1)) * 64 + lane) * 16;
      if (!odd) {
        *(u32x4*)(base) = u32x4{pk[0][0], pk[0][1], pk[1][0], pk[1][1]};
        *(u32x4*)(base + 1024) = u32x4{pk[2][0], pk[2][1], pk[3][0], pk[3][1]};
        if (five) *(u32x2*)(base + 2048) = pk[4];
      } else {
        *(u32x2*)(base + 8) = pk[0];
        *(u32x4*)(base + 1024) = u32x4{pk[1][0], pk[1][1], pk[2][0], pk[2][1]};
        *(u32x4*)(base + 2048) = u32x4{pk[3][0], pk[3][1], pk[4][0], pk[4][1]};
      }
    }
    return;
  }

#pragma unroll
  for (int r = 0; r < 2; ++r) {
    u32x4 bq[3];
#pragma unroll
    for (int c = 0; c < 5; ++c) {
      float v[4];
#pragma unroll
      for (int e = 0; e < 4; ++e) v[e] = fmaf(acc[r][c][e], stt[r][0], stt[r][1] * cs[c][e]);
      bq[c >> 1][(c & 1) * 2] = pk2(v[0], v[1]);
      bq[c >> 1][(c & 1) * 2 + 1] = pk2(v[2], v[3]);
    }
    bq[2][2] = (nj == 2) ? 0x00003f80u : 0u;
    bq[2][3] = 0u;
    const int tok = mi * 32 + r * 16 + m;
    if (nj == 0) {
      char* dst = ws + OFF_QT + ((size_t)(4 * grp + g) * 2048 + rb * 128 + tok) * 48;
      *(u32x4*)(dst) = bq[0];
      *(u32x4*)(dst + 16) = bq[1];
      *(u32x4*)(dst + 32) = bq[2];
    } else if (nj == 2) {
      LDSP char* dst = lds + K1_VIMG + tok * V_PITCH + g * 48;
      *(LDSP u32x4*)(dst) = bq[0];
      *(LDSP u32x4*)(dst + 16) = bq[1];
      *(LDSP u32x4*)(dst + 32) = bq[2];
    } else {
      LDSP char* wl = lds + ((p4 == g) ? K1_WPHI + g * 800 + q4 * 40 : K1_ZERO);
      f32x4 pk[5];
      head_proj(wl, bq, pk);
      unsigned ph[20];
      feature_map(pk, ph);
      LDSP char* dst = lds + tok * PK_PITCH + g * 80;
#pragma unroll
      for (int i = 0; i < 5; ++i) *(LDSP u32x4*)(dst + i * 16) = u32x4{ph[4 * i], ph[4 * i + 1], ph[4 * i + 2], ph[4 * i + 3]};
    }
  }
  __syncthreads();
  {
    LDSP char* pa = lds + (4 * g + q4) * PK_PITCH + mi * 80 + nj * 32 + p4 * 8;
    LDSP char* pb = lds + K1_VIMG + (4 * g + q4) * V_PITCH + mi * 48 + p4 * 8;
    f32x4 d0 = {0.f, 0.f, 0.f, 0.f}, d1 = {0.f, 0.f, 0.f, 0.f};
#pragma unroll
    for (int kk = 0; kk < 4; ++kk) {
      u32x4 a = trread2(pa + (32 * kk) * PK_PITCH, pa + (32 * kk + 16) * PK_PITCH);
      u32x4 v0 = trread2(pb + (32 * kk) * V_PITCH, pb + (32 * kk + 16) * V_PITCH);
      u32x4 v1 = trread2(pb + (32 * kk) * V_PITCH + 32, pb + (32 * kk + 16) * V_PITCH + 32);
      d0 = mfma(a, v0, d0);
      d1 = mfma(a, v1, d1);
    }
    if (16 * nj + 4 * g < 40) {
      char* dst = ws + OFF_KVP + (size_t)((4 * grp + mi) * 16 + rb) * 3360 + (16 * nj + 4 * g) * 4;
      *(f32x4*)(dst + m * 160) = d0;
      if (m < 5) *(f32x4*)(dst + (16 + m) * 160) = d1;
    }
  }
}

constexpr unsigned K2_YD = 0;
constexpr unsigned K2_SD = 4 * 15360;
constexpr unsigned K2_KVT = K2_SD + 4 * 20480;
constexpr unsigned K2_ZERO = K2_KVT + 1920;
constexpr unsigned K2_WPHI = K2_ZERO + 1792;
constexpr unsigned K2_FLAG = K2_WPHI + 3200;
constexpr unsigned K2_LDS = K2_FLAG + 16;

DEVI u32x4 bld(__amdgpu_buffer_rsrc_t rs, unsigned voff, unsigned soff) {
  return __builtin_amdgcn_raw_buffer_load_b128(rs, voff, soff, 0);
}
DEVI u32x4 bld_sc1(__amdgpu_buffer_rsrc_t rs, unsigned voff, unsigned soff) {
  return __builtin_amdgcn_raw_buffer_load_b128(rs, voff, soff, 16);
}

__global__ __launch_bounds__(512, 2) void hedgehog_k2(const u16* __restrict__ x, const u16* __restrict__ wphi,
                                                      char* __restrict__ ws, u16* __restrict__ out) {
  extern __shared__ __attribute__((aligned(16))) char smem[];
  LDSP char* lds = (LDSP char*)smem;
  const int tid = threadIdx.x, lane = tid & 63;
  const int wave = __builtin_amdgcn_readfirstlane(tid >> 6);
  const int g = lane >> 4, m = lane & 15, q4 = m >> 2, p4 = lane & 3;
  const int b = blockIdx.x, xcd = b & 7, jx = b >> 3;
  const int rb2 = 8 * (xcd >> 1) + (jx & 7), cb = 4 * (xcd & 1) + (jx >> 3);
  const __amdgpu_buffer_rsrc_t rs = __builtin_amdgcn_make_buffer_rsrc((void*)ws, 0, WS_END, 0x00020000);
  const unsigned l16 = lane * 16;
  f32x4 acc[4][5];
#pragma unroll
  for (int r = 0; r < 4; ++r)
#pragma unroll
    for (int c = 0; c < 5; ++c) acc[r][c] = f32x4{0.f, 0.f, 0.f, 0.f};

  if (wave < 4) {
    const int ks0 = wave < 2 ? 14 * wave : 28 + 13 * (wave - 2);
    const bool n14 = wave < 2;
    unsigned ab[4], bb[5];
#pragma unroll
    for (int l = 0; l < 4; ++l) ab[l] = OFF_ACT + ((4 * rb2 + ((l + wave) & 3)) * 54 + ks0) * 1024;
#pragma unroll
    for (int c = 0; c < 5; ++c) bb[c] = OFF_WT + ((5 * cb + c) * 74 + ks0) * 1024;
    u32x4 A[3][4], B[3][5];
    auto load = [&](int i) {
#pragma unroll
      for (int l = 0; l < 4; ++l) A[i % 3][l] = bld(rs, l16, ab[l] + i * 1024);
#pragma unroll
      for (int c = 0; c < 5; ++c) B[i % 3][c] = bld(rs, l16, bb[c] + i * 1024);
    };
    auto comp = [&](int i) {
#pragma unroll
      for (int l = 0; l < 4; ++l)
#pragma unroll
        for (int c = 0; c < 5; ++c) acc[l][c] = mfma(B[i % 3][c], A[i % 3][l], acc[l][c]);
    };
    load(0);
    load(1);
    u32x2 xv[5];
    const size_t orow = (size_t)(64 * rb2 + 16 * wave + m) * 640 + 80 * cb + 4 * g;
#pragma unroll
    for (int c = 0; c < 5; ++c) xv[c] = *(const u32x2*)(x + orow + 16 * c);
    __builtin_amdgcn_s_barrier();
#pragma unroll
    for (int i = 0; i < 13; ++i) {
      if (i + 2 < 13) load(i + 2);
      else if (i + 2 == 13) { if (n14) load(13); }
      __builtin_amdgcn_sched_barrier(0);
      comp(i);
      __builtin_amdgcn_sched_barrier(0);
    }
    if (n14) comp(13);
#pragma unroll
    for (int l = 1; l < 4; ++l)
#pragma unroll
      for (int c = 0; c < 5; ++c)
        *(LDSP f32x4*)(lds + K2_YD + wave * 15360 + ((l - 1) * 5 + c) * 1024 + l16) = acc[l][c];
    __syncthreads();
#pragma unroll
    for (int c = 0; c < 5; ++c) {
      f32x4 y = acc[0][c];
#pragma unroll
      for (int l = 1; l < 4; ++l)
        y += *(LDSP f32x4*)(lds + K2_YD + ((wave - l) & 3) * 15360 + ((l - 1) * 5 + c) * 1024 + l16);
      f32x4 s = *(LDSP f32x4*)(lds + K2_SD + (wave * 5 + c) * 1024 + l16);
#pragma unroll
      for (int a = 1; a < 4; ++a) s += *(LDSP f32x4*)(lds + K2_SD + a * 20480 + (wave * 5 + c) * 1024 + l16);
      float o[4];
#pragma unroll
      for (int e = 0; e < 4; ++e) {
        const float gate = y[e] * __builtin_amdgcn_rcpf(1.f + __builtin_amdgcn_exp2f(-y[e] * L2E));
        const unsigned xw = xv[c][e >> 1];
        o[e] = fmaf(s[e], gate, (e & 1) ? bfhi(xw) : bflo(xw));
      }
      *(u32x2*)(out + orow + 16 * c) = u32x2{pk2(o[0], o[1]), pk2(o[2], o[3])};
    }
    return;
  }

  const int aw = wave - 4;
  const int h = 4 * xcd + (jx & 3), sb = jx >> 2;
  const int tok = sb * 256 + aw * 64 + lane;
  if (aw == 0) {
    *(LDSP u32x4*)(lds + K2_ZERO + l16) = u32x4{0u, 0u, 0u, 0u};
    if (lane < 48) *(LDSP u32x4*)(lds + K2_ZERO + 1024 + l16) = u32x4{0u, 0u, 0u, 0u};
    if (lane == 0) *(LDSP u32x4*)(lds + K2_FLAG) = u32x4{0u, 0u, 0u, 0u};
  }
  u32x4 wv = *(const u32x4*)(wphi + h * 400 + (lane < 50 ? lane : 49) * 8);
  u32x4 bq[3];
#pragma unroll
  for (int s = 0; s < 3; ++s) bq[s] = bld(rs, (unsigned)tok * 48, OFF_QT + h * 2048 * 48 + s * 16);
  int item = aw * 53 + (lane < 53 ? lane : 52);
  item = item < 210 ? item : 209;
  u32x4 part[16];
#pragma unroll
  for (int r = 0; r < 16; ++r) part[r] = bld(rs, (unsigned)item * 16, OFF_KVP + (h * 16 + r) * 3360);
  WAIT_LGKM0();
  __builtin_amdgcn_s_barrier();
  if (lane < 50) *(LDSP u32x4*)(lds + K2_WPHI + aw * 800 + l16) = wv;
  unsigned ph[20];
  {
    LDSP char* wl = lds + ((p4 == g) ? K2_WPHI + aw * 800 + q4 * 40 : K2_ZERO);
    f32x4 pq[5];
    head_proj(wl, bq, pq);
    feature_map(pq, ph);
  }
  {
    f32x4 sum = __builtin_bit_cast(f32x4, part[0]);
#pragma unroll
    for (int r = 1; r < 16; ++r) sum += __builtin_bit_cast(f32x4, part[r]);
    const int d = item / 10, fq = item - 10 * d;
    *(LDSP u32x2*)(lds + K2_KVT + d * 80 + fq * 8) = u32x2{pk2(sum[0], sum[1]), pk2(sum[2], sum[3])};
    WAIT_LGKM0();
    if (lane == 0) *(volatile LDSP unsigned*)(lds + K2_FLAG + aw * 4) = 1u;
    for (int spin = 0; spin < (1 << 22); ++spin) {
      const u32x4 f = *(volatile LDSP u32x4*)(lds + K2_FLAG);
      if (__builtin_amdgcn_readfirstlane(f[0] & f[1] & f[2] & f[3])) break;
      __builtin_amdgcn_s_sleep(1);
    }
    asm volatile("" ::: "memory");
  }
  const int ks0 = 5 * aw;
  u32x4 B[5][5];
#pragma unroll
  for (int i = 0; i < 2; ++i)
#pragma unroll
    for (int c = 0; c < 5; ++c) B[i][c] = bld(rs, l16, OFF_WT + ((5 * cb + c) * 74 + 54 + ks0 + i) * 1024);
  {
    LDSP char* kl = lds + (((m >> 2) == g) ? K2_KVT + (m & 3) * 80 : K2_ZERO);
    f32x4 nd[6];
#pragma unroll
    for (int t = 0; t < 6; ++t) {
      f32x4 c = {0.f, 0.f, 0.f, 0.f};
#pragma unroll
      for (int s = 0; s < 5; ++s)
        c = mfma(*(LDSP u32x4*)(kl + t * 320 + s * 16), u32x4{ph[4 * s], ph[4 * s + 1], ph[4 * s + 2], ph[4 * s + 3]}, c);
      nd[t] = c;
    }
    const float rd = __builtin_amdgcn_rcpf(nd[5][0] + 1e-6f);
    unsigned ow[10];
#pragma unroll
    for (int t = 0; t < 5; ++t) {
      ow[2 * t] = min(pk2(nd[t][0] * rd, nd[t][1] * rd), 0xFFFFFFFEu);
      ow[2 * t + 1] = min(pk2(nd[t][2] * rd, nd[t][3] * rd), 0xFFFFFFFEu);
    }
    const int e0 = 5 * (h >> 1) + 2 * (h & 1), e1 = e0 + 1, e4 = 5 * (h >> 1) + 4;
    const unsigned vo = (unsigned)(tok >> 4) * 20480 + m * 16;
    __builtin_amdgcn_raw_buffer_store_b128(u32x4{ow[0], ow[1], ow[2], ow[3]}, rs, vo, OFF_OT + (e0 >> 2) * 1024 + (e0 & 3) * 256, 16);
    __builtin_amdgcn_raw_buffer_store_b128(u32x4{ow[4], ow[5], ow[6], ow[7]}, rs, vo, OFF_OT + (e1 >> 2) * 1024 + (e1 & 3) * 256, 16);
    __builtin_amdgcn_raw_buffer_store_b64(u32x2{ow[8], ow[9]}, rs, vo, OFF_OT + (e4 >> 2) * 1024 + (e4 & 3) * 256 + (h & 1) * 8, 16);
  }
  const unsigned ob = OFF_OT + (4 * rb2 * 20 + ks0) * 1024;
  {
    const int l2 = lane < 40 ? lane : 39;
    const unsigned pv = 3 * 20480 + (l2 >> 3) * 1024 + ((l2 >> 1) & 3) * 256 + 15 * 16 + (l2 & 1) * 8;
    for (int spin = 0; spin < (1 << 20); ++spin) {
      asm volatile("" ::: "memory");
      const unsigned w = __builtin_amdgcn_raw_buffer_load_b32(rs, pv, ob, 16);
      if (!__any(w == 0xFFFFFFFFu)) break;
      __builtin_amdgcn_s_sleep(4);
    }
  }
  u32x4 A[5][4];
  for (int spin = 0; spin < (1 << 16); ++spin) {
    asm volatile("" ::: "memory");
    unsigned mx = 0;
#pragma unroll
    for (int i = 0; i < 5; ++i)
#pragma unroll
      for (int l = 0; l < 4; ++l) A[i][l] = bld_sc1(rs, l16, ob + l * 20480 + i * 1024);
#pragma unroll
    for (int i = 0; i < 5; ++i)
#pragma unroll
      for (int l = 0; l < 4; ++l) mx = max(max(mx, max(A[i][l][0], A[i][l][1])), max(A[i][l][2], A[i][l][3]));
    if (!__any(mx == 0xFFFFFFFFu)) break;
    __builtin_amdgcn_s_sleep(4);
  }
#pragma unroll
  for (int i = 0; i < 5; ++i) {
    if (i + 2 < 5) {
#pragma unroll
      for (int c = 0; c < 5; ++c) B[i + 2][c] = bld(rs, l16, OFF_WT + ((5 * cb + c) * 74 + 54 + ks0 + i + 2) * 1024);
    }
    __builtin_amdgcn_sched_barrier(0);
#pragma unroll
    for (int l = 0; l < 4; ++l)
#pragma unroll
      for (int c = 0; c < 5; ++c) acc[l][c] = mfma(B[i][c], A[i][l], acc[l][c]);
    __builtin_amdgcn_sched_barrier(0);
  }
#pragma unroll
  for (int l = 0; l < 4; ++l)
#pragma unroll
    for (int c = 0; c < 5; ++c) *(LDSP f32x4*)(lds + K2_SD + aw * 20480 + (l * 5 + c) * 1024 + l16) = acc[l][c];
  __syncthreads();
}

extern "C" void kernel_launch(void* const* d_in, const int* in_sizes, int n_in, void* d_out, int out_size, void* d_ws,
                              size_t ws_size, hipStream_t stream) {
  const u16* x = (const u16*)d_in[0];
  const u16* wqkv = (const u16*)d_in[1];
  const u16* wphi = (const u16*)d_in[2];
  const u16* wo = (const u16*)d_in[3];
  const u16* w1 = (const u16*)d_in[5];
  const u16* b1 = (const u16*)d_in[6];
  const u16* w2 = (const u16*)d_in[7];
  (void)hipFuncSetAttribute((const void*)hedgehog_k1, hipFuncAttributeMaxDynamicSharedMemorySize, K1_LDS);
  (void)hipFuncSetAttribute((const void*)hedgehog_k2, hipFuncAttributeMaxDynamicSharedMemorySize, K2_LDS);
  hedgehog_k1<<<256, 768, K1_LDS, stream>>>(x, wqkv, wphi, wo, w1, b1, w2, (char*)d_ws);
  hedgehog_k2<<<256, 512, K2_LDS, stream>>>(x, wphi, (char*)d_ws, (u16*)d_out);
}
```

```cpp
#include <hip/hip_runtime.h>
#include <stdint.h>

typedef unsigned short u16;
typedef __attribute__((ext_vector_type(2))) __bf16 bf16x2;
typedef __attribute__((ext_vector_type(8))) __bf16 bf16x8;
typedef __attribute__((ext_vector_type(4))) short s16x4;
typedef __attribute__((ext_vector_type(2))) float f32x2;
typedef __attribute__((ext_vector_type(4))) float f32x4;
typedef __attribute__((ext_vector_type(2))) unsigned u32x2;
typedef __attribute__((ext_vector_type(4))) unsigned u32x4;
#define LDSP __attribute__((address_space(3)))
#define DEVI __device__ __forceinline__

constexpr unsigned OFF_ACT = 0;
constexpr unsigned OFF_WT = OFF_ACT + 128u * 54u * 1024u;
constexpr unsigned OFF_OT = OFF_WT + 40u * 74u * 1024u;
constexpr unsigned OFF_QT = OFF_OT + 128u * 20u * 1024u;
constexpr unsigned OFF_KVP = OFF_QT + 32u * 2048u * 48u;
constexpr unsigned WS_END = OFF_KVP + 32u * 16u * 3360u;

#define K1_MAP 1
constexpr float L2E = 1.4426950408889634f;

DEVI unsigned pk2(float a, float b) {
  bf16x2 v = {(__bf16)a, (__bf16)b};
  return __builtin_bit_cast(unsigned, v);
}
DEVI float bflo(unsigned u) { return __uint_as_float(u << 16); }
DEVI float bfhi(unsigned u) { return __uint_as_float(u & 0xffff0000u); }
DEVI f32x4 mfma(u32x4 a, u32x4 b, f32x4 c) {
  return __builtin_amdgcn_mfma_f32_16x16x32_bf16(__builtin_bit_cast(bf16x8, a), __builtin_bit_cast(bf16x8, b), c, 0, 0, 0);
}
DEVI u32x2 trread(LDSP char* p) {
  return __builtin_bit_cast(u32x2, __builtin_amdgcn_ds_read_tr16_b64_v4i16((LDSP s16x4*)p));
}
DEVI u32x4 trread2(LDSP char* p0, LDSP char* p1) {
  u32x2 a = trread(p0), b = trread(p1);
  return u32x4{a[0], a[1], b[0], b[1]};
}
DEVI float dot2(unsigned a, unsigned b, float c) {
  return __builtin_amdgcn_fdot2_f32_bf16(__builtin_bit_cast(bf16x2, a), __builtin_bit_cast(bf16x2, b), c, false);
}
DEVI float dot_ones(u32x4 a, float c) {
  c = dot2(a[0], 0x3f803f80u, c); c = dot2(a[1], 0x3f803f80u, c);
  c = dot2(a[2], 0x3f803f80u, c); c = dot2(a[3], 0x3f803f80u, c);
  return c;
}
DEVI float dot_self(u32x4 a, float c) {
  c = dot2(a[0], a[0], c); c = dot2(a[1], a[1], c); c = dot2(a[2], a[2], c); c = dot2(a[3], a[3], c);
  return c;
}
DEVI float red_g(float v) {
  v += __shfl_xor(v, 16);
  v += __shfl_xor(v, 32);
  return v;
}
DEVI void glds16(unsigned voff, const void* sbase, unsigned lds_dst) {
  unsigned keep;
  asm volatile("s_mov_b32 %0, m0\n\ts_mov_b32 m0, %3\n\ts_nop 0\n\tglobal_load_lds_dwordx4 %1, %2\n\ts_mov_b32 m0, %0"
               : "=&s"(keep) : "v"(voff), "s"(sbase), "s"(lds_dst) : "memory");
}
#define DG(...)
#define WAIT_VM(n) asm volatile("s_waitcnt vmcnt(" #n ")" ::: "memory")
#define WAIT_LGKM0() asm volatile("s_waitcnt lgkmcnt(0)" ::: "memory")

DEVI void feature_map(const f32x4 (&p)[5], unsigned (&o)[20]) {
  float mx = p[0][0], mn = p[0][0];
#pragma unroll
  for (int t = 0; t < 5; ++t)
#pragma unroll
    for (int r = 0; r < 4; ++r) { mx = fmaxf(mx, p[t][r]); mn = fminf(mn, p[t][r]); }
  const float a = -mx * L2E, b = mn * L2E;
  float e1[20], e2[20], s1 = 0.f, s2 = 0.f;
#pragma unroll
  for (int t = 0; t < 5; ++t)
#pragma unroll
    for (int r = 0; r < 4; ++r) {
      e1[4 * t + r] = __builtin_amdgcn_exp2f(fmaf(p[t][r], L2E, a));
      e2[4 * t + r] = __builtin_amdgcn_exp2f(fmaf(p[t][r], -L2E, b));
      s1 += e1[4 * t + r]; s2 += e2[4 * t + r];
    }
  const float r1 = __builtin_amdgcn_rcpf(s1), r2 = __builtin_amdgcn_rcpf(s2);
#pragma unroll
  for (int i = 0; i < 10; ++i) {
    o[i] = pk2(e1[2 * i] * r1, e1[2 * i + 1] * r1);
    o[10 + i] = pk2(e2[2 * i] * r2, e2[2 * i + 1] * r2);
  }
}
DEVI void head_proj(LDSP char* wl, const u32x4 (&b)[3], f32x4 (&p)[5]) {
#pragma unroll
  for (int t = 0; t < 5; ++t) {
    u32x4 a0 = trread2(wl + 0 * 40 + t * 8, wl + 4 * 40 + t * 8);
    u32x4 a1 = trread2(wl + 8 * 40 + t * 8, wl + 12 * 40 + t * 8);
    u32x2 h2 = trread(wl + 16 * 40 + t * 8);
    u32x4 a2 = {h2[0], h2[1], 0u, 0u};
    f32x4 c = {0.f, 0.f, 0.f, 0.f};
    c = mfma(a0, b[0], c);
    c = mfma(a1, b[1], c);
    c = mfma(a2, b[2], c);
    p[t] = c;
  }
}

constexpr unsigned XSLOT = 16384, WSUB = 5120, WSLOT = 3 * WSUB;
constexpr unsigned K1_W = 4 * XSLOT;
constexpr unsigned K1_WPHI = K1_W + 5 * WSLOT;
constexpr unsigned K1_ZERO = K1_WPHI + 3200;
constexpr unsigned K1_STAT = K1_ZERO + 768;
constexpr unsigned K1_CSP = K1_STAT + 1024;
constexpr unsigned K1_CS = K1_CSP + 3840;
constexpr unsigned K1_BL = K1_CS + 960;
constexpr unsigned K1_LDS = K1_BL + 960;
constexpr unsigned PK_PITCH = 352, V_PITCH = 224, K1_VIMG = 128 * PK_PITCH;

struct K1State {
  LDSP char* lds;
  unsigned voff0, voff1;
  const char* p0; const char* p1;
  unsigned inc0;
  int inc1a, inc1b;
  unsigned dst0, dst1;
  unsigned end0, end1, ring1, step1;
  unsigned xa, wa;
  unsigned wr;
  int phase;
  DG(unsigned long long t1;)
};
DEVI void glds16n(unsigned voff, const void* sbase, unsigned lds_dst) {
  asm volatile("s_mov_b32 m0, %2\n\ts_nop 0\n\tglobal_load_lds_dwordx4 %0, %1" ::"v"(voff), "s"(sbase), "s"(lds_dst) : "memory");
}
template <int ODD>
DEVI void k1_issue(K1State& S) {
  glds16n(S.voff0, S.p0, S.dst0);
  glds16n(S.voff1, S.p1, S.dst1);
  S.p0 += S.inc0;
  S.p1 += ODD ? S.inc1b : S.inc1a;
  S.dst0 += WSLOT;
  S.dst0 = S.dst0 == S.end0 ? S.end0 - 5 * WSLOT : S.dst0;
  S.dst1 += S.step1;
  S.dst1 = S.dst1 == S.end1 ? S.end1 - S.ring1 : S.dst1;
}
template <int TYPE>
DEVI void k1_read(K1State& S, int s, u32x4 (&xf)[2], u32x4 (&wf)[5]) {
  constexpr int CS = TYPE == 0 ? 8 : 32, NC = TYPE == 2 ? 4 : 5;
  LDSP char* px = S.lds + ((s >> 1) & 3) * XSLOT;
  LDSP char* pw = S.lds + K1_W + S.wr + S.wa;
  xf[0] = *(LDSP u32x4*)(px + (S.xa ^ ((s & 1) << 6)));
  wf[0] = trread2(pw, pw + 1280);
  xf[1] = *(LDSP u32x4*)(px + (S.xa ^ (((s & 1) << 6) | 2048)));
#pragma unroll
  for (int c = 1; c < NC; ++c) wf[c] = trread2(pw + c * CS, pw + c * CS + 1280);
  S.wr = S.wr == 4 * WSLOT ? 0 : S.wr + WSLOT;
}
template <int TYPE, bool STATS, int WAITN, int ISSUE, bool READ>
DEVI void k1_step(K1State& S, int s, const u32x4 (&xc)[2], const u32x4 (&wc)[5], u32x4 (&xn)[2], u32x4 (&wn)[5],
                  f32x4 (&acc)[2][5], f32x4 (&cs)[5], f32x4& s1, f32x4& s2, const u32x4& ones) {
  constexpr int NC = TYPE == 2 ? 4 : 5;
  if (READ) {
    if (WAITN == 7) WAIT_VM(7); else if (WAITN == 6) WAIT_VM(6); else if (WAITN == 4) WAIT_VM(4); else if (WAITN == 2) WAIT_VM(2); else WAIT_VM(0);
    __builtin_amdgcn_s_waitcnt(0xC07F);
    __builtin_amdgcn_s_barrier();
    __builtin_amdgcn_sched_barrier(0);
    k1_read<TYPE>(S, s + 1, xn, wn);
  }
#pragma unroll
  for (int c = 0; c < NC; ++c) {
    acc[0][c] = mfma(wc[c], xc[0], acc[0][c]);
    acc[1][c] = mfma(wc[c], xc[1], acc[1][c]);
  }
  if (STATS) {
    s1 = mfma(ones, xc[0], s1);
    s2 = mfma(xc[0], xc[0], s2);
  }
  if (READ) {
#pragma unroll
    for (int i = 0; i < 2; ++i) {
      __builtin_amdgcn_sched_group_barrier(0x8, 1, 0);
      __builtin_amdgcn_sched_group_barrier(0x100, 2, 0);
    }
#pragma unroll
    for (int i = 2; i < 2 * NC; ++i) {
      __builtin_amdgcn_sched_group_barrier(0x8, 1, 0);
      __builtin_amdgcn_sched_group_barrier(0x100, 1, 0);
    }
  }
  __builtin_amdgcn_sched_barrier(0);
  if (ISSUE) k1_issue<ISSUE - 1>(S);
  if (((s + S.phase) & 3) == 0) {
#pragma unroll
    for (int c = 0; c < NC; ++c) cs[c] = mfma(wc[c], ones, cs[c]);
  }
}
template <int TYPE, bool STATS>
DEVI void k1_main(K1State& S, f32x4 (&acc)[2][5], f32x4 (&cs)[5], f32x4& s1, f32x4& s2) {
  u32x4 xa[2], wa[5], xb[2], wb[5];
  u32x4 ones = {0x3f803f80u, 0x3f803f80u, 0x3f803f80u, 0x3f803f80u};
  asm volatile("" : "+v"(ones));
  WAIT_VM(8);
  __builtin_amdgcn_s_barrier();
  DG(S.t1 = stamp();)
  k1_read<TYPE>(S, 0, xa, wa);
#pragma unroll 1
  for (int s = 0; s < 14; s += 2) {
    k1_step<TYPE, STATS, 6, 1, true>(S, s, xa, wa, xb, wb, acc, cs, s1, s2, ones);
    k1_step<TYPE, STATS, 6, 2, true>(S, s + 1, xb, wb, xa, wa, acc, cs, s1, s2, ones);
  }
  S.p1 -= (S.inc1a != S.inc1b) ? 128 : 0;
  k1_step<TYPE, STATS, 6, 1, true>(S, 14, xa, wa, xb, wb, acc, cs, s1, s2, ones);
  k1_step<TYPE, STATS, 6, 0, true>(S, 15, xb, wb, xa, wa, acc, cs, s1, s2, ones);
  k1_step<TYPE, STATS, 4, 0, true>(S, 16, xa, wa, xb, wb, acc, cs, s1, s2, ones);
  k1_step<TYPE, STATS, 2, 0, true>(S, 17, xb, wb, xa, wa, acc, cs, s1, s2, ones);
  k1_step<TYPE, STATS, 0, 0, true>(S, 18, xa, wa, xb, wb, acc, cs, s1, s2, ones);
  k1_step<TYPE, STATS, 0, 0, false>(S, 19, xb, wb, xa, wa, acc, cs, s1, s2, ones);
}

template <int AUX = 0> DEVI void st16(__amdgpu_buffer_rsrc_t rs, u32x4 v, unsigned voff, unsigned soff) {
  __builtin_amdgcn_raw_buffer_store_b128(v, rs, voff + soff, 0, AUX);
}
template <int AUX = 0> DEVI void st8(__amdgpu_buffer_rsrc_t rs, u32x2 v, unsigned voff, unsigned soff) {
  __builtin_amdgcn_raw_buffer_store_b64(v, rs, voff + soff, 0, AUX);
}

__global__ __launch_bounds__(768, 3) void hedgehog_k1(const u16* __restrict__ x, const u16* __restrict__ wqkv,
                                                      const u16* __restrict__ wphi, const u16* __restrict__ wo,
                                                      const u16* __restrict__ w1, const u16* __restrict__ b1,
                                                      const u16* __restrict__ w2, char* __restrict__ ws) {
  extern __shared__ __attribute__((aligned(16))) char smem[];
  LDSP char* lds = (LDSP char*)smem;
  asm volatile("" ::"s"(x), "s"(wqkv), "s"(wphi), "s"(wo), "s"(w1), "s"(b1), "s"(w2), "s"(ws));
  int tid = threadIdx.x, lane = tid & 63;
  const int wave = __builtin_amdgcn_readfirstlane(tid >> 6);
  const int mi = wave & 3, nj = wave >> 2;
  int g = lane >> 4, m = lane & 15, q4 = m >> 2, p4 = lane & 3;
  const int b = blockIdx.x, xcd = b & 7, jx = b >> 3;
  const int rb = 8 * (xcd >> 2) + (jx & 7);
  const int cgi = jx >> 3;
  const bool atype = cgi < 2;
  const int grp = 2 * (xcd & 3) + (cgi & 1);
  const u16* W = atype ? wqkv : w1;
  const int ldw = atype ? 1920 : 1728;
  const int c0 = atype ? 80 * grp : (grp < 6 ? 224 * grp : 1344 + 192 * (grp - 6));
  const int sub = atype ? 640 : (grp < 6 ? 80 : 64);
  const bool five = atype || (grp < 6 && nj < 2);
  const int rsw = nj & 1;
  DG(unsigned long long T[8], U[4]; T[0] = stamp(); T[7] = rstamp(); T[1] = T[6] = 0;)

  K1State S;
  S.lds = lds;
  const unsigned ldsbase = (unsigned)(size_t)lds;
  auto wpiece = [&](int id, unsigned& voff, unsigned& dst) {
    const int sj = id / 5, ss = id - 5 * sj;
    const int c = 64 * ss + lane, rho = c / 10, cc = c - 10 * rho;
    const int k = (rho & 0x13) | ((rho & 4) << 1) | ((rho & 8) >> 1);
    const bool narrow = !atype && (grp >= 6 || sj == 2);
    const int col = c0 + sub * sj + 8 * ((narrow && cc >= 8) ? cc - 2 : cc);
    voff = (k * ldw + col) * 2;
    dst = ldsbase + K1_W + sj * WSUB + ss * 1024;
  };
  if (atype) {
    if (tid < 200) glds16n(lane * 16, (const char*)wphi + grp * 3200 + wave * 1024, ldsbase + K1_WPHI + wave * 1024);
  } else if (tid < 30) {
    const int col = c0 + sub * (lane / 10) + 8 * (lane % 10);
    glds16n((col > 1720 ? 1720 : col) * 2, (const char*)b1, ldsbase + K1_WPHI);
  }
  wpiece(wave, S.voff0, S.dst0);
  S.p0 = (const char*)W;
  S.inc0 = 64 * ldw;
  S.end0 = S.dst0 + 5 * WSLOT;
  if (wave >= 3) {
    const int pc = wave < 11 ? wave - 3 : 7, row = pc * 8 + (lane >> 3);
    S.voff1 = row * 1280 + (((lane & 7) ^ ((row >> 1) & 7)) << 4);
    S.dst1 = ldsbase + pc * 1024;
    S.p1 = (const char*)x + (size_t)rb * 128 * 1280;
    glds16n(S.voff1, S.p1, S.dst1);
    S.end1 = S.dst1 + 4 * XSLOT; S.ring1 = 4 * XSLOT; S.step1 = 8192;
    S.p1 += 64 * 1280; S.dst1 += 8192;
    S.inc1a = 64 * 1280;
    S.inc1b = 128 - 64 * 1280;
  } else {
    wpiece(wave + 12, S.voff1, S.dst1);
    S.p1 = (const char*)W;
    S.end1 = S.dst1 + 5 * WSLOT; S.ring1 = 5 * WSLOT; S.step1 = WSLOT;
    S.inc1a = S.inc1b = 64 * ldw;
  }
  k1_issue<1>(S);
  __builtin_amdgcn_s_barrier();
  k1_issue<0>(S);
  k1_issue<1>(S);
  k1_issue<0>(S);
  k1_issue<1>(S);
  S.wr = 0;
  const int gw = b * 12 + wave;
  unsigned rt_lo, rt_hi;
  const u16* rt_src;
  unsigned rt_dst;
  {
    const int fid = gw < 2960 ? gw : 2959;
    const int f4 = fid >> 2;
    const bool isw2 = f4 < 540;
    const int f2 = isw2 ? f4 : f4 - 540;
    const int cq = isw2 ? f2 / 54 : f2 / 20, ks = f2 - (isw2 ? 54 : 20) * cq;
    const int ct = 4 * cq + (fid & 3);
    const int e = 4 * ks + g, i5 = e / 5, r5 = e - 5 * i5;
    const int lo = isw2 ? 32 * ks + 4 * g : 40 * i5 + (r5 == 4 ? 16 : (r5 >> 1) * 20 + (r5 & 1) * 8);
    const int hi = lo + (isw2 ? 16 : (r5 == 4 ? 20 : 4));
    rt_dst = OFF_WT + (ct * 74 + (isw2 ? 0 : 54) + ks) * 1024;
    rt_src = isw2 ? w2 : wo;
    rt_lo = lo * 640 + 16 * ct + m;
    rt_hi = hi * 640 + 16 * ct + m;
  }
  asm volatile("" : "+v"(rt_lo), "+v"(rt_hi));
  S.xa = (mi * 32 + rsw * 16 + m) * 128 + ((g ^ (m >> 1)) << 4);
  S.wa = nj * WSUB + (16 * (g >> 1) + 4 * (g & 1) + q4) * 160 + p4 * (atype ? 40 : 8);
  S.phase = mi - nj;

  f32x4 acc[2][5], cs[5];
#pragma unroll
  for (int c = 0; c < 5; ++c) { acc[0][c] = f32x4{0.f, 0.f, 0.f, 0.f}; acc[1][c] = acc[0][c]; cs[c] = acc[0][c]; }
  f32x4 sv1 = {0.f, 0.f, 0.f, 0.f}, sv2 = {0.f, 0.f, 0.f, 0.f};
  if (atype) {
    if (nj < 2) k1_main<0, true>(S, acc, cs, sv1, sv2); else k1_main<0, false>(S, acc, cs, sv1, sv2);
  } else if (five) {
    k1_main<1, true>(S, acc, cs, sv1, sv2);
  } else {
    if (nj < 2) k1_main<2, true>(S, acc, cs, sv1, sv2); else k1_main<2, false>(S, acc, cs, sv1, sv2);
  }
  DG(T[2] = stamp(); T[1] = S.t1;)

  tid = threadIdx.x;
  asm volatile("" : "+v"(tid));
  lane = tid & 63; g = lane >> 4; m = lane & 15; q4 = m >> 2; p4 = lane & 3;
  const __amdgpu_buffer_rsrc_t rs = __builtin_amdgcn_make_buffer_rsrc((void*)ws, 0, WS_END, 0x00020000);
  u16 rt_v[8];
#pragma unroll
  for (int j = 0; j < 4; ++j) {
    rt_v[j] = rt_src[rt_lo + j * 640]; rt_v[4 + j] = rt_src[rt_hi + j * 640];
  }
  DG(U[0] = stamp();)
  if (gw < 2560) st16(rs, u32x4{~0u, ~0u, ~0u, ~0u}, lane * 16, OFF_OT + gw * 1024);
  DG(U[1] = stamp();)
  const float st1 = sv1[0];
  const float st2 = (m & 2) ? ((m & 1) ? sv2[3] : sv2[2]) : ((m & 1) ? sv2[1] : sv2[0]);
  if (m == 0) {
#pragma unroll
    for (int c = 0; c < 5; ++c) *(LDSP f32x4*)(lds + K1_CSP + ((nj * 4 + mi) * 80 + c * 16 + 4 * g) * 4) = cs[c];
  }
  if (g == q4 && nj < 2) {
    const float mean = st1 * (1.0f / 640.0f);
    const float var = fmaxf(st2 * (1.0f / 640.0f) - mean * mean, 0.f);
    float rsd = __builtin_amdgcn_rsqf(var + 1e-5f);
    if (!atype) rsd *= L2E;
    *(LDSP f32x2*)(lds + K1_STAT + (mi * 32 + nj * 16 + m) * 8) = f32x2{rsd, -mean * rsd};
  }
  WAIT_LGKM0();
  DG(U[2] = stamp();)
  __builtin_amdgcn_s_barrier();
  DG(U[3] = stamp();)
  if (tid < 240) {
    const int j3 = tid / 80, ix = tid - 80 * j3;
    LDSP float* pp = (LDSP float*)(lds + K1_CSP) + j3 * 320 + ix;
    *(LDSP float*)(lds + K1_CS + tid * 4) = (pp[0] + pp[80]) + (pp[160] + pp[240]);
    if (!atype) *(LDSP float*)(lds + K1_BL + tid * 4) = bflo(*(LDSP u16*)(lds + K1_WPHI + tid * 2)) * L2E;
  } else if (tid < 288) {
    *(LDSP u32x4*)(lds + K1_ZERO + (tid - 240) * 16) = u32x4{0u, 0u, 0u, 0u};
  }
  WAIT_LGKM0();
  __builtin_amdgcn_s_barrier();
  asm volatile("" ::: "memory");
  DG(T[3] = stamp();)
#define DG_OUT1() DG({ T[5] = stamp(); unsigned long long re = rstamp(); if (lane == 0) { unsigned long long* dp = (unsigned long long*)(ws + OFF_DBG) + (size_t)gw * 16; for (int i = 0; i < 8; ++i) dp[i] = T[i]; dp[8] = re; for (int i = 0; i < 4; ++i) dp[9 + i] = U[i]; } })

  f32x2 stt[2];
  stt[0] = *(LDSP f32x2*)(lds + K1_STAT + (mi * 32 + rsw * 16 + m) * 8);
  stt[1] = *(LDSP f32x2*)(lds + K1_STAT + (mi * 32 + (rsw ^ 1) * 16 + m) * 8);
#pragma unroll
  for (int c = 0; c < 5; ++c) cs[c] = *(LDSP f32x4*)(lds + K1_CS + (nj * 80 + c * 16 + 4 * g) * 4);
  auto finish_side = [&]() {
    u32x4 v;
#pragma unroll
    for (int j = 0; j < 4; ++j) v[j] = (unsigned)rt_v[2 * j] | ((unsigned)rt_v[2 * j + 1] << 16);
    if (gw < 2960) st16(rs, v, lane * 16, rt_dst);
  };

  if (!atype) {
    const int T0 = (c0 + sub * nj) >> 4;
    const int odd = T0 & 1;
    f32x4 bl[5];
#pragma unroll
    for (int c = 0; c < 5; ++c) bl[c] = *(LDSP f32x4*)(lds + K1_BL + (nj * 80 + c * 16 + 4 * g) * 4);
#pragma unroll
    for (int r = 0; r < 2; ++r) {
      u32x2 pk[5];
#pragma unroll
      for (int c = 0; c < 5; ++c) {
        if (c == 4 && !five) { pk[4] = u32x2{0u, 0u}; break; }
        float res[4];
#pragma unroll
        for (int e = 0; e < 4; ++e) {
          const float hl = fmaf(acc[r][c][e], stt[r][0], fmaf(stt[r][1], cs[c][e], bl[c][e]));
          const float ex = __builtin_amdgcn_exp2f(fminf(hl, 40.f));
          const float n = ex * (ex + 2.f);
          const float rc = __builtin_amdgcn_rcpf(fmaf(n, L2E, 2.f * L2E));
          res[e] = hl * (n * rc);
        }
        pk[c] = u32x2{pk2(res[0], res[1]), pk2(res[2], res[3])};
      }
      const unsigned base = OFF_ACT + ((rb * 8 + mi * 2 + (r ^ rsw)) * 54 + (T0 >> 1)) * 1024;
      if (!odd) {
        st16(rs, u32x4{pk[0][0], pk[0][1], pk[1][0], pk[1][1]}, lane * 16, base);
        st16(rs, u32x4{pk[2][0], pk[2][1], pk[3][0], pk[3][1]}, lane * 16, base + 1024);
        if (five) st8(rs, pk[4], lane * 16, base + 2048);
      } else {
        st8(rs, pk[0], lane * 16, base + 8);
        st16(rs, u32x4{pk[1][0], pk[1][1], pk[2][0], pk[2][1]}, lane * 16, base + 1024);
        st16(rs, u32x4{pk[3][0], pk[3][1], pk[4][0], pk[4][1]}, lane * 16, base + 2048);
      }
    }
    finish_side();
    DG(T[4] = stamp();)
    DG_OUT1();
    return;
  }

#pragma unroll
  for (int r = 0; r < 2; ++r) {
    u32x4 bq[3];
#pragma unroll
    for (int c = 0; c < 5; ++c) {
      float v[4];
#pragma unroll
      for (int e = 0; e < 4; ++e) v[e] = fmaf(acc[r][c][e], stt[r][0], stt[r][1] * cs[c][e]);
      bq[c >> 1][(c & 1) * 2] = pk2(v[0], v[1]);
      bq[c >> 1][(c & 1) * 2 + 1] = pk2(v[2], v[3]);
    }
    bq[2][2] = (nj == 2) ? 0x00003f80u : 0u;
    bq[2][3] = 0u;
    const int tok = mi * 32 + (r ^ rsw) * 16 + m;
    if (nj == 0) {
      const unsigned vo = (g * 2048 + tok) * 48, so = OFF_QT + (4 * grp * 2048 + rb * 128) * 48;
      st16(rs, bq[0], vo, so);
      st16(rs, bq[1], vo, so + 16);
      st16(rs, bq[2], vo, so + 32);
    } else if (nj == 2) {
      LDSP char* dst = lds + K1_VIMG + tok * V_PITCH + g * 48;
      *(LDSP u32x4*)(dst) = bq[0];
      *(LDSP u32x4*)(dst + 16) = bq[1];
      *(LDSP u32x4*)(dst + 32) = bq[2];
    } else {
      LDSP char* wl = lds + ((p4 == g) ? K1_WPHI + g * 800 + q4 * 40 : K1_ZERO);
      f32x4 pk[5];
      head_proj(wl, bq, pk);
      unsigned ph[20];
      feature_map(pk, ph);
      LDSP char* dst = lds + tok * PK_PITCH + g * 80;
#pragma unroll
      for (int i = 0; i < 5; ++i) *(LDSP u32x4*)(dst + i * 16) = u32x4{ph[4 * i], ph[4 * i + 1], ph[4 * i + 2], ph[4 * i + 3]};
    }
  }
  DG(T[4] = stamp();)
  WAIT_LGKM0();
  __builtin_amdgcn_s_barrier();
  asm volatile("" ::: "memory");
  {
    LDSP char* pa = lds + (4 * g + q4) * PK_PITCH + mi * 80 + nj * 32 + p4 * 8;
    LDSP char* pb = lds + K1_VIMG + (4 * g + q4) * V_PITCH + mi * 48 + p4 * 8;
    f32x4 d0 = {0.f, 0.f, 0.f, 0.f}, d1 = {0.f, 0.f, 0.f, 0.f};
#pragma unroll
    for (int kk = 0; kk < 4; ++kk) {
      u32x4 a = trread2(pa + (32 * kk) * PK_PITCH, pa + (32 * kk + 16) * PK_PITCH);
      u32x4 v0 = trread2(pb + (32 * kk) * V_PITCH, pb + (32 * kk + 16) * V_PITCH);
      u32x4 v1 = trread2(pb + (32 * kk) * V_PITCH + 32, pb + (32 * kk + 16) * V_PITCH + 32);
      d0 = mfma(a, v0, d0);
      d1 = mfma(a, v1, d1);
    }
    if (16 * nj + 4 * g < 40) {
      const unsigned so = OFF_KVP + ((4 * grp + mi) * 16 + rb) * 3360 + 64 * nj;
      st16(rs, __builtin_bit_cast(u32x4, d0), m * 160 + g * 16, so);
      if (m < 5) st16(rs, __builtin_bit_cast(u32x4, d1), (16 + m) * 160 + g * 16, so);
    }
  }
  finish_side();
  DG_OUT1();
}

constexpr unsigned K2_YD = 0;
constexpr unsigned K2_SD = 4 * 15360;
constexpr unsigned K2_KVT = K2_SD + 4 * 20480;
constexpr unsigned K2_ZERO = K2_KVT + 1920;
constexpr unsigned K2_WPHI = K2_ZERO + 1792;
constexpr unsigned K2_FLAG = K2_WPHI + 3200;
constexpr unsigned K2_LDS = K2_FLAG + 16;

DEVI u32x4 bld(__amdgpu_buffer_rsrc_t rs, unsigned voff, unsigned soff) {
  return __builtin_amdgcn_raw_buffer_load_b128(rs, voff, soff, 0);
}
DEVI u32x4 bld_sc1(__amdgpu_buffer_rsrc_t rs, unsigned voff, unsigned soff) {
  return __builtin_amdgcn_raw_buffer_load_b128(rs, voff, soff, 16);
}

__global__ __launch_bounds__(512, 2) void hedgehog_k2(const u16* __restrict__ x, const u16* __restrict__ wphi,
                                                      char* __restrict__ ws, u16* __restrict__ out) {
  extern __shared__ __attribute__((aligned(16))) char smem[];
  LDSP char* lds = (LDSP char*)smem;
  const int tid = threadIdx.x, lane = tid & 63;
  const int wave = __builtin_amdgcn_readfirstlane(tid >> 6);
  const int g = lane >> 4, m = lane & 15, q4 = m >> 2, p4 = lane & 3;
  const int b = blockIdx.x, xcd = b & 7, jx = b >> 3;
  const int rb2 = 8 * (xcd >> 1) + (jx & 7), cb = 4 * (xcd & 1) + (jx >> 3);
  const __amdgpu_buffer_rsrc_t rs = __builtin_amdgcn_make_buffer_rsrc((void*)ws, 0, WS_END, 0x00020000);
  const unsigned l16 = lane * 16;
  DG(unsigned long long T[8]; T[0] = stamp(); T[7] = rstamp(); T[1] = T[2] = T[3] = T[4] = T[5] = T[6] = 0;)
#define DG_OUT2() DG({ unsigned long long re = rstamp(); if (lane == 0) { unsigned long long* dp = (unsigned long long*)(ws + OFF_DBG + (1u << 20)) + (size_t)(b * 8 + wave) * 16; for (int i = 0; i < 8; ++i) dp[i] = T[i]; dp[8] = re; } })
  f32x4 acc[4][5];
#pragma unroll
  for (int r = 0; r < 4; ++r)
#pragma unroll
    for (int c = 0; c < 5; ++c) acc[r][c] = f32x4{0.f, 0.f, 0.f, 0.f};

  if (wave < 4) {
    const int ks0 = wave < 2 ? 14 * wave : 28 + 13 * (wave - 2);
    const bool n14 = wave < 2;
    unsigned ab[4], bb[5];
#pragma unroll
    for (int l = 0; l < 4; ++l) ab[l] = OFF_ACT + ((4 * rb2 + ((l + wave) & 3)) * 54 + ks0) * 1024;
#pragma unroll
    for (int c = 0; c < 5; ++c) bb[c] = OFF_WT + ((5 * cb + c) * 74 + ks0) * 1024;
    u32x4 A[3][4], B[3][5];
    auto load = [&](int i) {
#pragma unroll
      for (int l = 0; l < 4; ++l) A[i % 3][l] = bld(rs, l16, ab[l] + i * 1024);
#pragma unroll
      for (int c = 0; c < 5; ++c) B[i % 3][c] = bld(rs, l16, bb[c] + i * 1024);
    };
    auto comp = [&](int i) {
#pragma unroll
      for (int l = 0; l < 4; ++l)
#pragma unroll
        for (int c = 0; c < 5; ++c) acc[l][c] = mfma(B[i % 3][c], A[i % 3][l], acc[l][c]);
    };
    load(0);
    load(1);
    u32x2 xv[5];
    const size_t orow = (size_t)(64 * rb2 + 16 * wave + m) * 640 + 80 * cb + 4 * g;
#pragma unroll
    for (int c = 0; c < 5; ++c) xv[c] = *(const u32x2*)(x + orow + 16 * c);
    __builtin_amdgcn_s_barrier();
    DG(T[1] = stamp();)
#pragma unroll
    for (int i = 0; i < 13; ++i) {
      if (i + 2 < 13) load(i + 2);
      else if (i + 2 == 13) { if (n14) load(13); }
      __builtin_amdgcn_sched_barrier(0);
      comp(i);
      __builtin_amdgcn_sched_barrier(0);
      DG(if (i == 0) T[2] = stamp();)
    }
    if (n14) comp(13);
    DG(T[3] = stamp();)
#pragma unroll
    for (int l = 1; l < 4; ++l)
#pragma unroll
      for (int c = 0; c < 5; ++c)
        *(LDSP f32x4*)(lds + K2_YD + wave * 15360 + ((l - 1) * 5 + c) * 1024 + l16) = acc[l][c];
    DG(T[4] = stamp();)
    __syncthreads();
    DG(T[5] = stamp();)
#pragma unroll
    for (int c = 0; c < 5; ++c) {
      f32x4 y = acc[0][c];
#pragma unroll
      for (int l = 1; l < 4; ++l)
        y += *(LDSP f32x4*)(lds + K2_YD + ((wave - l) & 3) * 15360 + ((l - 1) * 5 + c) * 1024 + l16);
      f32x4 s = *(LDSP f32x4*)(lds + K2_SD + (wave * 5 + c) * 1024 + l16);
#pragma unroll
      for (int a = 1; a < 4; ++a) s += *(LDSP f32x4*)(lds + K2_SD + a * 20480 + (wave * 5 + c) * 1024 + l16);
      float o[4];
#pragma unroll
      for (int e = 0; e < 4; ++e) {
        const float gate = y[e] * __builtin_amdgcn_rcpf(1.f + __builtin_amdgcn_exp2f(-y[e] * L2E));
        const unsigned xw = xv[c][e >> 1];
        o[e] = fmaf(s[e], gate, (e & 1) ? bfhi(xw) : bflo(xw));
      }
      *(u32x2*)(out + orow + 16 * c) = u32x2{pk2(o[0], o[1]), pk2(o[2], o[3])};
    }
    DG(T[6] = stamp();)
    DG_OUT2();
    return;
  }

  const int aw = wave - 4;
  const int h = 4 * xcd + (jx & 3), sb = jx >> 2;
  const int tok = sb * 256 + aw * 64 + lane;
  if (aw == 0) {
    *(LDSP u32x4*)(lds + K2_ZERO + l16) = u32x4{0u, 0u, 0u, 0u};
    if (lane < 48) *(LDSP u32x4*)(lds + K2_ZERO + 1024 + l16) = u32x4{0u, 0u, 0u, 0u};
    if (lane == 0) *(LDSP u32x4*)(lds + K2_FLAG) = u32x4{0u, 0u, 0u, 0u};
  }
  u32x4 wv = *(const u32x4*)(wphi + h * 400 + (lane < 50 ? lane : 49) * 8);
  u32x4 bq[3];
#pragma unroll
  for (int s = 0; s < 3; ++s) bq[s] = bld(rs, (unsigned)tok * 48, OFF_QT + h * 2048 * 48 + s * 16);
  int item = aw * 53 + (lane < 53 ? lane : 52);
  item = item < 210 ? item : 209;
  u32x4 part[16];
#pragma unroll
  for (int r = 0; r < 16; ++r) part[r] = bld(rs, (unsigned)item * 16, OFF_KVP + (h * 16 + r) * 3360);
  WAIT_LGKM0();
  __builtin_amdgcn_s_barrier();
  __builtin_amdgcn_s_setprio(2);
  *(LDSP u32x4*)(lds + K2_WPHI + aw * 800 + (lane < 50 ? lane : 49) * 16) = wv;
  unsigned ph[20];
  {
    LDSP char* wl = lds + ((p4 == g) ? K2_WPHI + aw * 800 + q4 * 40 : K2_ZERO);
    f32x4 pq[5];
    head_proj(wl, bq, pq);
    feature_map(pq, ph);
#pragma unroll
    for (int i = 0; i < 20; ++i) asm volatile("" : "+v"(ph[i]));
  }
  DG(T[1] = stamp();)
  {
    f32x4 sum = __builtin_bit_cast(f32x4, part[0]);
#pragma unroll
    for (int r = 1; r < 16; ++r) sum += __builtin_bit_cast(f32x4, part[r]);
    const int d = item / 10, fq = item - 10 * d;
    *(LDSP u32x2*)(lds + K2_KVT + d * 80 + fq * 8) = u32x2{pk2(sum[0], sum[1]), pk2(sum[2], sum[3])};
    WAIT_LGKM0();
    if (lane == 0) *(volatile LDSP unsigned*)(lds + K2_FLAG + aw * 4) = 1u;
    for (int spin = 0; spin < (1 << 22); ++spin) {
      const u32x4 f = *(volatile LDSP u32x4*)(lds + K2_FLAG);
      if (__builtin_amdgcn_readfirstlane(f[0] & f[1] & f[2] & f[3])) break;
      __builtin_amdgcn_s_sleep(1);
    }
    asm volatile("" ::: "memory");
  }
  DG(T[2] = stamp();)
  const int ks0 = 5 * aw;
  u32x4 B[5][5];
#pragma unroll
  for (int i = 0; i < 2; ++i)
#pragma unroll
    for (int c = 0; c < 5; ++c) B[i][c] = bld(rs, l16, OFF_WT + ((5 * cb + c) * 74 + 54 + ks0 + i) * 1024);
  {
    LDSP char* kl = lds + (((m >> 2) == g) ? K2_KVT + (m & 3) * 80 : K2_ZERO);
    f32x4 nd[6];
#pragma unroll
    for (int t = 0; t < 6; ++t) {
      f32x4 c = {0.f, 0.f, 0.f, 0.f};
#pragma unroll
      for (int s = 0; s < 5; ++s)
        c = mfma(*(LDSP u32x4*)(kl + t * 320 + s * 16), u32x4{ph[4 * s], ph[4 * s + 1], ph[4 * s + 2], ph[4 * s + 3]}, c);
      nd[t] = c;
    }
    const float rd = __builtin_amdgcn_rcpf(nd[5][0] + 1e-6f);
    unsigned ow[10];
#pragma unroll
    for (int t = 0; t < 5; ++t) {
      ow[2 * t] = min(pk2(nd[t][0] * rd, nd[t][1] * rd), 0xFFFFFFFEu);
      ow[2 * t + 1] = min(pk2(nd[t][2] * rd, nd[t][3] * rd), 0xFFFFFFFEu);
    }
    const int e0 = 5 * (h >> 1) + 2 * (h & 1), e1 = e0 + 1, e4 = 5 * (h >> 1) + 4;
    const unsigned vo = (unsigned)(tok >> 4) * 20480 + m * 16;
    st16<16>(rs, u32x4{ow[0], ow[1], ow[2], ow[3]}, vo, OFF_OT + (e0 >> 2) * 1024 + (e0 & 3) * 256);
    st16<16>(rs, u32x4{ow[4], ow[5], ow[6], ow[7]}, vo, OFF_OT + (e1 >> 2) * 1024 + (e1 & 3) * 256);
    st8<16>(rs, u32x2{ow[8], ow[9]}, vo, OFF_OT + (e4 >> 2) * 1024 + (e4 & 3) * 256 + (h & 1) * 8);
  }
  DG(T[3] = stamp();)
  const unsigned ob = OFF_OT + (4 * rb2 * 20 + ks0) * 1024;
  {
    const int l2 = lane < 40 ? lane : 39;
    const unsigned pv = 3 * 20480 + (l2 >> 3) * 1024 + ((l2 >> 1) & 3) * 256 + 15 * 16 + (l2 & 1) * 8;
    for (int spin = 0; spin < (1 << 20); ++spin) {
      asm volatile("" ::: "memory");
      const unsigned w = __builtin_amdgcn_raw_buffer_load_b32(rs, pv, ob, 16);
      if (!__any(w == 0xFFFFFFFFu)) break;
      __builtin_amdgcn_s_sleep(4);
    }
  }
  DG(T[4] = stamp();)
  u32x4 A[5][4];
  for (int spin = 0; spin < (1 << 16); ++spin) {
    asm volatile("" ::: "memory");
    unsigned mx = 0;
#pragma unroll
    for (int i = 0; i < 5; ++i)
#pragma unroll
      for (int l = 0; l < 4; ++l) A[i][l] = bld_sc1(rs, l16, ob + l * 20480 + i * 1024);
#pragma unroll
    for (int i = 0; i < 5; ++i)
#pragma unroll
      for (int l = 0; l < 4; ++l) mx = max(max(mx, max(A[i][l][0], A[i][l][1])), max(A[i][l][2], A[i][l][3]));
    if (!__any(mx == 0xFFFFFFFFu)) break;
    __builtin_amdgcn_s_sleep(4);
  }
  DG(T[5] = stamp();)
#pragma unroll
  for (int i = 0; i < 5; ++i) {
    if (i + 2 < 5) {
#pragma unroll
      for (int c = 0; c < 5; ++c) B[i + 2][c] = bld(rs, l16, OFF_WT + ((5 * cb + c) * 74 + 54 + ks0 + i + 2) * 1024);
    }
    __builtin_amdgcn_sched_barrier(0);
#pragma unroll
    for (int l = 0; l < 4; ++l)
#pragma unroll
      for (int c = 0; c < 5; ++c) acc[l][c] = mfma(B[i][c], A[i][l], acc[l][c]);
    __builtin_amdgcn_sched_barrier(0);
  }
#pragma unroll
  for (int l = 0; l < 4; ++l)
#pragma unroll
    for (int c = 0; c < 5; ++c) *(LDSP f32x4*)(lds + K2_SD + aw * 20480 + (l * 5 + c) * 1024 + l16) = acc[l][c];
  DG(T[6] = stamp();)
  __syncthreads();
  DG_OUT2();
}

extern "C" void kernel_launch(void* const* d_in, const int* in_sizes, int n_in, void* d_out, int out_size, void* d_ws,
                              size_t ws_size, hipStream_t stream) {
  const u16* x = (const u16*)d_in[0];
  const u16* wqkv = (const u16*)d_in[1];
  const u16* wphi = (const u16*)d_in[2];
  const u16* wo = (const u16*)d_in[3];
  const u16* w1 = (const u16*)d_in[5];
  const u16* b1 = (const u16*)d_in[6];
  const u16* w2 = (const u16*)d_in[7];
  (void)hipFuncSetAttribute((const void*)hedgehog_k1, hipFuncAttributeMaxDynamicSharedMemorySize, K1_LDS);
  (void)hipFuncSetAttribute((const void*)hedgehog_k2, hipFuncAttributeMaxDynamicSharedMemorySize, K2_LDS);
  hedgehog_k1<<<256, 768, K1_LDS, stream>>>(x, wqkv, wphi, wo, w1, b1, w2, (char*)d_ws);
  hedgehog_k2<<<256, 512, K2_LDS, stream>>>(x, wphi, (char*)d_ws, (u16*)d_out);
}
```

```cpp
#define NOROT
#include <hip/hip_runtime.h>
#include <stdint.h>

typedef unsigned short u16;
typedef __attribute__((ext_vector_type(2))) __bf16 bf16x2;
typedef __attribute__((ext_vector_type(8))) __bf16 bf16x8;
typedef __attribute__((ext_vector_type(4))) short s16x4;
typedef __attribute__((ext_vector_type(2))) float f32x2;
typedef __attribute__((ext_vector_type(4))) float f32x4;
typedef __attribute__((ext_vector_type(2))) unsigned u32x2;
typedef __attribute__((ext_vector_type(4))) unsigned u32x4;
#define LDSP __attribute__((address_space(3)))
#define DEVI __device__ __forceinline__

constexpr unsigned OFF_ACT = 0;
constexpr unsigned OFF_WT = OFF_ACT + 128u * 54u * 1024u;
constexpr unsigned OFF_OT = OFF_WT + 40u * 74u * 1024u;
constexpr unsigned OFF_QT = OFF_OT + 128u * 20u * 1024u;
constexpr unsigned OFF_KVP = OFF_QT + 32u * 2048u * 48u;
constexpr unsigned KVP_B = 3 * 1344;
constexpr unsigned OFF_DUMMY = OFF_KVP + 32u * 16u * KVP_B;
constexpr unsigned WS_END = OFF_DUMMY + 640u * 1024u;

#define CODE_BYTES (46 * 1024)
#define K1_MAP 1
constexpr float L2E = 1.4426950408889634f;

DEVI unsigned pk2(float a, float b) {
  bf16x2 v = {(__bf16)a, (__bf16)b};
  return __builtin_bit_cast(unsigned, v);
}
DEVI float bflo(unsigned u) { return __uint_as_float(u << 16); }
DEVI float bfhi(unsigned u) { return __uint_as_float(u & 0xffff0000u); }
DEVI f32x4 mfma(u32x4 a, u32x4 b, f32x4 c) {
  return __builtin_amdgcn_mfma_f32_16x16x32_bf16(__builtin_bit_cast(bf16x8, a), __builtin_bit_cast(bf16x8, b), c, 0, 0, 0);
}
DEVI u32x2 trread(LDSP char* p) {
  return __builtin_bit_cast(u32x2, __builtin_amdgcn_ds_read_tr16_b64_v4i16((LDSP s16x4*)p));
}
DEVI u32x4 trread2(LDSP char* p0, LDSP char* p1) {
  u32x2 a = trread(p0), b = trread(p1);
  return u32x4{a[0], a[1], b[0], b[1]};
}
DEVI float dot2(unsigned a, unsigned b, float c) {
  return __builtin_amdgcn_fdot2_f32_bf16(__builtin_bit_cast(bf16x2, a), __builtin_bit_cast(bf16x2, b), c, false);
}
DEVI float dot_ones(u32x4 a, float c) {
  c = dot2(a[0], 0x3f803f80u, c); c = dot2(a[1], 0x3f803f80u, c);
  c = dot2(a[2], 0x3f803f80u, c); c = dot2(a[3], 0x3f803f80u, c);
  return c;
}
DEVI float dot_self(u32x4 a, float c) {
  c = dot2(a[0], a[0], c); c = dot2(a[1], a[1], c); c = dot2(a[2], a[2], c); c = dot2(a[3], a[3], c);
  return c;
}
DEVI float red_g(float v) {
  v += __shfl_xor(v, 16);
  v += __shfl_xor(v, 32);
  return v;
}
DEVI void glds16(unsigned voff, const void* sbase, unsigned lds_dst) {
  unsigned keep;
  asm volatile("s_mov_b32 %0, m0\n\ts_mov_b32 m0, %3\n\ts_nop 0\n\tglobal_load_lds_dwordx4 %1, %2\n\ts_mov_b32 m0, %0"
               : "=&s"(keep) : "v"(voff), "s"(sbase), "s"(lds_dst) : "memory");
}
#define DG(...)
#define WAIT_VM(n) asm volatile("s_waitcnt vmcnt(" #n ")" ::: "memory")
#define WAIT_LGKM0() asm volatile("s_waitcnt lgkmcnt(0)" ::: "memory")

DEVI void feature_map(const f32x4 (&p)[5], unsigned (&o)[20]) {
  float mx = p[0][0], mn = p[0][0];
#pragma unroll
  for (int t = 0; t < 5; ++t)
#pragma unroll
    for (int r = 0; r < 4; ++r) { mx = fmaxf(mx, p[t][r]); mn = fminf(mn, p[t][r]); }
  const float a = -mx * L2E, b = mn * L2E;
  float e1[20], e2[20], s1 = 0.f, s2 = 0.f;
#pragma unroll
  for (int t = 0; t < 5; ++t)
#pragma unroll
    for (int r = 0; r < 4; ++r) {
      e1[4 * t + r] = __builtin_amdgcn_exp2f(fmaf(p[t][r], L2E, a));
      e2[4 * t + r] = __builtin_amdgcn_exp2f(fmaf(p[t][r], -L2E, b));
      s1 += e1[4 * t + r]; s2 += e2[4 * t + r];
    }
  const float r1 = __builtin_amdgcn_rcpf(s1), r2 = __builtin_amdgcn_rcpf(s2);
#pragma unroll
  for (int i = 0; i < 10; ++i) {
    o[i] = pk2(e1[2 * i] * r1, e1[2 * i + 1] * r1);
    o[10 + i] = pk2(e2[2 * i] * r2, e2[2 * i + 1] * r2);
  }
}
DEVI void head_proj(LDSP char* wl, const u32x4 (&b)[3], f32x4 (&p)[5]) {
#pragma unroll
  for (int t = 0; t < 5; ++t) {
    u32x4 a0 = trread2(wl + 0 * 40 + t * 8, wl + 4 * 40 + t * 8);
    u32x4 a1 = trread2(wl + 8 * 40 + t * 8, wl + 12 * 40 + t * 8);
    u32x2 h2 = trread(wl + 16 * 40 + t * 8);
    u32x4 a2 = {h2[0], h2[1], 0u, 0u};
    f32x4 c = {0.f, 0.f, 0.f, 0.f};
    c = mfma(a0, b[0], c);
    c = mfma(a1, b[1], c);
    c = mfma(a2, b[2], c);
    p[t] = c;
  }
}

constexpr unsigned XSLOT = 16384, WSUB = 5120, WSLOT = 3 * WSUB;
constexpr unsigned K1_W = 4 * XSLOT;
constexpr unsigned K1_WPHI = K1_W + 5 * WSLOT;
constexpr unsigned K1_ZERO = K1_WPHI + 3200;
constexpr unsigned K1_STAT = K1_ZERO + 768;
constexpr unsigned K1_CS = K1_STAT + 1024;
constexpr unsigned K1_BL = K1_CS + 960;
constexpr unsigned K1_SCR = K1_BL + 960;
constexpr unsigned K1_MB = K1_SCR + 256;
constexpr unsigned K1_LDS = K1_MB + 16;
constexpr unsigned PK_PITCH = 352, V_PITCH = 224, K1_VIMG = 128 * PK_PITCH;
constexpr unsigned K1_HO = K1_VIMG + 128 * V_PITCH;
constexpr unsigned K1_RT = K1_W + 2 * WSLOT;

DEVI void glds16n(unsigned voff, const void* sbase, unsigned lds_dst) {
  asm volatile("s_mov_b32 m0, %2\n\ts_nop 0\n\tglobal_load_lds_dwordx4 %0, %1" ::"v"(voff), "s"(sbase), "s"(lds_dst) : "memory");
}
DEVI void st16(__amdgpu_buffer_rsrc_t rs, u32x4 v, unsigned voff, unsigned soff) {
  __builtin_amdgcn_raw_buffer_store_b128(v, rs, voff + soff, 0, 16);
}
DEVI void st8(__amdgpu_buffer_rsrc_t rs, u32x2 v, unsigned voff, unsigned soff) {
  __builtin_amdgcn_raw_buffer_store_b64(v, rs, voff + soff, 0, 16);
}
#define STEP_SYNC() do { __builtin_amdgcn_s_waitcnt(0xC07F); __builtin_amdgcn_s_barrier(); __builtin_amdgcn_sched_barrier(0); } while (0)

DEVI void mb_arrive(LDSP char* lds, int i) {
  WAIT_LGKM0();
  if ((threadIdx.x & 63) == 0) __hip_atomic_fetch_add((LDSP unsigned*)(lds + K1_MB + 4 * i), 1u, __ATOMIC_RELAXED, __HIP_MEMORY_SCOPE_WORKGROUP);
}
DEVI void mb_wait(LDSP char* lds, int i) {
  for (int spin = 0; spin < (1 << 22); ++spin) {
    if (__builtin_amdgcn_readfirstlane(*(volatile LDSP unsigned*)(lds + K1_MB + 4 * i)) >= 6u) break;
    __builtin_amdgcn_s_sleep(1);
  }
  asm volatile("" ::: "memory");
  __builtin_amdgcn_sched_barrier(0);
}
struct K1C {
  LDSP char* lds;
  unsigned xa, wa[5], wr;
  DG(unsigned long long t1;)
};
template <int NC>
DEVI void c_read(K1C& S, int s, u32x4 (&xf)[4], u32x4 (&wf)[5]) {
  LDSP char* px = S.lds + ((s >> 1) & 3) * XSLOT + (S.xa ^ ((s & 1) << 6));
  LDSP char* pw = S.lds + S.wr;
  xf[0] = *(LDSP u32x4*)(px);
  wf[0] = trread2(pw + S.wa[0], pw + S.wa[0] + 1280);
#pragma unroll
  for (int r = 1; r < 4; ++r) xf[r] = *(LDSP u32x4*)(px + r * 2048);
#pragma unroll
  for (int c = 1; c < NC; ++c) wf[c] = trread2(pw + S.wa[c], pw + S.wa[c] + 1280);
  S.wr = S.wr == 4 * WSLOT ? 0 : S.wr + WSLOT;
}
template <int NC, int ROLE, bool READ, bool MINI = false>
DEVI void c_step(K1C& S, int s, const u32x4 (&xc)[4], const u32x4 (&wc)[5], u32x4 (&xn)[4], u32x4 (&wn)[5], f32x4 (&acc)[4][5],
                 f32x4 (&cs)[3], f32x4 (&s1)[4], f32x4 (&s2)[4], const u32x4& ones) {
  if (READ) {
    if (MINI) { WAIT_VM(0); mb_arrive(S.lds, 1); mb_wait(S.lds, 1); } else STEP_SYNC();
    c_read<NC>(S, s + 1, xn, wn);
  }
#pragma unroll
  for (int c = 0; c < NC; ++c)
#pragma unroll
    for (int r = 0; r < 4; ++r) acc[r][c] = mfma(wc[c], xc[r], acc[r][c]);
  if (ROLE) {
    constexpr int C0 = ROLE == 1 ? 0 : NC - 2, C1 = ROLE == 1 ? NC - 2 : NC;
#pragma unroll
    for (int r = 0; r < 4; ++r) {
      s1[r] = mfma(ones, xc[r], s1[r]);
      s2[r] = mfma(xc[r], xc[r], s2[r]);
    }
#pragma unroll
    for (int c = C0; c < C1; ++c) cs[c - C0] = mfma(wc[c], ones, cs[c - C0]);
  }
  if (READ) {
#pragma unroll
    for (int i = 0; i < 4 + 2 * NC; ++i) {
      __builtin_amdgcn_sched_group_barrier(0x8, 1, 0);
      __builtin_amdgcn_sched_group_barrier(0x100, 1, 0);
    }
  }
  __builtin_amdgcn_sched_barrier(0);
}
template <int NC, int ROLE>
DEVI void c_main(K1C& S, f32x4 (&acc)[4][5], f32x4 (&cs)[3], f32x4 (&s1)[4], f32x4 (&s2)[4]) {
  u32x4 xa[4], wa[5], xb[4], wb[5];
  u32x4 ones = {0x3f803f80u, 0x3f803f80u, 0x3f803f80u, 0x3f803f80u};
  asm volatile("" : "+v"(ones));
  c_read<NC>(S, 0, xa, wa);
  c_step<NC, ROLE, true, true>(S, 0, xa, wa, xb, wb, acc, cs, s1, s2, ones);
#pragma unroll 1
  for (int s = 1; s < 19; s += 2) {
    c_step<NC, ROLE, true>(S, s, xb, wb, xa, wa, acc, cs, s1, s2, ones);
    c_step<NC, ROLE, true>(S, s + 1, xa, wa, xb, wb, acc, cs, s1, s2, ones);
  }
  c_step<NC, ROLE, false>(S, 19, xb, wb, xa, wa, acc, cs, s1, s2, ones);
}

struct K1H {
  LDSP char* lds;
  unsigned ldsbase;
  unsigned vw[8], vx[4];
  const char* pw; const char* px;
  unsigned incw, wrapw, wrapx;
  unsigned dw, dx;
  unsigned wa[5], wr;
  __amdgpu_buffer_rsrc_t rs;
  const u16* w2; const u16* wo;
  int first;
  DG(unsigned long long t1;)
};
template <int HP, int ODD>
DEVI void h_issue(K1H& S) {
#pragma unroll
  for (int i = 0; i < 8 - HP; ++i) glds16n(S.vw[i], S.pw, S.ldsbase + K1_W + HP * 8192 + i * 1024 + S.dw);
#pragma unroll
  for (int i = 0; i < 4; ++i) glds16n(S.vx[i], S.px, S.ldsbase + HP * 4096 + i * 1024 + S.dx);
  S.pw += S.incw;
  S.pw -= (unsigned)(size_t)S.pw == S.wrapw ? 18 * S.incw : 0u;
  S.px += ODD ? 128 - 64 * 1280 : 64 * 1280;
  S.px -= (unsigned)(size_t)S.px == S.wrapx ? 1152u : 0u;
  S.dw = S.dw == 4 * WSLOT ? 0 : S.dw + WSLOT;
  S.dx = (S.dx + 8192) & 0xffff;
}
template <int HP, int G> DEVI void h_wait() {
  constexpr int N = G * (12 - HP);
  asm volatile("s_waitcnt vmcnt(%0)" ::"i"(N) : "memory");
}
DEVI void h_read(K1H& S, u32x4 (&wf)[5]) {
  LDSP char* pw = S.lds + S.wr;
#pragma unroll
  for (int c = 0; c < 5; ++c) wf[c] = trread2(pw + S.wa[c], pw + S.wa[c] + 1280);
  S.wr = S.wr == 4 * WSLOT ? 0 : S.wr + WSLOT;
}
template <int HP, int G, int ISSUE>
DEVI void h_step(K1H& S, const u32x4 (&wc)[5], u32x4 (&wn)[5], f32x4 (&cs)[5], const u32x4& ones) {
  if (G >= 0 && G < 9) h_wait<HP, (G >= 0 && G < 9 ? G : 0)>();
  if (G >= 0) STEP_SYNC();
#pragma unroll
  for (int c = 0; c < 5; ++c) cs[c] = mfma(wc[c], ones, cs[c]);
  h_read(S, wn);
  __builtin_amdgcn_sched_barrier(0);
  if (ISSUE) h_issue<HP, ISSUE - 1>(S);
  __builtin_amdgcn_sched_barrier(0);
}
template <int HP>
DEVI void h_main(K1H& S, f32x4 (&cs)[5]) {
  u32x4 wa[5], wb[5];
  u32x4 ones = {0x3f803f80u, 0x3f803f80u, 0x3f803f80u, 0x3f803f80u};
  asm volatile("" : "+v"(ones));
  h_issue<HP, 1>(S);
  h_issue<HP, 0>(S);
  h_issue<HP, 1>(S);
  mb_wait(S.lds, 0);
  h_read(S, wa);
  mb_wait(S.lds, 1);
  h_step<HP, -1, 1>(S, wa, wb, cs, ones);
  h_step<HP, 3, 2>(S, wb, wa, cs, ones);
#pragma unroll 1
  for (int s = 2; s < 14; s += 2) {
    h_step<HP, 3, 1>(S, wa, wb, cs, ones);
    h_step<HP, 3, 2>(S, wb, wa, cs, ones);
  }
  h_step<HP, 3, 1>(S, wa, wb, cs, ones);
  constexpr int n = 12 - HP;
  const int lane = threadIdx.x & 63;
  h_step<HP, 3, 0>(S, wb, wa, cs, ones);
#pragma unroll
  for (int i = 0; i < 5; ++i) st16(S.rs, u32x4{~0u, ~0u, ~0u, ~0u}, lane * 16, OFF_OT + (blockIdx.x * 10 + HP * 5 + i) * 1024);
  asm volatile("s_waitcnt vmcnt(%0)" ::"i"(2 * n + 5) : "memory");
  h_step<HP, 9, 0>(S, wa, wb, cs, ones);
  unsigned rv[6];
#pragma unroll
  for (int i = 0; i < 6; ++i) {
    const int gi = S.first + i, fid = gi < 2960 ? gi : 2959;
    const int f4 = fid >> 2;
    const bool isw2 = f4 < 540;
    const int f2 = isw2 ? f4 : f4 - 540;
    const int cq = isw2 ? f2 / 54 : f2 / 20, ks = f2 - (isw2 ? 54 : 20) * cq;
    const int ct = 4 * cq + (fid & 3);
    const int rho = lane >> 1, e = 4 * ks + ((rho >> 2) & 3), i5 = e / 5, r5 = e - 5 * i5;
    const int lo = 40 * i5 + (r5 == 4 ? 16 : (r5 >> 1) * 20 + (r5 & 1) * 8);
    const int row = isw2 ? 32 * ks + rho : lo + ((rho & 16) ? (r5 == 4 ? 20 : 4) : 0) + (rho & 3);
    rv[i] = row * 1280 + ct * 32 + (lane & 1) * 16;
    asm volatile("" : "+v"(rv[i]));
  }
  asm volatile("s_waitcnt vmcnt(%0)" ::"i"(n + 5) : "memory");
  h_step<HP, 9, 0>(S, wb, wa, cs, ones);
#pragma unroll
  for (int i = 0; i < 6; ++i) glds16n(rv[i], S.first + i < 2160 ? S.w2 : S.wo, S.ldsbase + K1_RT + (HP * 6 + i) * 1024);
  WAIT_VM(11);
  h_step<HP, 9, 0>(S, wa, wb, cs, ones);
#pragma unroll
  for (int c = 0; c < 5; ++c) cs[c] = mfma(wb[c], ones, cs[c]);
  __builtin_amdgcn_s_setprio(0);
}

__global__ __launch_bounds__(512, 2) void hedgehog_k1(const u16* __restrict__ x, const u16* __restrict__ wqkv,
                                                      const u16* __restrict__ wphi, const u16* __restrict__ wo,
                                                      const u16* __restrict__ w1, const u16* __restrict__ b1,
                                                      const u16* __restrict__ w2, char* __restrict__ ws) {
  extern __shared__ __attribute__((aligned(16))) char smem[];
  LDSP char* lds = (LDSP char*)smem;
  const unsigned long long kpc = __builtin_amdgcn_s_getpc();
  const int tid = threadIdx.x, lane = tid & 63;
  const int wave = __builtin_amdgcn_readfirstlane(tid >> 6);
  const int b = blockIdx.x, xcd = b & 7, jx = b >> 3;
  const unsigned ldsbase = (unsigned)(size_t)lds;
  const bool helper = wave >= 6;
  DG(unsigned long long T[8]; T[0] = stamp(); T[7] = rstamp(); T[1] = T[6] = 0;)
  if (helper) {
    const char* pc = (const char*)(kpc & ~(unsigned long long)127) + min(((jx % 3) * 2 + (wave & 1)) * 8192 + lane * 128, CODE_BYTES);
    asm volatile("s_mov_b32 m0, %1\n\ts_nop 0\n\tglobal_load_lds_dword %0, off" ::"v"(pc), "s"(ldsbase + K1_SCR) : "memory");
  }
  if (tid == 0) *(LDSP u32x2*)(lds + K1_MB) = u32x2{0u, 0u};
  WAIT_LGKM0();
  asm volatile("" ::"s"(x), "s"(wqkv), "s"(wphi), "s"(wo), "s"(w1), "s"(b1), "s"(w2), "s"(ws));
  __builtin_amdgcn_s_barrier();
  const int mh = wave & 1;
  const int nj = wave < 4 ? wave >> 1 : 2;
  const int cj = helper ? 2 * mh : nj;
  const int g = lane >> 4, m = lane & 15, q4 = m >> 2, p4 = lane & 3;
  const int rb = 8 * (xcd >> 2) + (jx & 7);
  const int cgi = jx >> 3;
  const bool atype = cgi < 2;
  const int grp = 2 * (xcd & 3) + (cgi & 1);
  const u16* W = atype ? wqkv : w1;
  const int ldw = atype ? 1920 : 1728;
  const int c0 = atype ? 80 * grp : (grp < 6 ? 224 * grp : 1344 + 192 * (grp - 6));
  const int sub = atype ? 640 : (grp < 6 ? 80 : 64);
  const unsigned incw = 64 * ldw;
  const char* xrow = (const char*)x + (size_t)rb * 128 * 1280;
  const int t1 = 1;
  auto wvoff = [&](int id) {
    const int sj = id / 5, ss = id - 5 * sj;
    const int c = 64 * ss + lane, rho = c / 10, cc = c - 10 * rho;
    const int k = (rho & 0x13) | ((rho & 4) << 1) | ((rho & 8) >> 1);
    const bool narrow = !atype && (grp >= 6 || sj == 2);
    return (unsigned)(k * ldw + c0 + sub * sj + 8 * ((narrow && cc >= 8) ? cc - 2 : cc)) * 2;
  };
  auto xvoff = [&](int pc) {
    const int row = pc * 8 + (lane >> 3);
    return (unsigned)(row * 1280 + (((lane & 7) ^ ((row >> 1) & 7)) << 4));
  };
  const unsigned wa0 = K1_W + cj * WSUB + (16 * (g >> 1) + 4 * (g & 1) + q4) * 160 + p4 * (atype ? 40 : 8);
  const unsigned cstr = atype ? 8 : 32;
  const __amdgpu_buffer_rsrc_t rs = __builtin_amdgcn_make_buffer_rsrc((void*)ws, 0, WS_END, 0x00020000);
  f32x4 acc[4][5];

  if (!helper) {
    const unsigned x0 = xvoff(wave), x1 = xvoff(wave < 2 ? wave + 6 : 7);
    auto xgrp = [&](int u, int xt) {
      const char* px = xrow + xt * 128 + (u & 1) * (64 * 1280);
      const unsigned d = ldsbase + u * 8192 + wave * 1024;
      glds16n(x0, px, d);
      if (wave < 2) glds16n(x1, px, d + 6144);
    };
    xgrp(0, 0);
    xgrp(1, 0);
    const unsigned dw = ldsbase + K1_W + wave * 1024;
    const unsigned v0 = wvoff(wave);
    glds16n(v0, W, dw);
    const unsigned v1 = wvoff(wave + 6);
    glds16n(v1, W, dw + 6144);
    const unsigned v2 = wvoff(wave < 3 ? wave + 12 : 14);
    if (wave < 3) glds16n(v2, W, dw + 12288);
    glds16n(v0, (const char*)W + incw, dw + WSLOT);
    glds16n(v1, (const char*)W + incw, dw + WSLOT + 6144);
    if (wave < 3) glds16n(v2, (const char*)W + incw, dw + WSLOT + 12288);
    xgrp(2, t1);
    K1C S;
    S.lds = lds;
    S.xa = (mh * 64 + m) * 128 + ((g ^ (m >> 1)) << 4);
#pragma unroll
    for (int c = 0; c < 5; ++c) S.wa[c] = wa0 + c * cstr;
    S.wr = 0;
    DG(S.t1 = 0;)
    f32x4 cs[3], s1[4], s2[4];
#pragma unroll
    for (int i = 0; i < 4; ++i) {
      s1[i] = f32x4{0.f, 0.f, 0.f, 0.f}; s2[i] = s1[i]; cs[i % 3] = s1[i];
#pragma unroll
      for (int c = 0; c < 5; ++c) acc[i][c] = s1[i];
    }
    if (wave == 4 && lane < 48) *(LDSP u32x4*)(lds + K1_ZERO + lane * 16) = u32x4{0u, 0u, 0u, 0u};
    const bool five = atype || (grp < 6 && nj < 2);
    if (wave < 2) WAIT_VM(5); else if (wave == 2) WAIT_VM(4); else WAIT_VM(3);
    mb_arrive(lds, 0);
    {
      const int col = c0 + sub * (lane / 10) + 8 * (lane % 10);
      const unsigned vo = atype ? tid * 16 : (col > 1720 ? 1720 : col) * 2;
      if (tid < (atype ? 200 : 30)) glds16n(vo, atype ? (const char*)wphi + grp * 3200 : (const char*)b1, ldsbase + K1_WPHI + wave * 1024);
    }
    mb_wait(lds, 0);
    DG(T[1] = stamp();)
    if (nj == 1) {
      if (five) { if (mh == 0) c_main<5, 1>(S, acc, cs, s1, s2); else c_main<5, 2>(S, acc, cs, s1, s2); }
      else { if (mh == 0) c_main<4, 1>(S, acc, cs, s1, s2); else c_main<4, 2>(S, acc, cs, s1, s2); }
      const int cb = mh == 0 ? 0 : (five ? 3 : 2), cn = mh == 0 ? (five ? 3 : 2) : 2;
      if (m == 0) {
#pragma unroll
        for (int c = 0; c < 3; ++c)
          if (c < cn) *(LDSP f32x4*)(lds + K1_CS + (80 + (cb + c) * 16 + 4 * g) * 4) = cs[c];
      }
      if (g == q4) {
#pragma unroll
        for (int r = 0; r < 4; ++r) {
          const float sq = (m & 2) ? ((m & 1) ? s2[r][3] : s2[r][2]) : ((m & 1) ? s2[r][1] : s2[r][0]);
          const float mean = s1[r][0] * (1.0f / 640.0f);
          const float var = fmaxf(sq * (1.0f / 640.0f) - mean * mean, 0.f);
          float rsd = __builtin_amdgcn_rsqf(var + 1e-5f);
          if (!atype) rsd *= L2E;
          *(LDSP f32x2*)(lds + K1_STAT + (mh * 64 + r * 16 + m) * 8) = f32x2{rsd, -mean * rsd};
        }
      }
      if (atype) {
#pragma unroll
        for (int r = 2; r < 4; ++r)
#pragma unroll
          for (int c = 0; c < 5; ++c) *(LDSP f32x4*)(lds + K1_HO + (mh * 2 + r - 2) * 5120 + c * 1024 + lane * 16) = acc[r][c];
      }
    } else {
      if (five) c_main<5, 0>(S, acc, cs, s1, s2); else c_main<4, 0>(S, acc, cs, s1, s2);
      if (!atype) {
#pragma unroll
        for (int c = 0; c < 5; ++c) *(LDSP f32x4*)(lds + K1_HO + (mh * 2 + (nj >> 1)) * 5120 + c * 1024 + lane * 16) = acc[3][c];
        if (nj == 0 && tid < 120) {
          const unsigned w = *(LDSP unsigned*)(lds + K1_WPHI + tid * 4);
          *(LDSP f32x2*)(lds + K1_BL + tid * 8) = f32x2{bflo(w) * L2E, bfhi(w) * L2E};
        }
      }
    }
    DG(T[6] = S.t1;)
  } else {
    __builtin_amdgcn_s_setprio(3);
    K1H S;
    S.lds = lds; S.ldsbase = ldsbase;
    S.incw = incw;
    S.pw = (const char*)W + 2 * t1 * incw;
    S.wrapw = (unsigned)(size_t)W + 20 * incw;
    S.px = xrow + t1 * 128 + 64 * 1280;
    S.wrapx = (unsigned)(size_t)xrow + 1280;
    S.dw = 2 * WSLOT; S.dx = 3 * 8192; S.wr = 0;
    DG(S.t1 = 0;)
#pragma unroll
    for (int i = 0; i < 4; ++i) S.vx[i] = xvoff(4 * mh + i);
#pragma unroll
    for (int i = 0; i < 8; ++i) S.vw[i] = wvoff(8 * mh + i < 15 ? 8 * mh + i : 14);
#pragma unroll
    for (int c = 0; c < 5; ++c) { S.wa[c] = wa0 + c * cstr; acc[0][c] = f32x4{0.f, 0.f, 0.f, 0.f}; }
    S.rs = rs; S.w2 = w2; S.wo = wo; S.first = b * 12 + mh * 6;
    if (mh == 0) h_main<0>(S, acc[0]); else h_main<1>(S, acc[0]);
    DG(T[6] = S.t1;)
    if (m == 0) {
#pragma unroll
      for (int c = 0; c < 5; ++c) *(LDSP f32x4*)(lds + K1_CS + (cj * 80 + c * 16 + 4 * g) * 4) = acc[0][c];
    }
  }
  DG(T[2] = stamp();)
  WAIT_LGKM0();
  __builtin_amdgcn_s_barrier();
  asm volatile("" ::: "memory");
  DG(T[3] = stamp();)
#define DG_OUT1() DG({ T[5] = stamp(); unsigned long long re = rstamp(); if (lane == 0) { unsigned long long* dp = (unsigned long long*)(ws + OFF_DBG) + (size_t)(b * 8 + wave) * 16; for (int i = 0; i < 8; ++i) dp[i] = T[i]; dp[8] = re; } if (lane < 20) ((unsigned*)(ws + OFF_DBG + (2u << 20)))[(b * 8 + wave) * 32 + lane] = *(LDSP unsigned*)(lds + K1_LDS + wave * 128 + lane * 4) - (unsigned)T[0]; })
  auto side_store = [&]() {
    WAIT_VM(0);
    LDSP char* pr = lds + K1_RT + mh * 6144 + (4 * g + q4) * 32 + p4 * 8;
#pragma unroll
    for (int i = 0; i < 6; ++i) {
      const int gi = b * 12 + mh * 6 + i, f4 = gi >> 2;
      const bool isw2 = f4 < 540;
      const int f2 = isw2 ? f4 : f4 - 540;
      const int cq = isw2 ? f2 / 54 : f2 / 20, ks = f2 - (isw2 ? 54 : 20) * cq;
      const u32x4 v = trread2(pr + i * 1024, pr + i * 1024 + 512);
      if (gi < 2960) st16(rs, v, lane * 16, OFF_WT + ((4 * cq + (gi & 3)) * 74 + (isw2 ? 0 : 54) + ks) * 1024);
    }
  };

  if (!atype) {
    auto round = [&](const f32x4 (&a)[5], int ej, int rt) {
      const bool five = grp < 6 && ej < 2;
      const f32x2 st = *(LDSP f32x2*)(lds + K1_STAT + (rt * 16 + m) * 8);
      u32x2 pk[5];
#pragma unroll
      for (int c = 0; c < 5; ++c) {
        if (c == 4 && !five) { pk[4] = u32x2{0u, 0u}; break; }
        const f32x4 cs = *(LDSP f32x4*)(lds + K1_CS + (ej * 80 + c * 16 + 4 * g) * 4);
        const f32x4 bl = *(LDSP f32x4*)(lds + K1_BL + (ej * 80 + c * 16 + 4 * g) * 4);
        float res[4];
#pragma unroll
        for (int e = 0; e < 4; ++e) {
          const float hl = fmaf(a[c][e], st[0], fmaf(st[1], cs[e], bl[e]));
          const float ex = __builtin_amdgcn_exp2f(fminf(hl, 40.f));
          const float n = ex * (ex + 2.f);
          const float rc = __builtin_amdgcn_rcpf(fmaf(n, L2E, 2.f * L2E));
          res[e] = hl * (n * rc);
        }
        pk[c] = u32x2{pk2(res[0], res[1]), pk2(res[2], res[3])};
      }
      const int T0 = (c0 + sub * ej) >> 4;
      const unsigned base = OFF_ACT + ((rb * 8 + rt) * 54 + (T0 >> 1)) * 1024;
      if (!(T0 & 1)) {
        st16(rs, u32x4{pk[0][0], pk[0][1], pk[1][0], pk[1][1]}, lane * 16, base);
        st16(rs, u32x4{pk[2][0], pk[2][1], pk[3][0], pk[3][1]}, lane * 16, base + 1024);
        if (five) st8(rs, pk[4], lane * 16, base + 2048);
      } else {
        st8(rs, pk[0], lane * 16, base + 8);
        st16(rs, u32x4{pk[1][0], pk[1][1], pk[2][0], pk[2][1]}, lane * 16, base + 1024);
        st16(rs, u32x4{pk[3][0], pk[3][1], pk[4][0], pk[4][1]}, lane * 16, base + 2048);
      }
    };
    if (helper) {
#pragma unroll
      for (int r = 0; r < 2; ++r)
#pragma unroll
        for (int c = 0; c < 5; ++c) acc[r][c] = *(LDSP f32x4*)(lds + K1_HO + (mh * 2 + r) * 5120 + c * 1024 + lane * 16);
    }
#pragma unroll 1
    for (int it = 0; it < (helper ? 1 : 2); ++it) {
      round(acc[0], helper ? 0 : nj, mh * 4 + (helper ? 3 : 2 * it));
      if (it == 0 || nj == 1) round(acc[1], helper ? 2 : nj, mh * 4 + (helper ? 3 : 2 * it + 1));
#pragma unroll
      for (int c = 0; c < 5; ++c) { acc[0][c] = acc[2][c]; acc[1][c] = acc[3][c]; }
    }
    DG(T[4] = stamp();)
    if (helper) side_store();
    DG_OUT1();
    return;
  }

  auto normalise = [&](const f32x4 (&a)[5], const f32x4 (&cs)[5], int rt, u32x4 (&bq)[3], unsigned one) {
    const f32x2 st = *(LDSP f32x2*)(lds + K1_STAT + (rt * 16 + m) * 8);
#pragma unroll
    for (int c = 0; c < 5; ++c) {
      float v[4];
#pragma unroll
      for (int e = 0; e < 4; ++e) v[e] = fmaf(a[c][e], st[0], st[1] * cs[c][e]);
      bq[c >> 1][(c & 1) * 2] = pk2(v[0], v[1]);
      bq[c >> 1][(c & 1) * 2 + 1] = pk2(v[2], v[3]);
    }
    bq[2][2] = one;
    bq[2][3] = 0u;
  };
  f32x4 csv[5];
  if (!helper && nj != 1) {
#pragma unroll
    for (int c = 0; c < 5; ++c) csv[c] = *(LDSP f32x4*)(lds + K1_CS + (nj * 80 + c * 16 + 4 * g) * 4);
#pragma unroll
    for (int r = 0; r < 4; ++r) {
      u32x4 bq[3];
      normalise(acc[r], csv, mh * 4 + r, bq, nj == 2 ? 0x00003f80u : 0u);
      if (nj == 0) {
        const unsigned so = OFF_QT + (4 * grp * 128 + rb * 8 + mh * 4 + r) * 768;
#pragma unroll
        for (int i = 0; i < 3; ++i) st16(rs, bq[i], g * (128 * 768) + m * 16, so + i * 256);
      } else {
#pragma unroll
        for (int i = 0; i < 3; ++i) *(LDSP u32x4*)(lds + K1_VIMG + (mh * 64 + r * 16 + m) * V_PITCH + g * 48 + i * 16) = bq[i];
      }
    }
  }
  if (helper || nj != 2) {
    const int kr0 = helper ? 3 : (nj == 1 ? 0 : 2);
    if (kr0) {
#pragma unroll
      for (int c = 0; c < 5; ++c) acc[0][c] = *(LDSP f32x4*)(lds + K1_HO + (mh * 2 + kr0 - 2) * 5120 + c * 1024 + lane * 16);
    }
#pragma unroll
    for (int c = 0; c < 5; ++c) csv[c] = *(LDSP f32x4*)(lds + K1_CS + (80 + c * 16 + 4 * g) * 4);
    LDSP char* wl = lds + ((p4 == g) ? K1_WPHI + g * 800 + q4 * 40 : K1_ZERO);
#pragma unroll 1
    for (int kr = kr0; kr < (kr0 ? kr0 + 1 : 2); ++kr) {
      u32x4 bq[3];
      normalise(acc[0], csv, mh * 4 + kr, bq, 0u);
      f32x4 pk[5];
      head_proj(wl, bq, pk);
      unsigned ph[20];
      feature_map(pk, ph);
      LDSP char* dst = lds + (mh * 64 + kr * 16 + m) * PK_PITCH + g * 80;
#pragma unroll
      for (int i = 0; i < 5; ++i) *(LDSP u32x4*)(dst + i * 16) = u32x4{ph[4 * i], ph[4 * i + 1], ph[4 * i + 2], ph[4 * i + 3]};
#pragma unroll
      for (int c = 0; c < 5; ++c) acc[0][c] = acc[1][c];
    }
  }
  DG(T[4] = stamp();)
  WAIT_LGKM0();
  __builtin_amdgcn_s_barrier();
  asm volatile("" ::: "memory");
  if (!helper) {
#pragma unroll 1
    for (int job = wave; job < 12; job += 6) {
      const int hh = job & 3, ft = job >> 2;
      LDSP char* pa = lds + (4 * g + q4) * PK_PITCH + hh * 80 + ft * 32 + p4 * 8;
      LDSP char* pb = lds + K1_VIMG + (4 * g + q4) * V_PITCH + hh * 48 + p4 * 8;
      f32x4 d0 = {0.f, 0.f, 0.f, 0.f}, d1 = {0.f, 0.f, 0.f, 0.f};
#pragma unroll
      for (int kk = 0; kk < 4; ++kk) {
        u32x4 a = trread2(pa + (32 * kk) * PK_PITCH, pa + (32 * kk + 16) * PK_PITCH);
        u32x4 v0 = trread2(pb + (32 * kk) * V_PITCH, pb + (32 * kk + 16) * V_PITCH);
        u32x4 v1 = trread2(pb + (32 * kk) * V_PITCH + 32, pb + (32 * kk + 16) * V_PITCH + 32);
        d0 = mfma(a, v0, d0);
        d1 = mfma(a, v1, d1);
      }
      const unsigned so = OFF_KVP + ((4 * grp + hh) * 16 + rb) * KVP_B + 1344 * ft;
      st16(rs, __builtin_bit_cast(u32x4, d0), m * 64 + g * 16, so);
      if (m < 5) st16(rs, __builtin_bit_cast(u32x4, d1), (16 + m) * 64 + g * 16, so);
    }
  } else {
    side_store();
  }
  DG_OUT1();
}

constexpr unsigned K2_YD = 0;
constexpr unsigned K2_SD = 4 * 15360;
constexpr unsigned K2_KVT = K2_SD + 4 * 20480;
constexpr unsigned K2_ZERO = K2_KVT + 1920;
constexpr unsigned K2_WPHI = K2_ZERO + 1792;
constexpr unsigned K2_FLAG = K2_WPHI + 3200;
constexpr unsigned K2_LDS = K2_FLAG + 16;

DEVI u32x4 bld(__amdgpu_buffer_rsrc_t rs, unsigned voff, unsigned soff) {
  return __builtin_amdgcn_raw_buffer_load_b128(rs, voff, soff, 0);
}
DEVI u32x4 bld_sc1(__amdgpu_buffer_rsrc_t rs, unsigned voff, unsigned soff) {
  return __builtin_amdgcn_raw_buffer_load_b128(rs, voff, soff, 16);
}

__global__ __launch_bounds__(512, 2) void hedgehog_k2(const u16* __restrict__ x, const u16* __restrict__ wphi,
                                                      char* __restrict__ ws, u16* __restrict__ out) {
  extern __shared__ __attribute__((aligned(16))) char smem[];
  LDSP char* lds = (LDSP char*)smem;
  const int tid = threadIdx.x, lane = tid & 63;
  const int wave = __builtin_amdgcn_readfirstlane(tid >> 6);
  const int g = lane >> 4, m = lane & 15, q4 = m >> 2, p4 = lane & 3;
  const int b = blockIdx.x, xcd = b & 7, jx = b >> 3;
  const int rb2 = 8 * (xcd >> 1) + (jx & 7), cb = 4 * (xcd & 1) + (jx >> 3);
  const __amdgpu_buffer_rsrc_t rs = __builtin_amdgcn_make_buffer_rsrc((void*)ws, 0, WS_END, 0x00020000);
  const unsigned l16 = lane * 16;
  DG(unsigned long long T[8]; T[0] = stamp(); T[7] = rstamp(); T[1] = T[2] = T[3] = T[4] = T[5] = T[6] = 0;)
#define DG_OUT2() DG({ unsigned long long re = rstamp(); if (lane == 0) { unsigned long long* dp = (unsigned long long*)(ws + OFF_DBG + (1u << 20)) + (size_t)(b * 8 + wave) * 16; for (int i = 0; i < 8; ++i) dp[i] = T[i]; dp[8] = re; } })
  f32x4 acc[4][5];
#pragma unroll
  for (int r = 0; r < 4; ++r)
#pragma unroll
    for (int c = 0; c < 5; ++c) acc[r][c] = f32x4{0.f, 0.f, 0.f, 0.f};

  if (wave < 4) {
    const int ks0 = wave < 2 ? 14 * wave : 28 + 13 * (wave - 2);
    const bool n14 = wave < 2;
    unsigned ab[4], bb[5];
#pragma unroll
    for (int l = 0; l < 4; ++l) ab[l] = OFF_ACT + ((4 * rb2 + ((l + wave) & 3)) * 54 + ks0) * 1024;
#pragma unroll
    for (int c = 0; c < 5; ++c) bb[c] = OFF_WT + ((5 * cb + c) * 74 + ks0) * 1024;
    u32x4 A[3][4], B[3][5];
    auto load = [&](int i) {
#pragma unroll
      for (int l = 0; l < 4; ++l) A[i % 3][l] = bld(rs, l16, ab[l] + i * 1024);
#pragma unroll
      for (int c = 0; c < 5; ++c) B[i % 3][c] = bld(rs, l16, bb[c] + i * 1024);
    };
    auto comp = [&](int i) {
#pragma unroll
      for (int l = 0; l < 4; ++l)
#pragma unroll
        for (int c = 0; c < 5; ++c) acc[l][c] = mfma(B[i % 3][c], A[i % 3][l], acc[l][c]);
    };
    load(0);
    load(1);
    u32x2 xv[5];
    const size_t orow = (size_t)(64 * rb2 + 16 * wave + m) * 640 + 80 * cb + 4 * g;
#pragma unroll
    for (int c = 0; c < 5; ++c) xv[c] = *(const u32x2*)(x + orow + 16 * c);
    __builtin_amdgcn_s_barrier();
    DG(T[1] = stamp();)
#pragma unroll
    for (int i = 0; i < 13; ++i) {
      if (i + 2 < 13) load(i + 2);
      else if (i + 2 == 13) { if (n14) load(13); }
      __builtin_amdgcn_sched_barrier(0);
      comp(i);
      __builtin_amdgcn_sched_barrier(0);
      DG(if (i == 0) T[2] = stamp();)
    }
    if (n14) comp(13);
    DG(T[3] = stamp();)
#pragma unroll
    for (int l = 1; l < 4; ++l)
#pragma unroll
      for (int c = 0; c < 5; ++c)
        *(LDSP f32x4*)(lds + K2_YD + wave * 15360 + ((l - 1) * 5 + c) * 1024 + l16) = acc[l][c];
    DG(T[4] = stamp();)
    __syncthreads();
    DG(T[5] = stamp();)
#pragma unroll
    for (int c = 0; c < 5; ++c) {
      f32x4 y = acc[0][c];
#pragma unroll
      for (int l = 1; l < 4; ++l)
        y += *(LDSP f32x4*)(lds + K2_YD + ((wave - l) & 3) * 15360 + ((l - 1) * 5 + c) * 1024 + l16);
      f32x4 s = *(LDSP f32x4*)(lds + K2_SD + (wave * 5 + c) * 1024 + l16);
#pragma unroll
      for (int a = 1; a < 4; ++a) s += *(LDSP f32x4*)(lds + K2_SD + a * 20480 + (wave * 5 + c) * 1024 + l16);
      float o[4];
#pragma unroll
      for (int e = 0; e < 4; ++e) {
        const float gate = y[e] * __builtin_amdgcn_rcpf(1.f + __builtin_amdgcn_exp2f(-y[e] * L2E));
        const unsigned xw = xv[c][e >> 1];
        o[e] = fmaf(s[e], gate, (e & 1) ? bfhi(xw) : bflo(xw));
      }
      *(u32x2*)(out + orow + 16 * c) = u32x2{pk2(o[0], o[1]), pk2(o[2], o[3])};
    }
    DG(T[6] = stamp();)
    DG_OUT2();
    return;
  }

  const int aw = wave - 4;
  const int h = 4 * xcd + (jx & 3), sb = jx >> 2;
  const int tok = sb * 256 + aw * 64 + lane;
  if (aw == 0) {
    *(LDSP u32x4*)(lds + K2_ZERO + l16) = u32x4{0u, 0u, 0u, 0u};
    if (lane < 48) *(LDSP u32x4*)(lds + K2_ZERO + 1024 + l16) = u32x4{0u, 0u, 0u, 0u};
    if (lane == 0) *(LDSP u32x4*)(lds + K2_FLAG) = u32x4{0u, 0u, 0u, 0u};
  }
  u32x4 wv = *(const u32x4*)(wphi + h * 400 + (lane < 50 ? lane : 49) * 8);
  u32x4 bq[3];
#pragma unroll
  for (int s = 0; s < 3; ++s) bq[s] = bld(rs, (unsigned)(tok >> 4) * 768 + m * 16, OFF_QT + h * (128 * 768) + s * 256);
  int item = aw * 53 + (lane < 53 ? lane : 52);
  item = item < 210 ? item : 209;
  const int d = item / 10, fq = item - 10 * d;
  u32x4 part[16];
#pragma unroll
  for (int r = 0; r < 16; ++r) part[r] = bld(rs, (unsigned)((fq >> 2) * 1344 + d * 64 + (fq & 3) * 16), OFF_KVP + (h * 16 + r) * KVP_B);
  WAIT_LGKM0();
  __builtin_amdgcn_s_barrier();
  __builtin_amdgcn_s_setprio(2);
  *(LDSP u32x4*)(lds + K2_WPHI + aw * 800 + (lane < 50 ? lane : 49) * 16) = wv;
  unsigned ph[20];
  {
    LDSP char* wl = lds + ((p4 == g) ? K2_WPHI + aw * 800 + q4 * 40 : K2_ZERO);
    f32x4 pq[5];
    head_proj(wl, bq, pq);
    feature_map(pq, ph);
#pragma unroll
    for (int i = 0; i < 20; ++i) asm volatile("" : "+v"(ph[i]));
  }
  DG(T[1] = stamp();)
  {
    f32x4 sum = __builtin_bit_cast(f32x4, part[0]);
#pragma unroll
    for (int r = 1; r < 16; ++r) sum += __builtin_bit_cast(f32x4, part[r]);
    *(LDSP u32x2*)(lds + K2_KVT + d * 80 + fq * 8) = u32x2{pk2(sum[0], sum[1]), pk2(sum[2], sum[3])};
    WAIT_LGKM0();
    if (lane == 0) *(volatile LDSP unsigned*)(lds + K2_FLAG + aw * 4) = 1u;
    for (int spin = 0; spin < (1 << 22); ++spin) {
      const u32x4 f = *(volatile LDSP u32x4*)(lds + K2_FLAG);
      if (__builtin_amdgcn_readfirstlane(f[0] & f[1] & f[2] & f[3])) break;
      __builtin_amdgcn_s_sleep(1);
    }
    asm volatile("" ::: "memory");
  }
  DG(T[2] = stamp();)
  const int ks0 = 5 * aw;
  u32x4 B[5][5];
#pragma unroll
  for (int i = 0; i < 2; ++i)
#pragma unroll
    for (int c = 0; c < 5; ++c) B[i][c] = bld(rs, l16, OFF_WT + ((5 * cb + c) * 74 + 54 + ks0 + i) * 1024);
  {
    LDSP char* kl = lds + (((m >> 2) == g) ? K2_KVT + (m & 3) * 80 : K2_ZERO);
    f32x4 nd[6];
#pragma unroll
    for (int t = 0; t < 6; ++t) {
      f32x4 c = {0.f, 0.f, 0.f, 0.f};
#pragma unroll
      for (int s = 0; s < 5; ++s)
        c = mfma(*(LDSP u32x4*)(kl + t * 320 + s * 16), u32x4{ph[4 * s], ph[4 * s + 1], ph[4 * s + 2], ph[4 * s + 3]}, c);
      nd[t] = c;
    }
    const float rd = __builtin_amdgcn_rcpf(nd[5][0] + 1e-6f);
    unsigned ow[10];
#pragma unroll
    for (int t = 0; t < 5; ++t) {
      ow[2 * t] = min(pk2(nd[t][0] * rd, nd[t][1] * rd), 0xFFFFFFFEu);
      ow[2 * t + 1] = min(pk2(nd[t][2] * rd, nd[t][3] * rd), 0xFFFFFFFEu);
    }
    const int e0 = 5 * (h >> 1) + 2 * (h & 1), e1 = e0 + 1, e4 = 5 * (h >> 1) + 4;
    const unsigned vo = (unsigned)(tok >> 4) * 20480 + m * 16;
    st16(rs, u32x4{ow[0], ow[1], ow[2], ow[3]}, vo, OFF_OT + (e0 >> 2) * 1024 + (e0 & 3) * 256);
    st16(rs, u32x4{ow[4], ow[5], ow[6], ow[7]}, vo, OFF_OT + (e1 >> 2) * 1024 + (e1 & 3) * 256);
    st8(rs, u32x2{ow[8], ow[9]}, vo, OFF_OT + (e4 >> 2) * 1024 + (e4 & 3) * 256 + (h & 1) * 8);
  }
  DG(T[3] = stamp();)
  const unsigned ob = OFF_OT + (4 * rb2 * 20 + ks0) * 1024;
  {
    const int l2 = lane < 40 ? lane : 39;
    const unsigned pv = 3 * 20480 + (l2 >> 3) * 1024 + ((l2 >> 1) & 3) * 256 + 15 * 16 + (l2 & 1) * 8;
    for (int spin = 0; spin < (1 << 20); ++spin) {
      asm volatile("" ::: "memory");
      const unsigned w = __builtin_amdgcn_raw_buffer_load_b32(rs, pv, ob, 16);
      if (!__any(w == 0xFFFFFFFFu)) break;
      __builtin_amdgcn_s_sleep(4);
    }
  }
  DG(T[4] = stamp();)
  u32x4 A[5][4];
  for (int spin = 0; spin < (1 << 16); ++spin) {
    asm volatile("" ::: "memory");
    unsigned mx = 0;
#pragma unroll
    for (int i = 0; i < 5; ++i)
#pragma unroll
      for (int l = 0; l < 4; ++l) A[i][l] = bld_sc1(rs, l16, ob + l * 20480 + i * 1024);
#pragma unroll
    for (int i = 0; i < 5; ++i)
#pragma unroll
      for (int l = 0; l < 4; ++l) mx = max(max(mx, max(A[i][l][0], A[i][l][1])), max(A[i][l][2], A[i][l][3]));
    if (!__any(mx == 0xFFFFFFFFu)) break;
    __builtin_amdgcn_s_sleep(4);
  }
  DG(T[5] = stamp();)
#pragma unroll
  for (int i = 0; i < 5; ++i) {
    if (i + 2 < 5) {
#pragma unroll
      for (int c = 0; c < 5; ++c) B[i + 2][c] = bld(rs, l16, OFF_WT + ((5 * cb + c) * 74 + 54 + ks0 + i + 2) * 1024);
    }
    __builtin_amdgcn_sched_barrier(0);
#pragma unroll
    for (int l = 0; l < 4; ++l)
#pragma unroll
      for (int c = 0; c < 5; ++c) acc[l][c] = mfma(B[i][c], A[i][l], acc[l][c]);
    __builtin_amdgcn_sched_barrier(0);
  }
#pragma unroll
  for (int l = 0; l < 4; ++l)
#pragma unroll
    for (int c = 0; c < 5; ++c) *(LDSP f32x4*)(lds + K2_SD + aw * 20480 + (l * 5 + c) * 1024 + l16) = acc[l][c];
  DG(T[6] = stamp();)
  __syncthreads();
  DG_OUT2();
}

extern "C" void kernel_launch(void* const* d_in, const int* in_sizes, int n_in, void* d_out, int out_size, void* d_ws,
                              size_t ws_size, hipStream_t stream) {
  const u16* x = (const u16*)d_in[0];
  const u16* wqkv = (const u16*)d_in[1];
  const u16* wphi = (const u16*)d_in[2];
  const u16* wo = (const u16*)d_in[3];
  const u16* w1 = (const u16*)d_in[5];
  const u16* b1 = (const u16*)d_in[6];
  const u16* w2 = (const u16*)d_in[7];
  (void)hipFuncSetAttribute((const void*)hedgehog_k1, hipFuncAttributeMaxDynamicSharedMemorySize, K1_LDS DG(+ 1024));
  (void)hipFuncSetAttribute((const void*)hedgehog_k2, hipFuncAttributeMaxDynamicSharedMemorySize, K2_LDS);
  hedgehog_k1<<<256, 512, K1_LDS DG(+ 1024), stream>>>(x, wqkv, wphi, wo, w1, b1, w2, (char*)d_ws);
  hedgehog_k2<<<256, 512, K2_LDS, stream>>>(x, wphi, (char*)d_ws, (u16*)d_out);
}
```

```cpp
#define NOROT
#include <hip/hip_runtime.h>
#include <stdint.h>

typedef unsigned short u16;
typedef __attribute__((ext_vector_type(2))) __bf16 bf16x2;
typedef __attribute__((ext_vector_type(8))) __bf16 bf16x8;
typedef __attribute__((ext_vector_type(4))) short s16x4;
typedef __attribute__((ext_vector_type(2))) float f32x2;
typedef __attribute__((ext_vector_type(4))) float f32x4;
typedef __attribute__((ext_vector_type(2))) unsigned u32x2;
typedef __attribute__((ext_vector_type(4))) unsigned u32x4;
#define LDSP __attribute__((address_space(3)))
#define DEVI __device__ __forceinline__

constexpr unsigned OFF_ACT = 0;
constexpr unsigned OFF_WT = OFF_ACT + 128u * 54u * 1024u;
constexpr unsigned OFF_OT = OFF_WT + 40u * 74u * 1024u;
constexpr unsigned OFF_QT = OFF_OT + 128u * 20u * 1024u;
constexpr unsigned OFF_KVP = OFF_QT + 32u * 2048u * 48u;
constexpr unsigned KVP_B = 3 * 1344;
constexpr unsigned OFF_DUMMY = OFF_KVP + 32u * 16u * KVP_B;
constexpr unsigned WS_END = OFF_DUMMY + 640u * 1024u;

#define CODE_BYTES (46 * 1024)
#define K1_MAP 1
constexpr float L2E = 1.4426950408889634f, LN2 = 0.6931471805599453f;

DEVI unsigned pk2(float a, float b) {
  bf16x2 v = {(__bf16)a, (__bf16)b};
  return __builtin_bit_cast(unsigned, v);
}
DEVI float bflo(unsigned u) { return __uint_as_float(u << 16); }
DEVI float bfhi(unsigned u) { return __uint_as_float(u & 0xffff0000u); }
DEVI f32x4 mfma(u32x4 a, u32x4 b, f32x4 c) {
  return __builtin_amdgcn_mfma_f32_16x16x32_bf16(__builtin_bit_cast(bf16x8, a), __builtin_bit_cast(bf16x8, b), c, 0, 0, 0);
}
DEVI u32x2 trread(LDSP char* p) {
  return __builtin_bit_cast(u32x2, __builtin_amdgcn_ds_read_tr16_b64_v4i16((LDSP s16x4*)p));
}
DEVI u32x4 trread2(LDSP char* p0, LDSP char* p1) {
  u32x2 a = trread(p0), b = trread(p1);
  return u32x4{a[0], a[1], b[0], b[1]};
}
DEVI float dot2(unsigned a, unsigned b, float c) {
  return __builtin_amdgcn_fdot2_f32_bf16(__builtin_bit_cast(bf16x2, a), __builtin_bit_cast(bf16x2, b), c, false);
}
DEVI float dot_ones(u32x4 a, float c) {
  c = dot2(a[0], 0x3f803f80u, c); c = dot2(a[1], 0x3f803f80u, c);
  c = dot2(a[2], 0x3f803f80u, c); c = dot2(a[3], 0x3f803f80u, c);
  return c;
}
DEVI float dot_self(u32x4 a, float c) {
  c = dot2(a[0], a[0], c); c = dot2(a[1], a[1], c); c = dot2(a[2], a[2], c); c = dot2(a[3], a[3], c);
  return c;
}
DEVI float red_g(float v) {
  v += __shfl_xor(v, 16);
  v += __shfl_xor(v, 32);
  return v;
}
DEVI void glds16(unsigned voff, const void* sbase, unsigned lds_dst) {
  unsigned keep;
  asm volatile("s_mov_b32 %0, m0\n\ts_mov_b32 m0, %3\n\ts_nop 0\n\tglobal_load_lds_dwordx4 %1, %2\n\ts_mov_b32 m0, %0"
               : "=&s"(keep) : "v"(voff), "s"(sbase), "s"(lds_dst) : "memory");
}
#define DG(...)
#define WAIT_VM(n) asm volatile("s_waitcnt vmcnt(" #n ")" ::: "memory")
#define WAIT_LGKM0() asm volatile("s_waitcnt lgkmcnt(0)" ::: "memory")

DEVI void feature_map(const f32x4 (&p)[5], unsigned (&o)[20]) {
  float mx = p[0][0], mn = p[0][0];
#pragma unroll
  for (int t = 0; t < 5; ++t)
#pragma unroll
    for (int r = 0; r < 4; ++r) { mx = fmaxf(mx, p[t][r]); mn = fminf(mn, p[t][r]); }
  const float a = -mx * L2E, b = mn * L2E;
  float e1[20], e2[20], s1 = 0.f, s2 = 0.f;
#pragma unroll
  for (int t = 0; t < 5; ++t)
#pragma unroll
    for (int r = 0; r < 4; ++r) {
      e1[4 * t + r] = __builtin_amdgcn_exp2f(fmaf(p[t][r], L2E, a));
      e2[4 * t + r] = __builtin_amdgcn_exp2f(fmaf(p[t][r], -L2E, b));
      s1 += e1[4 * t + r]; s2 += e2[4 * t + r];
    }
  const float r1 = __builtin_amdgcn_rcpf(s1), r2 = __builtin_amdgcn_rcpf(s2);
#pragma unroll
  for (int i = 0; i < 10; ++i) {
    o[i] = pk2(e1[2 * i] * r1, e1[2 * i + 1] * r1);
    o[10 + i] = pk2(e2[2 * i] * r2, e2[2 * i + 1] * r2);
  }
}
DEVI void head_proj(LDSP char* wl, const u32x4 (&b)[3], f32x4 (&p)[5]) {
#pragma unroll
  for (int t = 0; t < 5; ++t) {
    u32x4 a0 = trread2(wl + 0 * 40 + t * 8, wl + 4 * 40 + t * 8);
    u32x4 a1 = trread2(wl + 8 * 40 + t * 8, wl + 12 * 40 + t * 8);
    u32x2 h2 = trread(wl + 16 * 40 + t * 8);
    u32x4 a2 = {h2[0], h2[1], 0u, 0u};
    f32x4 c = {0.f, 0.f, 0.f, 0.f};
    c = mfma(a0, b[0], c);
    c = mfma(a1, b[1], c);
    c = mfma(a2, b[2], c);
    p[t] = c;
  }
}

constexpr unsigned XSLOT = 16384, WSUB = 5120, WSLOT = 3 * WSUB;
constexpr unsigned K1_W = 4 * XSLOT;
constexpr unsigned K1_WPHI = K1_W + 5 * WSLOT;
constexpr unsigned K1_ZERO = K1_WPHI + 3200;
constexpr unsigned K1_STAT = K1_ZERO + 768;
constexpr unsigned K1_CS = K1_STAT + 1024;
constexpr unsigned K1_BL = K1_CS + 960;
constexpr unsigned K1_SCR = K1_BL + 960;
constexpr unsigned K1_MB = K1_SCR + 256;
constexpr unsigned K1_LDS = K1_MB + 16;
constexpr unsigned PK_PITCH = 352, V_PITCH = 224, K1_VIMG = 128 * PK_PITCH;
constexpr unsigned K1_HO = K1_VIMG + 128 * V_PITCH;
constexpr unsigned K1_RT = K1_W + 2 * WSLOT;

DEVI void glds16n(unsigned voff, const void* sbase, unsigned lds_dst) {
  asm volatile("s_mov_b32 m0, %2\n\ts_nop 0\n\tglobal_load_lds_dwordx4 %0, %1" ::"v"(voff), "s"(sbase), "s"(lds_dst) : "memory");
}
DEVI void st16(__amdgpu_buffer_rsrc_t rs, u32x4 v, unsigned voff, unsigned soff) {
  __builtin_amdgcn_raw_buffer_store_b128(v, rs, voff + soff, 0, 16);
}
DEVI void st8(__amdgpu_buffer_rsrc_t rs, u32x2 v, unsigned voff, unsigned soff) {
  __builtin_amdgcn_raw_buffer_store_b64(v, rs, voff + soff, 0, 16);
}
#define STEP_SYNC() do { __builtin_amdgcn_s_waitcnt(0xC07F); __builtin_amdgcn_s_barrier(); __builtin_amdgcn_sched_barrier(0); } while (0)

DEVI void mb_arrive(LDSP char* lds, int i) {
  WAIT_LGKM0();
  if ((threadIdx.x & 63) == 0) __hip_atomic_fetch_add((LDSP unsigned*)(lds + K1_MB + 4 * i), 1u, __ATOMIC_RELAXED, __HIP_MEMORY_SCOPE_WORKGROUP);
}
DEVI void mb_wait(LDSP char* lds, int i) {
  for (int spin = 0; spin < (1 << 22); ++spin) {
    if (__builtin_amdgcn_readfirstlane(*(volatile LDSP unsigned*)(lds + K1_MB + 4 * i)) >= 6u) break;
    __builtin_amdgcn_s_sleep(1);
  }
  asm volatile("" ::: "memory");
  __builtin_amdgcn_sched_barrier(0);
}
struct K1C {
  LDSP char* lds;
  unsigned xa, wa[5], wr;
  DG(unsigned long long t1;)
};
template <int NC>
DEVI void c_read(K1C& S, int s, u32x4 (&xf)[4], u32x4 (&wf)[5]) {
  LDSP char* px = S.lds + ((s >> 1) & 3) * XSLOT + (S.xa ^ ((s & 1) << 6));
  LDSP char* pw = S.lds + S.wr;
  xf[0] = *(LDSP u32x4*)(px);
  wf[0] = trread2(pw + S.wa[0], pw + S.wa[0] + 1280);
#pragma unroll
  for (int r = 1; r < 4; ++r) xf[r] = *(LDSP u32x4*)(px + r * 2048);
#pragma unroll
  for (int c = 1; c < NC; ++c) wf[c] = trread2(pw + S.wa[c], pw + S.wa[c] + 1280);
  S.wr = S.wr == 4 * WSLOT ? 0 : S.wr + WSLOT;
}
template <int NC, int ROLE, bool READ, bool MINI = false>
DEVI void c_step(K1C& S, int s, const u32x4 (&xc)[4], const u32x4 (&wc)[5], u32x4 (&xn)[4], u32x4 (&wn)[5], f32x4 (&acc)[4][5],
                 f32x4 (&cs)[3], f32x4 (&s1)[4], f32x4 (&s2)[4], const u32x4& ones) {
  if (READ) {
    if (MINI) { WAIT_VM(0); mb_arrive(S.lds, 1); mb_wait(S.lds, 1); } else STEP_SYNC();
    c_read<NC>(S, s + 1, xn, wn);
  }
#pragma unroll
  for (int c = 0; c < NC; ++c)
#pragma unroll
    for (int r = 0; r < 4; ++r) acc[r][c] = mfma(wc[c], xc[r], acc[r][c]);
  if (ROLE) {
    constexpr int C0 = ROLE == 1 ? 0 : NC - 2, C1 = ROLE == 1 ? NC - 2 : NC;
#pragma unroll
    for (int r = 0; r < 4; ++r) {
      s1[r] = mfma(ones, xc[r], s1[r]);
      s2[r] = mfma(xc[r], xc[r], s2[r]);
    }
#pragma unroll
    for (int c = C0; c < C1; ++c) cs[c - C0] = mfma(wc[c], ones, cs[c - C0]);
  }
  if (READ) {
#pragma unroll
    for (int i = 0; i < 4 + 2 * NC; ++i) {
      __builtin_amdgcn_sched_group_barrier(0x8, 1, 0);
      __builtin_amdgcn_sched_group_barrier(0x100, 1, 0);
    }
  }
  __builtin_amdgcn_sched_barrier(0);
}
template <int NC, int ROLE>
DEVI void c_main(K1C& S, f32x4 (&acc)[4][5], f32x4 (&cs)[3], f32x4 (&s1)[4], f32x4 (&s2)[4]) {
  u32x4 xa[4], wa[5], xb[4], wb[5];
  u32x4 ones = {0x3f803f80u, 0x3f803f80u, 0x3f803f80u, 0x3f803f80u};
  asm volatile("" : "+v"(ones));
  c_read<NC>(S, 0, xa, wa);
  c_step<NC, ROLE, true, true>(S, 0, xa, wa, xb, wb, acc, cs, s1, s2, ones);
#pragma unroll 1
  for (int s = 1; s < 19; s += 2) {
    c_step<NC, ROLE, true>(S, s, xb, wb, xa, wa, acc, cs, s1, s2, ones);
    c_step<NC, ROLE, true>(S, s + 1, xa, wa, xb, wb, acc, cs, s1, s2, ones);
  }
  c_step<NC, ROLE, false>(S, 19, xb, wb, xa, wa, acc, cs, s1, s2, ones);
}

struct K1H {
  LDSP char* lds;
  unsigned ldsbase;
  unsigned vw[8], vx[4];
  const char* pw; const char* px;
  unsigned incw, wrapw, wrapx;
  unsigned dw, dx;
  unsigned wa[5], wr;
  __amdgpu_buffer_rsrc_t rs;
  const u16* w2; const u16* wo;
  int first;
  DG(unsigned long long t1;)
};
template <int HP, int ODD>
DEVI void h_issue(K1H& S) {
#pragma unroll
  for (int i = 0; i < 8 - HP; ++i) glds16n(S.vw[i], S.pw, S.ldsbase + K1_W + HP * 8192 + i * 1024 + S.dw);
#pragma unroll
  for (int i = 0; i < 4; ++i) glds16n(S.vx[i], S.px, S.ldsbase + HP * 4096 + i * 1024 + S.dx);
  S.pw += S.incw;
  S.pw -= (unsigned)(size_t)S.pw == S.wrapw ? 18 * S.incw : 0u;
  S.px += ODD ? 128 - 64 * 1280 : 64 * 1280;
  S.px -= (unsigned)(size_t)S.px == S.wrapx ? 1152u : 0u;
  S.dw = S.dw == 4 * WSLOT ? 0 : S.dw + WSLOT;
  S.dx = (S.dx + 8192) & 0xffff;
}
template <int HP, int G> DEVI void h_wait() {
  constexpr int N = G * (12 - HP);
  asm volatile("s_waitcnt vmcnt(%0)" ::"i"(N) : "memory");
}
DEVI void h_read(K1H& S, u32x4 (&wf)[5]) {
  LDSP char* pw = S.lds + S.wr;
#pragma unroll
  for (int c = 0; c < 5; ++c) wf[c] = trread2(pw + S.wa[c], pw + S.wa[c] + 1280);
  S.wr = S.wr == 4 * WSLOT ? 0 : S.wr + WSLOT;
}
template <int HP, int G, int ISSUE>
DEVI void h_step(K1H& S, const u32x4 (&wc)[5], u32x4 (&wn)[5], f32x4 (&cs)[5], const u32x4& ones) {
  if (G >= 0 && G < 9) h_wait<HP, (G >= 0 && G < 9 ? G : 0)>();
  if (G >= 0) STEP_SYNC();
#pragma unroll
  for (int c = 0; c < 5; ++c) cs[c] = mfma(wc[c], ones, cs[c]);
  h_read(S, wn);
  __builtin_amdgcn_sched_barrier(0);
  if (ISSUE) h_issue<HP, ISSUE - 1>(S);
  __builtin_amdgcn_sched_barrier(0);
}
template <int HP>
DEVI void h_main(K1H& S, f32x4 (&cs)[5]) {
  u32x4 wa[5], wb[5];
  u32x4 ones = {0x3f803f80u, 0x3f803f80u, 0x3f803f80u, 0x3f803f80u};
  asm volatile("" : "+v"(ones));
  h_issue<HP, 1>(S);
  h_issue<HP, 0>(S);
  h_issue<HP, 1>(S);
  mb_wait(S.lds, 0);
  h_read(S, wa);
  mb_wait(S.lds, 1);
  h_step<HP, -1, 1>(S, wa, wb, cs, ones);
  h_step<HP, 3, 2>(S, wb, wa, cs, ones);
#pragma unroll 1
  for (int s = 2; s < 14; s += 2) {
    h_step<HP, 3, 1>(S, wa, wb, cs, ones);
    h_step<HP, 3, 2>(S, wb, wa, cs, ones);
  }
  h_step<HP, 3, 1>(S, wa, wb, cs, ones);
  constexpr int n = 12 - HP;
  const int lane = threadIdx.x & 63;
  h_step<HP, 3, 0>(S, wb, wa, cs, ones);
#pragma unroll
  for (int i = 0; i < 5; ++i) st16(S.rs, u32x4{~0u, ~0u, ~0u, ~0u}, lane * 16, OFF_OT + (blockIdx.x * 10 + HP * 5 + i) * 1024);
  asm volatile("s_waitcnt vmcnt(%0)" ::"i"(2 * n + 5) : "memory");
  h_step<HP, 9, 0>(S, wa, wb, cs, ones);
  unsigned rv[6];
#pragma unroll
  for (int i = 0; i < 6; ++i) {
    const int gi = S.first + i, fid = gi < 2960 ? gi : 2959;
    const int f4 = fid >> 2;
    const bool isw2 = f4 < 540;
    const int f2 = isw2 ? f4 : f4 - 540;
    const int cq = isw2 ? f2 / 54 : f2 / 20, ks = f2 - (isw2 ? 54 : 20) * cq;
    const int ct = 4 * cq + (fid & 3);
    const int rho = lane >> 1, e = 4 * ks + ((rho >> 2) & 3), i5 = e / 5, r5 = e - 5 * i5;
    const int lo = 40 * i5 + (r5 == 4 ? 16 : (r5 >> 1) * 20 + (r5 & 1) * 8);
    const int row = isw2 ? 32 * ks + rho : lo + ((rho & 16) ? (r5 == 4 ? 20 : 4) : 0) + (rho & 3);
    rv[i] = row * 1280 + ct * 32 + (lane & 1) * 16;
    asm volatile("" : "+v"(rv[i]));
  }
  asm volatile("s_waitcnt vmcnt(%0)" ::"i"(n + 5) : "memory");
  h_step<HP, 9, 0>(S, wb, wa, cs, ones);
#pragma unroll
  for (int i = 0; i < 6; ++i) glds16n(rv[i], S.first + i < 2160 ? S.w2 : S.wo, S.ldsbase + K1_RT + (HP * 6 + i) * 1024);
  WAIT_VM(11);
  h_step<HP, 9, 0>(S, wa, wb, cs, ones);
#pragma unroll
  for (int c = 0; c < 5; ++c) cs[c] = mfma(wb[c], ones, cs[c]);
  __builtin_amdgcn_s_setprio(0);
}

__global__ __launch_bounds__(512, 2) void hedgehog_k1(const u16* __restrict__ x, const u16* __restrict__ wqkv,
                                                      const u16* __restrict__ wphi, const u16* __restrict__ wo,
                                                      const u16* __restrict__ w1, const u16* __restrict__ b1,
                                                      const u16* __restrict__ w2, char* __restrict__ ws) {
  extern __shared__ __attribute__((aligned(16))) char smem[];
  LDSP char* lds = (LDSP char*)smem;
  const unsigned long long kpc = __builtin_amdgcn_s_getpc();
  const int tid = threadIdx.x, lane = tid & 63;
  const int wave = __builtin_amdgcn_readfirstlane(tid >> 6);
  const int b = blockIdx.x, xcd = b & 7, jx = b >> 3;
  const unsigned ldsbase = (unsigned)(size_t)lds;
  const bool helper = wave >= 6;
  DG(unsigned long long T[8]; T[0] = stamp(); T[7] = rstamp(); T[1] = T[6] = 0;)
  if (helper) {
    const char* pc = (const char*)(kpc & ~(unsigned long long)127) + min(((jx % 3) * 2 + (wave & 1)) * 8192 + lane * 128, CODE_BYTES);
    asm volatile("s_mov_b32 m0, %1\n\ts_nop 0\n\tglobal_load_lds_dword %0, off" ::"v"(pc), "s"(ldsbase + K1_SCR) : "memory");
  }
  if (tid == 0) *(LDSP u32x2*)(lds + K1_MB) = u32x2{0u, 0u};
  WAIT_LGKM0();
  asm volatile("" ::"s"(x), "s"(wqkv), "s"(wphi), "s"(wo), "s"(w1), "s"(b1), "s"(w2), "s"(ws));
  __builtin_amdgcn_s_barrier();
  const int mh = wave & 1;
  const int nj = wave < 4 ? wave >> 1 : 2;
  const int cj = helper ? 2 * mh : nj;
  const int g = lane >> 4, m = lane & 15, q4 = m >> 2, p4 = lane & 3;
  const int rb = 8 * (xcd >> 2) + (jx & 7);
  const int cgi = jx >> 3;
  const bool atype = cgi < 2;
  const int grp = 2 * (xcd & 3) + (cgi & 1);
  const u16* W = atype ? wqkv : w1;
  const int ldw = atype ? 1920 : 1728;
  const int c0 = atype ? 80 * grp : (grp < 6 ? 224 * grp : 1344 + 192 * (grp - 6));
  const int sub = atype ? 640 : (grp < 6 ? 80 : 64);
  const unsigned incw = 64 * ldw;
  const char* xrow = (const char*)x + (size_t)rb * 128 * 1280;
  const int t1 = 1;
  auto wvoff = [&](int id) {
    const int sj = id / 5, ss = id - 5 * sj;
    const int c = 64 * ss + lane, rho = c / 10, cc = c - 10 * rho;
    const int k = (rho & 0x13) | ((rho & 4) << 1) | ((rho & 8) >> 1);
    const bool narrow = !atype && (grp >= 6 || sj == 2);
    return (unsigned)(k * ldw + c0 + sub * sj + 8 * ((narrow && cc >= 8) ? cc - 2 : cc)) * 2;
  };
  auto xvoff = [&](int pc) {
    const int row = pc * 8 + (lane >> 3);
    return (unsigned)(row * 1280 + (((lane & 7) ^ ((row >> 1) & 7)) << 4));
  };
  const unsigned wa0 = K1_W + cj * WSUB + (16 * (g >> 1) + 4 * (g & 1) + q4) * 160 + p4 * (atype ? 40 : 8);
  const unsigned cstr = atype ? 8 : 32;
  const __amdgpu_buffer_rsrc_t rs = __builtin_amdgcn_make_buffer_rsrc((void*)ws, 0, WS_END, 0x00020000);
  f32x4 acc[4][5];

  if (!helper) {
    const unsigned x0 = xvoff(wave), x1 = xvoff(wave < 2 ? wave + 6 : 7);
    auto xgrp = [&](int u, int xt) {
      const char* px = xrow + xt * 128 + (u & 1) * (64 * 1280);
      const unsigned d = ldsbase + u * 8192 + wave * 1024;
      glds16n(x0, px, d);
      if (wave < 2) glds16n(x1, px, d + 6144);
    };
    xgrp(0, 0);
    xgrp(1, 0);
    const unsigned dw = ldsbase + K1_W + wave * 1024;
    const unsigned v0 = wvoff(wave);
    glds16n(v0, W, dw);
    const unsigned v1 = wvoff(wave + 6);
    glds16n(v1, W, dw + 6144);
    const unsigned v2 = wvoff(wave < 3 ? wave + 12 : 14);
    if (wave < 3) glds16n(v2, W, dw + 12288);
    glds16n(v0, (const char*)W + incw, dw + WSLOT);
    glds16n(v1, (const char*)W + incw, dw + WSLOT + 6144);
    if (wave < 3) glds16n(v2, (const char*)W + incw, dw + WSLOT + 12288);
    xgrp(2, t1);
    K1C S;
    S.lds = lds;
    S.xa = (mh * 64 + m) * 128 + ((g ^ (m >> 1)) << 4);
#pragma unroll
    for (int c = 0; c < 5; ++c) S.wa[c] = wa0 + c * cstr;
    S.wr = 0;
    DG(S.t1 = 0;)
    f32x4 cs[3], s1[4], s2[4];
#pragma unroll
    for (int i = 0; i < 4; ++i) {
      s1[i] = f32x4{0.f, 0.f, 0.f, 0.f}; s2[i] = s1[i]; cs[i % 3] = s1[i];
#pragma unroll
      for (int c = 0; c < 5; ++c) acc[i][c] = s1[i];
    }
    if (wave == 4 && lane < 48) *(LDSP u32x4*)(lds + K1_ZERO + lane * 16) = u32x4{0u, 0u, 0u, 0u};
    const bool five = atype || (grp < 6 && nj < 2);
    if (wave < 2) WAIT_VM(5); else if (wave == 2) WAIT_VM(4); else WAIT_VM(3);
    mb_arrive(lds, 0);
    {
      const int col = c0 + sub * (lane / 10) + 8 * (lane % 10);
      const unsigned vo = atype ? tid * 16 : (col > 1720 ? 1720 : col) * 2;
      if (tid < (atype ? 200 : 30)) glds16n(vo, atype ? (const char*)wphi + grp * 3200 : (const char*)b1, ldsbase + K1_WPHI + wave * 1024);
    }
    mb_wait(lds, 0);
    DG(T[1] = stamp();)
    if (nj == 1) {
      if (five) { if (mh == 0) c_main<5, 1>(S, acc, cs, s1, s2); else c_main<5, 2>(S, acc, cs, s1, s2); }
      else { if (mh == 0) c_main<4, 1>(S, acc, cs, s1, s2); else c_main<4, 2>(S, acc, cs, s1, s2); }
      const int cb = mh == 0 ? 0 : (five ? 3 : 2), cn = mh == 0 ? (five ? 3 : 2) : 2;
      if (m == 0) {
#pragma unroll
        for (int c = 0; c < 3; ++c)
          if (c < cn) *(LDSP f32x4*)(lds + K1_CS + (80 + (cb + c) * 16 + 4 * g) * 4) = cs[c];
      }
      if (g == q4) {
#pragma unroll
        for (int r = 0; r < 4; ++r) {
          const float sq = (m & 2) ? ((m & 1) ? s2[r][3] : s2[r][2]) : ((m & 1) ? s2[r][1] : s2[r][0]);
          const float mean = s1[r][0] * (1.0f / 640.0f);
          const float var = fmaxf(sq * (1.0f / 640.0f) - mean * mean, 0.f);
          float rsd = __builtin_amdgcn_rsqf(var + 1e-5f);
          if (!atype) rsd *= L2E;
          *(LDSP f32x2*)(lds + K1_STAT + (mh * 64 + r * 16 + m) * 8) = f32x2{rsd, -mean * rsd};
        }
      }
      if (atype) {
#pragma unroll
        for (int r = 2; r < 4; ++r)
#pragma unroll
          for (int c = 0; c < 5; ++c) *(LDSP f32x4*)(lds + K1_HO + (mh * 2 + r - 2) * 5120 + c * 1024 + lane * 16) = acc[r][c];
      }
    } else {
      if (five) c_main<5, 0>(S, acc, cs, s1, s2); else c_main<4, 0>(S, acc, cs, s1, s2);
      if (!atype) {
#pragma unroll
        for (int c = 0; c < 5; ++c) *(LDSP f32x4*)(lds + K1_HO + (mh * 2 + (nj >> 1)) * 5120 + c * 1024 + lane * 16) = acc[3][c];
        if (nj == 0 && tid < 120) {
          const unsigned w = *(LDSP unsigned*)(lds + K1_WPHI + tid * 4);
          *(LDSP f32x2*)(lds + K1_BL + tid * 8) = f32x2{bflo(w) * L2E, bfhi(w) * L2E};
        }
      }
    }
    DG(T[6] = S.t1;)
  } else {
    __builtin_amdgcn_s_setprio(3);
    K1H S;
    S.lds = lds; S.ldsbase = ldsbase;
    S.incw = incw;
    S.pw = (const char*)W + 2 * t1 * incw;
    S.wrapw = (unsigned)(size_t)W + 20 * incw;
    S.px = xrow + t1 * 128 + 64 * 1280;
    S.wrapx = (unsigned)(size_t)xrow + 1280;
    S.dw = 2 * WSLOT; S.dx = 3 * 8192; S.wr = 0;
    DG(S.t1 = 0;)
#pragma unroll
    for (int i = 0; i < 4; ++i) S.vx[i] = xvoff(4 * mh + i);
#pragma unroll
    for (int i = 0; i < 8; ++i) S.vw[i] = wvoff(8 * mh + i < 15 ? 8 * mh + i : 14);
#pragma unroll
    for (int c = 0; c < 5; ++c) { S.wa[c] = wa0 + c * cstr; acc[0][c] = f32x4{0.f, 0.f, 0.f, 0.f}; }
    S.rs = rs; S.w2 = w2; S.wo = wo; S.first = b * 12 + mh * 6;
    if (mh == 0) h_main<0>(S, acc[0]); else h_main<1>(S, acc[0]);
    DG(T[6] = S.t1;)
    if (m == 0) {
#pragma unroll
      for (int c = 0; c < 5; ++c) *(LDSP f32x4*)(lds + K1_CS + (cj * 80 + c * 16 + 4 * g) * 4) = acc[0][c];
    }
  }
  DG(T[2] = stamp();)
  WAIT_LGKM0();
  __builtin_amdgcn_s_barrier();
  asm volatile("" ::: "memory");
  DG(T[3] = stamp();)
#define DG_OUT1() DG({ T[5] = stamp(); unsigned long long re = rstamp(); if (lane == 0) { unsigned long long* dp = (unsigned long long*)(ws + OFF_DBG) + (size_t)(b * 8 + wave) * 16; for (int i = 0; i < 8; ++i) dp[i] = T[i]; dp[8] = re; } if (lane < 20) ((unsigned*)(ws + OFF_DBG + (2u << 20)))[(b * 8 + wave) * 32 + lane] = *(LDSP unsigned*)(lds + K1_LDS + wave * 128 + lane * 4) - (unsigned)T[0]; })
  auto side_store = [&]() {
    WAIT_VM(0);
    LDSP char* pr = lds + K1_RT + mh * 6144 + (4 * g + q4) * 32 + p4 * 8;
#pragma unroll
    for (int i = 0; i < 6; ++i) {
      const int gi = b * 12 + mh * 6 + i, f4 = gi >> 2;
      const bool isw2 = f4 < 540;
      const int f2 = isw2 ? f4 : f4 - 540;
      const int cq = isw2 ? f2 / 54 : f2 / 20, ks = f2 - (isw2 ? 54 : 20) * cq;
      const u32x4 v = trread2(pr + i * 1024, pr + i * 1024 + 512);
      if (gi < 2960) st16(rs, v, lane * 16, OFF_WT + ((4 * cq + (gi & 3)) * 74 + (isw2 ? 0 : 54) + ks) * 1024);
    }
  };

  if (!atype) {
    auto round = [&](const f32x4 (&a)[5], int ej, int rt) {
      const bool five = grp < 6 && ej < 2;
      const f32x2 st = *(LDSP f32x2*)(lds + K1_STAT + (rt * 16 + m) * 8);
      u32x2 pk[5];
#pragma unroll
      for (int c = 0; c < 5; ++c) {
        if (c == 4 && !five) { pk[4] = u32x2{0u, 0u}; break; }
        const f32x4 cs = *(LDSP f32x4*)(lds + K1_CS + (ej * 80 + c * 16 + 4 * g) * 4);
        const f32x4 bl = *(LDSP f32x4*)(lds + K1_BL + (ej * 80 + c * 16 + 4 * g) * 4);
        float res[4];
#pragma unroll
        for (int e = 0; e < 4; ++e) {
          const float hl = fmaf(a[c][e], st[0], fmaf(st[1], cs[e], bl[e]));
          const float ex = __builtin_amdgcn_exp2f(hl);
          const float rc = __builtin_amdgcn_rcpf(fmaf(ex, ex + 2.f, 2.f));
          res[e] = hl * fmaf(rc, -2.f * LN2, LN2);
        }
        pk[c] = u32x2{pk2(res[0], res[1]), pk2(res[2], res[3])};
      }
      const int T0 = (c0 + sub * ej) >> 4;
      const unsigned base = OFF_ACT + ((rb * 8 + rt) * 54 + (T0 >> 1)) * 1024;
      if (!(T0 & 1)) {
        st16(rs, u32x4{pk[0][0], pk[0][1], pk[1][0], pk[1][1]}, lane * 16, base);
        st16(rs, u32x4{pk[2][0], pk[2][1], pk[3][0], pk[3][1]}, lane * 16, base + 1024);
        if (five) st8(rs, pk[4], lane * 16, base + 2048);
      } else {
        st8(rs, pk[0], lane * 16, base + 8);
        st16(rs, u32x4{pk[1][0], pk[1][1], pk[2][0], pk[2][1]}, lane * 16, base + 1024);
        st16(rs, u32x4{pk[3][0], pk[3][1], pk[4][0], pk[4][1]}, lane * 16, base + 2048);
      }
    };
    if (helper) {
#pragma unroll
      for (int r = 0; r < 2; ++r)
#pragma unroll
        for (int c = 0; c < 5; ++c) acc[r][c] = *(LDSP f32x4*)(lds + K1_HO + (mh * 2 + r) * 5120 + c * 1024 + lane * 16);
    }
#pragma unroll 1
    for (int it = 0; it < (helper ? 1 : 2); ++it) {
      round(acc[0], helper ? 0 : nj, mh * 4 + (helper ? 3 : 2 * it));
      if (it == 0 || nj == 1) round(acc[1], helper ? 2 : nj, mh * 4 + (helper ? 3 : 2 * it + 1));
#pragma unroll
      for (int c = 0; c < 5; ++c) { acc[0][c] = acc[2][c]; acc[1][c] = acc[3][c]; }
    }
    DG(T[4] = stamp();)
    if (helper) side_store();
    DG_OUT1();
    return;
  }

  auto normalise = [&](const f32x4 (&a)[5], const f32x4 (&cs)[5], int rt, u32x4 (&bq)[3], unsigned one) {
    const f32x2 st = *(LDSP f32x2*)(lds + K1_STAT + (rt * 16 + m) * 8);
#pragma unroll
    for (int c = 0; c < 5; ++c) {
      float v[4];
#pragma unroll
      for (int e = 0; e < 4; ++e) v[e] = fmaf(a[c][e], st[0], st[1] * cs[c][e]);
      bq[c >> 1][(c & 1) * 2] = pk2(v[0], v[1]);
      bq[c >> 1][(c & 1) * 2 + 1] = pk2(v[2], v[3]);
    }
    bq[2][2] = one;
    bq[2][3] = 0u;
  };
  f32x4 csv[5];
  if (!helper && nj != 1) {
#pragma unroll
    for (int c = 0; c < 5; ++c) csv[c] = *(LDSP f32x4*)(lds + K1_CS + (nj * 80 + c * 16 + 4 * g) * 4);
#pragma unroll
    for (int r = 0; r < 4; ++r) {
      u32x4 bq[3];
      normalise(acc[r], csv, mh * 4 + r, bq, nj == 2 ? 0x00003f80u : 0u);
      if (nj == 0) {
        const unsigned so = OFF_QT + (4 * grp * 128 + rb * 8 + mh * 4 + r) * 768;
#pragma unroll
        for (int i = 0; i < 3; ++i) st16(rs, bq[i], g * (128 * 768) + m * 16, so + i * 256);
      } else {
#pragma unroll
        for (int i = 0; i < 3; ++i) *(LDSP u32x4*)(lds + K1_VIMG + (mh * 64 + r * 16 + m) * V_PITCH + g * 48 + i * 16) = bq[i];
      }
    }
  }
  if (helper || nj != 2) {
    const int kr0 = helper ? 3 : (nj == 1 ? 0 : 2);
    if (kr0) {
#pragma unroll
      for (int c = 0; c < 5; ++c) acc[0][c] = *(LDSP f32x4*)(lds + K1_HO + (mh * 2 + kr0 - 2) * 5120 + c * 1024 + lane * 16);
    }
#pragma unroll
    for (int c = 0; c < 5; ++c) csv[c] = *(LDSP f32x4*)(lds + K1_CS + (80 + c * 16 + 4 * g) * 4);
    LDSP char* wl = lds + ((p4 == g) ? K1_WPHI + g * 800 + q4 * 40 : K1_ZERO);
#pragma unroll 1
    for (int kr = kr0; kr < (kr0 ? kr0 + 1 : 2); ++kr) {
      u32x4 bq[3];
      normalise(acc[0], csv, mh * 4 + kr, bq, 0u);
      f32x4 pk[5];
      head_proj(wl, bq, pk);
      unsigned ph[20];
      feature_map(pk, ph);
      LDSP char* dst = lds + (mh * 64 + kr * 16 + m) * PK_PITCH + g * 80;
#pragma unroll
      for (int i = 0; i < 5; ++i) *(LDSP u32x4*)(dst + i * 16) = u32x4{ph[4 * i], ph[4 * i + 1], ph[4 * i + 2], ph[4 * i + 3]};
#pragma unroll
      for (int c = 0; c < 5; ++c) acc[0][c] = acc[1][c];
    }
  }
  DG(T[4] = stamp();)
  WAIT_LGKM0();
  __builtin_amdgcn_s_barrier();
  asm volatile("" ::: "memory");
  if (!helper) {
#pragma unroll 1
    for (int job = wave; job < 12; job += 6) {
      const int hh = job & 3, ft = job >> 2;
      LDSP char* pa = lds + (4 * g + q4) * PK_PITCH + hh * 80 + ft * 32 + p4 * 8;
      LDSP char* pb = lds + K1_VIMG + (4 * g + q4) * V_PITCH + hh * 48 + p4 * 8;
      f32x4 d0 = {0.f, 0.f, 0.f, 0.f}, d1 = {0.f, 0.f, 0.f, 0.f};
#pragma unroll
      for (int kk = 0; kk < 4; ++kk) {
        u32x4 a = trread2(pa + (32 * kk) * PK_PITCH, pa + (32 * kk + 16) * PK_PITCH);
        u32x4 v0 = trread2(pb + (32 * kk) * V_PITCH, pb + (32 * kk + 16) * V_PITCH);
        u32x4 v1 = trread2(pb + (32 * kk) * V_PITCH + 32, pb + (32 * kk + 16) * V_PITCH + 32);
        d0 = mfma(a, v0, d0);
        d1 = mfma(a, v1, d1);
      }
      const unsigned so = OFF_KVP + ((4 * grp + hh) * 16 + rb) * KVP_B + 1344 * ft;
      st16(rs, __builtin_bit_cast(u32x4, d0), m * 64 + g * 16, so);
      if (m < 5) st16(rs, __builtin_bit_cast(u32x4, d1), (16 + m) * 64 + g * 16, so);
    }
  } else {
    side_store();
  }
  DG_OUT1();
}

constexpr unsigned K2_YD = 0;
constexpr unsigned K2_SD = 4 * 15360;
constexpr unsigned K2_KVT = K2_SD + 4 * 20480;
constexpr unsigned K2_ZERO = K2_KVT + 1920;
constexpr unsigned K2_WPHI = K2_ZERO + 1792;
constexpr unsigned K2_FLAG = K2_WPHI + 3200;
constexpr unsigned K2_LDS = K2_FLAG + 16;

DEVI u32x4 bld(__amdgpu_buffer_rsrc_t rs, unsigned voff, unsigned soff) {
  return __builtin_amdgcn_raw_buffer_load_b128(rs, voff, soff, 0);
}
DEVI u32x4 bld_sc1(__amdgpu_buffer_rsrc_t rs, unsigned voff, unsigned soff) {
  return __builtin_amdgcn_raw_buffer_load_b128(rs, voff, soff, 16);
}

__global__ __launch_bounds__(512, 2) void hedgehog_k2(const u16* __restrict__ x, const u16* __restrict__ wphi,
                                                      char* __restrict__ ws, u16* __restrict__ out) {
  extern __shared__ __attribute__((aligned(16))) char smem[];
  LDSP char* lds = (LDSP char*)smem;
  const int tid = threadIdx.x, lane = tid & 63;
  const int wave = __builtin_amdgcn_readfirstlane(tid >> 6);
  const int g = lane >> 4, m = lane & 15, q4 = m >> 2, p4 = lane & 3;
  const int b = blockIdx.x, xcd = b & 7, jx = b >> 3;
  const int rb2 = 8 * (xcd >> 1) + (jx & 7), cb = 4 * (xcd & 1) + (jx >> 3);
  const __amdgpu_buffer_rsrc_t rs = __builtin_amdgcn_make_buffer_rsrc((void*)ws, 0, WS_END, 0x00020000);
  const unsigned l16 = lane * 16;
  DG(unsigned long long T[8]; T[0] = stamp(); T[7] = rstamp(); T[1] = T[2] = T[3] = T[4] = T[5] = T[6] = 0;)
#define DG_OUT2() DG({ unsigned long long re = rstamp(); if (lane == 0) { unsigned long long* dp = (unsigned long long*)(ws + OFF_DBG + (1u << 20)) + (size_t)(b * 8 + wave) * 16; for (int i = 0; i < 8; ++i) dp[i] = T[i]; dp[8] = re; } })
  f32x4 acc[4][5];
#pragma unroll
  for (int r = 0; r < 4; ++r)
#pragma unroll
    for (int c = 0; c < 5; ++c) acc[r][c] = f32x4{0.f, 0.f, 0.f, 0.f};

  if (wave < 4) {
    const int ks0 = wave < 2 ? 14 * wave : 28 + 13 * (wave - 2);
    const bool n14 = wave < 2;
    unsigned ab[4], bb[5];
#pragma unroll
    for (int l = 0; l < 4; ++l) ab[l] = OFF_ACT + ((4 * rb2 + ((l + wave) & 3)) * 54 + ks0) * 1024;
#pragma unroll
    for (int c = 0; c < 5; ++c) bb[c] = OFF_WT + ((5 * cb + c) * 74 + ks0) * 1024;
    u32x4 A[3][4], B[3][5];
    auto load = [&](int i) {
#pragma unroll
      for (int l = 0; l < 4; ++l) A[i % 3][l] = bld(rs, l16, ab[l] + i * 1024);
#pragma unroll
      for (int c = 0; c < 5; ++c) B[i % 3][c] = bld(rs, l16, bb[c] + i * 1024);
    };
    auto comp = [&](int i) {
#pragma unroll
      for (int l = 0; l < 4; ++l)
#pragma unroll
        for (int c = 0; c < 5; ++c) acc[l][c] = mfma(B[i % 3][c], A[i % 3][l], acc[l][c]);
    };
    load(0);
    load(1);
    u32x2 xv[5];
    const size_t orow = (size_t)(64 * rb2 + 16 * wave + m) * 640 + 80 * cb + 4 * g;
#pragma unroll
    for (int c = 0; c < 5; ++c) xv[c] = *(const u32x2*)(x + orow + 16 * c);
    __builtin_amdgcn_s_barrier();
    DG(T[1] = stamp();)
#pragma unroll
    for (int i = 0; i < 13; ++i) {
      if (i + 2 < 13) load(i + 2);
      else if (i + 2 == 13) { if (n14) load(13); }
      __builtin_amdgcn_sched_barrier(0);
      comp(i);
      __builtin_amdgcn_sched_barrier(0);
      DG(if (i == 0) T[2] = stamp();)
    }
    if (n14) comp(13);
    DG(T[3] = stamp();)
#pragma unroll
    for (int l = 1; l < 4; ++l)
#pragma unroll
      for (int c = 0; c < 5; ++c)
        *(LDSP f32x4*)(lds + K2_YD + wave * 15360 + ((l - 1) * 5 + c) * 1024 + l16) = acc[l][c];
    DG(T[4] = stamp();)
    __syncthreads();
    DG(T[5] = stamp();)
#pragma unroll
    for (int c = 0; c < 5; ++c) {
      f32x4 y = acc[0][c];
#pragma unroll
      for (int l = 1; l < 4; ++l)
        y += *(LDSP f32x4*)(lds + K2_YD + ((wave - l) & 3) * 15360 + ((l - 1) * 5 + c) * 1024 + l16);
      f32x4 s = *(LDSP f32x4*)(lds + K2_SD + (wave * 5 + c) * 1024 + l16);
#pragma unroll
      for (int a = 1; a < 4; ++a) s += *(LDSP f32x4*)(lds + K2_SD + a * 20480 + (wave * 5 + c) * 1024 + l16);
      float o[4];
#pragma unroll
      for (int e = 0; e < 4; ++e) {
        const float gate = y[e] * __builtin_amdgcn_rcpf(1.f + __builtin_amdgcn_exp2f(-y[e] * L2E));
        const unsigned xw = xv[c][e >> 1];
        o[e] = fmaf(s[e], gate, (e & 1) ? bfhi(xw) : bflo(xw));
      }
      *(u32x2*)(out + orow + 16 * c) = u32x2{pk2(o[0], o[1]), pk2(o[2], o[3])};
    }
    DG(T[6] = stamp();)
    DG_OUT2();
    return;
  }

  const int aw = wave - 4;
  const int h = 4 * xcd + (jx & 3), sb = jx >> 2;
  const int tok = sb * 256 + aw * 64 + lane;
  if (aw == 0) {
    *(LDSP u32x4*)(lds + K2_ZERO + l16) = u32x4{0u, 0u, 0u, 0u};
    if (lane < 48) *(LDSP u32x4*)(lds + K2_ZERO + 1024 + l16) = u32x4{0u, 0u, 0u, 0u};
    if (lane == 0) *(LDSP u32x4*)(lds + K2_FLAG) = u32x4{0u, 0u, 0u, 0u};
  }
  u32x4 wv = *(const u32x4*)(wphi + h * 400 + (lane < 50 ? lane : 49) * 8);
  u32x4 bq[3];
#pragma unroll
  for (int s = 0; s < 3; ++s) bq[s] = bld(rs, (unsigned)(tok >> 4) * 768 + m * 16, OFF_QT + h * (128 * 768) + s * 256);
  int item = aw * 53 + (lane < 53 ? lane : 52);
  item = item < 210 ? item : 209;
  const int d = item / 10, fq = item - 10 * d;
  u32x4 part[16];
#pragma unroll
  for (int r = 0; r < 16; ++r) part[r] = bld(rs, (unsigned)((fq >> 2) * 1344 + d * 64 + (fq & 3) * 16), OFF_KVP + (h * 16 + r) * KVP_B);
  WAIT_LGKM0();
  __builtin_amdgcn_s_barrier();
  __builtin_amdgcn_s_setprio(2);
  *(LDSP u32x4*)(lds + K2_WPHI + aw * 800 + (lane < 50 ? lane : 49) * 16) = wv;
  unsigned ph[20];
  {
    LDSP char* wl = lds + ((p4 == g) ? K2_WPHI + aw * 800 + q4 * 40 : K2_ZERO);
    f32x4 pq[5];
    head_proj(wl, bq, pq);
    feature_map(pq, ph);
#pragma unroll
    for (int i = 0; i < 20; ++i) asm volatile("" : "+v"(ph[i]));
  }
  DG(T[1] = stamp();)
  {
    f32x4 sum = __builtin_bit_cast(f32x4, part[0]);
#pragma unroll
    for (int r = 1; r < 16; ++r) sum += __builtin_bit_cast(f32x4, part[r]);
    *(LDSP u32x2*)(lds + K2_KVT + d * 80 + fq * 8) = u32x2{pk2(sum[0], sum[1]), pk2(sum[2], sum[3])};
    WAIT_LGKM0();
    if (lane == 0) *(volatile LDSP unsigned*)(lds + K2_FLAG + aw * 4) = 1u;
    for (int spin = 0; spin < (1 << 22); ++spin) {
      const u32x4 f = *(volatile LDSP u32x4*)(lds + K2_FLAG);
      if (__builtin_amdgcn_readfirstlane(f[0] & f[1] & f[2] & f[3])) break;
      __builtin_amdgcn_s_sleep(1);
    }
    asm volatile("" ::: "memory");
  }
  DG(T[2] = stamp();)
  const int ks0 = 5 * aw;
  u32x4 B[5][5];
#pragma unroll
  for (int i = 0; i < 2; ++i)
#pragma unroll
    for (int c = 0; c < 5; ++c) B[i][c] = bld(rs, l16, OFF_WT + ((5 * cb + c) * 74 + 54 + ks0 + i) * 1024);
  {
    LDSP char* kl = lds + (((m >> 2) == g) ? K2_KVT + (m & 3) * 80 : K2_ZERO);
    f32x4 nd[6];
#pragma unroll
    for (int t = 0; t < 6; ++t) {
      f32x4 c = {0.f, 0.f, 0.f, 0.f};
#pragma unroll
      for (int s = 0; s < 5; ++s)
        c = mfma(*(LDSP u32x4*)(kl + t * 320 + s * 16), u32x4{ph[4 * s], ph[4 * s + 1], ph[4 * s + 2], ph[4 * s + 3]}, c);
      nd[t] = c;
    }
    const float rd = __builtin_amdgcn_rcpf(nd[5][0] + 1e-6f);
    unsigned ow[10];
#pragma unroll
    for (int t = 0; t < 5; ++t) {
      ow[2 * t] = min(pk2(nd[t][0] * rd, nd[t][1] * rd), 0xFFFFFFFEu);
      ow[2 * t + 1] = min(pk2(nd[t][2] * rd, nd[t][3] * rd), 0xFFFFFFFEu);
    }
    const int e0 = 5 * (h >> 1) + 2 * (h & 1), e1 = e0 + 1, e4 = 5 * (h >> 1) + 4;
    const unsigned vo = (unsigned)(tok >> 4) * 20480 + m * 16;
    st16(rs, u32x4{ow[0], ow[1], ow[2], ow[3]}, vo, OFF_OT + (e0 >> 2) * 1024 + (e0 & 3) * 256);
    st16(rs, u32x4{ow[4], ow[5], ow[6], ow[7]}, vo, OFF_OT + (e1 >> 2) * 1024 + (e1 & 3) * 256);
    st8(rs, u32x2{ow[8], ow[9]}, vo, OFF_OT + (e4 >> 2) * 1024 + (e4 & 3) * 256 + (h & 1) * 8);
  }
  DG(T[3] = stamp();)
  const unsigned ob = OFF_OT + (4 * rb2 * 20 + ks0) * 1024;
  {
    const int l2 = lane < 40 ? lane : 39;
    const unsigned pv = 3 * 20480 + (l2 >> 3) * 1024 + ((l2 >> 1) & 3) * 256 + 15 * 16 + (l2 & 1) * 8;
    for (int spin = 0; spin < (1 << 20); ++spin) {
      asm volatile("" ::: "memory");
      const unsigned w = __builtin_amdgcn_raw_buffer_load_b32(rs, pv, ob, 16);
      if (!__any(w == 0xFFFFFFFFu)) break;
      __builtin_amdgcn_s_sleep(4);
    }
  }
  DG(T[4] = stamp();)
  u32x4 A[5][4];
  for (int spin = 0; spin < (1 << 16); ++spin) {
    asm volatile("" ::: "memory");
    unsigned mx = 0;
#pragma unroll
    for (int i = 0; i < 5; ++i)
#pragma unroll
      for (int l = 0; l < 4; ++l) A[i][l] = bld_sc1(rs, l16, ob + l * 20480 + i * 1024);
#pragma unroll
    for (int i = 0; i < 5; ++i)
#pragma unroll
      for (int l = 0; l < 4; ++l) mx = max(max(mx, max(A[i][l][0], A[i][l][1])), max(A[i][l][2], A[i][l][3]));
    if (!__any(mx == 0xFFFFFFFFu)) break;
    __builtin_amdgcn_s_sleep(4);
  }
  DG(T[5] = stamp();)
#pragma unroll
  for (int i = 0; i < 5; ++i) {
    if (i + 2 < 5) {
#pragma unroll
      for (int c = 0; c < 5; ++c) B[i + 2][c] = bld(rs, l16, OFF_WT + ((5 * cb + c) * 74 + 54 + ks0 + i + 2) * 1024);
    }
    __builtin_amdgcn_sched_barrier(0);
#pragma unroll
    for (int l = 0; l < 4; ++l)
#pragma unroll
      for (int c = 0; c < 5; ++c) acc[l][c] = mfma(B[i][c], A[i][l], acc[l][c]);
    __builtin_amdgcn_sched_barrier(0);
  }
#pragma unroll
  for (int l = 0; l < 4; ++l)
#pragma unroll
    for (int c = 0; c < 5; ++c) *(LDSP f32x4*)(lds + K2_SD + aw * 20480 + (l * 5 + c) * 1024 + l16) = acc[l][c];
  DG(T[6] = stamp();)
  __syncthreads();
  DG_OUT2();
}

extern "C" void kernel_launch(void* const* d_in, const int* in_sizes, int n_in, void* d_out, int out_size, void* d_ws,
                              size_t ws_size, hipStream_t stream) {
  const u16* x = (const u16*)d_in[0];
  const u16* wqkv = (const u16*)d_in[1];
  const u16* wphi = (const u16*)d_in[2];
  const u16* wo = (const u16*)d_in[3];
  const u16* w1 = (const u16*)d_in[5];
  const u16* b1 = (const u16*)d_in[6];
  const u16* w2 = (const u16*)d_in[7];
  (void)hipFuncSetAttribute((const void*)hedgehog_k1, hipFuncAttributeMaxDynamicSharedMemorySize, K1_LDS DG(+ 1024));
  (void)hipFuncSetAttribute((const void*)hedgehog_k2, hipFuncAttributeMaxDynamicSharedMemorySize, K2_LDS);
  hedgehog_k1<<<256, 512, K1_LDS DG(+ 1024), stream>>>(x, wqkv, wphi, wo, w1, b1, w2, (char*)d_ws);
  hedgehog_k2<<<256, 512, K2_LDS, stream>>>(x, wphi, (char*)d_ws, (u16*)d_out);
}
```

```cpp
#define ROT3
#include <hip/hip_runtime.h>
#include <stdint.h>

typedef unsigned short u16;
typedef __attribute__((ext_vector_type(2))) __bf16 bf16x2;
typedef __attribute__((ext_vector_type(8))) __bf16 bf16x8;
typedef __attribute__((ext_vector_type(4))) short s16x4;
typedef __attribute__((ext_vector_type(2))) float f32x2;
typedef __attribute__((ext_vector_type(4))) float f32x4;
typedef __attribute__((ext_vector_type(2))) unsigned u32x2;
typedef __attribute__((ext_vector_type(4))) unsigned u32x4;
#define LDSP __attribute__((address_space(3)))
#define DEVI __device__ __forceinline__

constexpr unsigned OFF_ACT = 0;
constexpr unsigned OFF_WT = OFF_ACT + 128u * 54u * 1024u;
constexpr unsigned OFF_OT = OFF_WT + 40u * 74u * 1024u;
constexpr unsigned OFF_QT = OFF_OT + 128u * 20u * 1024u;
constexpr unsigned OFF_KVP = OFF_QT + 32u * 2048u * 48u;
constexpr unsigned KVP_B = 3 * 1344;
constexpr unsigned OFF_DUMMY = OFF_KVP + 32u * 16u * KVP_B;
constexpr unsigned WS_END = OFF_DUMMY + 640u * 1024u;

#define CODE_BYTES (46 * 1024)
#define CODE2_OFF (36 * 1024)
#define K2_NY 11
#define K1_MAP 1
constexpr float L2E = 1.4426950408889634f, LN2 = 0.6931471805599453f;

DEVI unsigned pk2(float a, float b) {
  bf16x2 v = {(__bf16)a, (__bf16)b};
  return __builtin_bit_cast(unsigned, v);
}
DEVI float bflo(unsigned u) { return __uint_as_float(u << 16); }
DEVI float bfhi(unsigned u) { return __uint_as_float(u & 0xffff0000u); }
DEVI f32x4 mfma(u32x4 a, u32x4 b, f32x4 c) {
  return __builtin_amdgcn_mfma_f32_16x16x32_bf16(__builtin_bit_cast(bf16x8, a), __builtin_bit_cast(bf16x8, b), c, 0, 0, 0);
}
DEVI u32x2 trread(LDSP char* p) {
  return __builtin_bit_cast(u32x2, __builtin_amdgcn_ds_read_tr16_b64_v4i16((LDSP s16x4*)p));
}
DEVI u32x4 trread2(LDSP char* p0, LDSP char* p1) {
  u32x2 a = trread(p0), b = trread(p1);
  return u32x4{a[0], a[1], b[0], b[1]};
}
DEVI float dot2(unsigned a, unsigned b, float c) {
  return __builtin_amdgcn_fdot2_f32_bf16(__builtin_bit_cast(bf16x2, a), __builtin_bit_cast(bf16x2, b), c, false);
}
DEVI float dot_ones(u32x4 a, float c) {
  c = dot2(a[0], 0x3f803f80u, c); c = dot2(a[1], 0x3f803f80u, c);
  c = dot2(a[2], 0x3f803f80u, c); c = dot2(a[3], 0x3f803f80u, c);
  return c;
}
DEVI float dot_self(u32x4 a, float c) {
  c = dot2(a[0], a[0], c); c = dot2(a[1], a[1], c); c = dot2(a[2], a[2], c); c = dot2(a[3], a[3], c);
  return c;
}
DEVI float red_g(float v) {
  v += __shfl_xor(v, 16);
  v += __shfl_xor(v, 32);
  return v;
}
DEVI void glds16(unsigned voff, const void* sbase, unsigned lds_dst) {
  unsigned keep;
  asm volatile("s_mov_b32 %0, m0\n\ts_mov_b32 m0, %3\n\ts_nop 0\n\tglobal_load_lds_dwordx4 %1, %2\n\ts_mov_b32 m0, %0"
               : "=&s"(keep) : "v"(voff), "s"(sbase), "s"(lds_dst) : "memory");
}
#define DG(...)
#define WAIT_VM(n) asm volatile("s_waitcnt vmcnt(" #n ")" ::: "memory")
#define WAIT_LGKM0() asm volatile("s_waitcnt lgkmcnt(0)" ::: "memory")

DEVI void feature_map(const f32x4 (&p)[5], unsigned (&o)[20]) {
  float mx = p[0][0], mn = p[0][0];
#pragma unroll
  for (int t = 0; t < 5; ++t)
#pragma unroll
    for (int r = 0; r < 4; ++r) { mx = fmaxf(mx, p[t][r]); mn = fminf(mn, p[t][r]); }
  const float a = -mx * L2E, b = mn * L2E;
  float e1[20], e2[20], s1 = 0.f, s2 = 0.f;
#pragma unroll
  for (int t = 0; t < 5; ++t)
#pragma unroll
    for (int r = 0; r < 4; ++r) {
      e1[4 * t + r] = __builtin_amdgcn_exp2f(fmaf(p[t][r], L2E, a));
      e2[4 * t + r] = __builtin_amdgcn_exp2f(fmaf(p[t][r], -L2E, b));
      s1 += e1[4 * t + r]; s2 += e2[4 * t + r];
    }
  const float r1 = __builtin_amdgcn_rcpf(s1), r2 = __builtin_amdgcn_rcpf(s2);
#pragma unroll
  for (int i = 0; i < 10; ++i) {
    o[i] = pk2(e1[2 * i] * r1, e1[2 * i + 1] * r1);
    o[10 + i] = pk2(e2[2 * i] * r2, e2[2 * i + 1] * r2);
  }
}
DEVI void head_proj(LDSP char* wl, const u32x4 (&b)[3], f32x4 (&p)[5]) {
#pragma unroll
  for (int t = 0; t < 5; ++t) {
    u32x4 a0 = trread2(wl + 0 * 40 + t * 8, wl + 4 * 40 + t * 8);
    u32x4 a1 = trread2(wl + 8 * 40 + t * 8, wl + 12 * 40 + t * 8);
    u32x2 h2 = trread(wl + 16 * 40 + t * 8);
    u32x4 a2 = {h2[0], h2[1], 0u, 0u};
    f32x4 c = {0.f, 0.f, 0.f, 0.f};
    c = mfma(a0, b[0], c);
    c = mfma(a1, b[1], c);
    c = mfma(a2, b[2], c);
    p[t] = c;
  }
}

constexpr unsigned XSLOT = 16384, WSUB = 5120, WSLOT = 3 * WSUB;
constexpr unsigned K1_W = 4 * XSLOT;
constexpr unsigned K1_WPHI = K1_W + 5 * WSLOT;
constexpr unsigned K1_ZERO = K1_WPHI + 3200;
constexpr unsigned K1_STAT = K1_ZERO + 768;
constexpr unsigned K1_CS = K1_STAT + 1024;
constexpr unsigned K1_BL = K1_CS + 960;
constexpr unsigned K1_SCR = K1_BL + 960;
constexpr unsigned K1_MB = K1_SCR + 256;
constexpr unsigned K1_LDS = K1_MB + 16;
constexpr unsigned PK_PITCH = 352, V_PITCH = 224, K1_VIMG = 128 * PK_PITCH;
constexpr unsigned K1_HO = K1_VIMG + 128 * V_PITCH;
constexpr unsigned K1_RT = K1_W + 2 * WSLOT;

DEVI void glds16n(unsigned voff, const void* sbase, unsigned lds_dst) {
  asm volatile("s_mov_b32 m0, %2\n\ts_nop 0\n\tglobal_load_lds_dwordx4 %0, %1" ::"v"(voff), "s"(sbase), "s"(lds_dst) : "memory");
}
DEVI void st16(__amdgpu_buffer_rsrc_t rs, u32x4 v, unsigned voff, unsigned soff) {
  __builtin_amdgcn_raw_buffer_store_b128(v, rs, voff + soff, 0, 16);
}
DEVI void st8(__amdgpu_buffer_rsrc_t rs, u32x2 v, unsigned voff, unsigned soff) {
  __builtin_amdgcn_raw_buffer_store_b64(v, rs, voff + soff, 0, 16);
}
#define STEP_SYNC() do { __builtin_amdgcn_s_waitcnt(0xC07F); __builtin_amdgcn_s_barrier(); __builtin_amdgcn_sched_barrier(0); } while (0)

DEVI void mb_arrive(LDSP char* lds, int i) {
  WAIT_LGKM0();
  if ((threadIdx.x & 63) == 0) __hip_atomic_fetch_add((LDSP unsigned*)(lds + K1_MB + 4 * i), 1u, __ATOMIC_RELAXED, __HIP_MEMORY_SCOPE_WORKGROUP);
}
DEVI void mb_wait(LDSP char* lds, int i) {
  for (int spin = 0; spin < (1 << 22); ++spin) {
    if (__builtin_amdgcn_readfirstlane(*(volatile LDSP unsigned*)(lds + K1_MB + 4 * i)) >= 6u) break;
    __builtin_amdgcn_s_sleep(1);
  }
  asm volatile("" ::: "memory");
  __builtin_amdgcn_sched_barrier(0);
}
struct K1C {
  LDSP char* lds;
  unsigned xa, wa[5], wr;
  DG(unsigned long long t1;)
};
template <int NC>
DEVI void c_read(K1C& S, int s, u32x4 (&xf)[4], u32x4 (&wf)[5]) {
  LDSP char* px = S.lds + ((s >> 1) & 3) * XSLOT + (S.xa ^ ((s & 1) << 6));
  LDSP char* pw = S.lds + S.wr;
  xf[0] = *(LDSP u32x4*)(px);
  wf[0] = trread2(pw + S.wa[0], pw + S.wa[0] + 1280);
#pragma unroll
  for (int r = 1; r < 4; ++r) xf[r] = *(LDSP u32x4*)(px + r * 2048);
#pragma unroll
  for (int c = 1; c < NC; ++c) wf[c] = trread2(pw + S.wa[c], pw + S.wa[c] + 1280);
  S.wr = S.wr == 4 * WSLOT ? 0 : S.wr + WSLOT;
}
template <int NC, int ROLE, bool READ, bool MINI = false>
DEVI void c_step(K1C& S, int s, const u32x4 (&xc)[4], const u32x4 (&wc)[5], u32x4 (&xn)[4], u32x4 (&wn)[5], f32x4 (&acc)[4][5],
                 f32x4 (&cs)[3], f32x4 (&s1)[4], f32x4 (&s2)[4], const u32x4& ones) {
  if (READ) {
    if (MINI) { WAIT_VM(0); mb_arrive(S.lds, 1); mb_wait(S.lds, 1); } else STEP_SYNC();
    c_read<NC>(S, s + 1, xn, wn);
  }
#pragma unroll
  for (int c = 0; c < NC; ++c)
#pragma unroll
    for (int r = 0; r < 4; ++r) acc[r][c] = mfma(wc[c], xc[r], acc[r][c]);
  if (ROLE) {
    constexpr int C0 = ROLE == 1 ? 0 : NC - 2, C1 = ROLE == 1 ? NC - 2 : NC;
#pragma unroll
    for (int r = 0; r < 4; ++r) {
      s1[r] = mfma(ones, xc[r], s1[r]);
      s2[r] = mfma(xc[r], xc[r], s2[r]);
    }
#pragma unroll
    for (int c = C0; c < C1; ++c) cs[c - C0] = mfma(wc[c], ones, cs[c - C0]);
  }
  if (READ) {
#pragma unroll
    for (int i = 0; i < 4 + 2 * NC; ++i) {
      __builtin_amdgcn_sched_group_barrier(0x8, 1, 0);
      __builtin_amdgcn_sched_group_barrier(0x100, 1, 0);
    }
  }
  __builtin_amdgcn_sched_barrier(0);
}
template <int NC, int ROLE>
DEVI void c_main(K1C& S, f32x4 (&acc)[4][5], f32x4 (&cs)[3], f32x4 (&s1)[4], f32x4 (&s2)[4]) {
  u32x4 xa[4], wa[5], xb[4], wb[5];
  u32x4 ones = {0x3f803f80u, 0x3f803f80u, 0x3f803f80u, 0x3f803f80u};
  asm volatile("" : "+v"(ones));
  c_read<NC>(S, 0, xa, wa);
  c_step<NC, ROLE, true, true>(S, 0, xa, wa, xb, wb, acc, cs, s1, s2, ones);
#pragma unroll 1
  for (int s = 1; s < 19; s += 2) {
    c_step<NC, ROLE, true>(S, s, xb, wb, xa, wa, acc, cs, s1, s2, ones);
    c_step<NC, ROLE, true>(S, s + 1, xa, wa, xb, wb, acc, cs, s1, s2, ones);
  }
  c_step<NC, ROLE, false>(S, 19, xb, wb, xa, wa, acc, cs, s1, s2, ones);
}

struct K1H {
  LDSP char* lds;
  unsigned ldsbase;
  unsigned vw[8], vx[4];
  const char* pw; const char* px;
  unsigned incw, wrapw, wrapx;
  unsigned dw, dx;
  unsigned wa[5], wr;
  __amdgpu_buffer_rsrc_t rs;
  const u16* w2; const u16* wo;
  int first;
  const char* pc2;
  DG(unsigned long long t1;)
};
template <int HP, int ODD>
DEVI void h_issue(K1H& S) {
#pragma unroll
  for (int i = 0; i < 8 - HP; ++i) glds16n(S.vw[i], S.pw, S.ldsbase + K1_W + HP * 8192 + i * 1024 + S.dw);
#pragma unroll
  for (int i = 0; i < 4; ++i) glds16n(S.vx[i], S.px, S.ldsbase + HP * 4096 + i * 1024 + S.dx);
  S.pw += S.incw;
  S.pw -= (unsigned)(size_t)S.pw == S.wrapw ? 18 * S.incw : 0u;
  S.px += ODD ? 128 - 64 * 1280 : 64 * 1280;
  S.px -= (unsigned)(size_t)S.px == S.wrapx ? 1152u : 0u;
  S.dw = S.dw == 4 * WSLOT ? 0 : S.dw + WSLOT;
  S.dx = (S.dx + 8192) & 0xffff;
}
template <int HP, int G> DEVI void h_wait() {
  constexpr int N = G * (12 - HP);
  asm volatile("s_waitcnt vmcnt(%0)" ::"i"(N) : "memory");
}
DEVI void h_read(K1H& S, u32x4 (&wf)[5]) {
  LDSP char* pw = S.lds + S.wr;
#pragma unroll
  for (int c = 0; c < 5; ++c) wf[c] = trread2(pw + S.wa[c], pw + S.wa[c] + 1280);
  S.wr = S.wr == 4 * WSLOT ? 0 : S.wr + WSLOT;
}
template <int HP, int G, int ISSUE>
DEVI void h_step(K1H& S, const u32x4 (&wc)[5], u32x4 (&wn)[5], f32x4 (&cs)[5], const u32x4& ones) {
  if (G >= 0 && G < 9) h_wait<HP, (G >= 0 && G < 9 ? G : 0)>();
  if (G >= 0) STEP_SYNC();
#pragma unroll
  for (int c = 0; c < 5; ++c) cs[c] = mfma(wc[c], ones, cs[c]);
  h_read(S, wn);
  __builtin_amdgcn_sched_barrier(0);
  if (ISSUE) h_issue<HP, ISSUE - 1>(S);
  __builtin_amdgcn_sched_barrier(0);
}
template <int HP>
DEVI void h_main(K1H& S, f32x4 (&cs)[5]) {
  u32x4 wa[5], wb[5];
  u32x4 ones = {0x3f803f80u, 0x3f803f80u, 0x3f803f80u, 0x3f803f80u};
  asm volatile("" : "+v"(ones));
  h_issue<HP, 1>(S);
  h_issue<HP, 0>(S);
  h_issue<HP, 1>(S);
  mb_wait(S.lds, 0);
  h_read(S, wa);
  mb_wait(S.lds, 1);
  h_step<HP, -1, 1>(S, wa, wb, cs, ones);
  h_step<HP, 3, 2>(S, wb, wa, cs, ones);
#pragma unroll 1
  for (int s = 2; s < 14; s += 2) {
    h_step<HP, 3, 1>(S, wa, wb, cs, ones);
    h_step<HP, 3, 2>(S, wb, wa, cs, ones);
  }
  h_step<HP, 3, 1>(S, wa, wb, cs, ones);
  constexpr int n = 12 - HP;
  const int lane = threadIdx.x & 63;
  h_step<HP, 3, 0>(S, wb, wa, cs, ones);
  if (S.pc2) {
    asm volatile("s_mov_b32 m0, %1\n\ts_nop 0\n\tglobal_load_lds_dword %0, off" ::"v"(S.pc2 + HP * 8192 + lane * 128), "s"(S.ldsbase + K1_SCR) : "memory");
  }
#pragma unroll
  for (int i = 0; i < 5; ++i) st16(S.rs, u32x4{~0u, ~0u, ~0u, ~0u}, lane * 16, OFF_OT + (blockIdx.x * 10 + HP * 5 + i) * 1024);
  asm volatile("s_waitcnt vmcnt(%0)" ::"i"(2 * n + 5) : "memory");
  h_step<HP, 9, 0>(S, wa, wb, cs, ones);
  asm volatile("s_waitcnt vmcnt(%0)" ::"i"(n + 5) : "memory");
  h_step<HP, 9, 0>(S, wb, wa, cs, ones);
  {
    const int rho = lane >> 1;
    const unsigned v2 = rho * 1280 + (lane & 1) * 16;
    const unsigned vo = (((rho & 12) << 1) | ((rho & 16) >> 2) | (rho & 3)) * 1280 + (lane & 1) * 16;
#pragma unroll
    for (int i = 0; i < 6; ++i) {
      const int gi = S.first + i, fid = gi < 2960 ? gi : 2959;
      const int cq = (fid >> 2) / 74, ks = (fid >> 2) - 74 * cq;
      const char* src = (ks < 54 ? (const char*)S.w2 + ks * 40960 : (const char*)S.wo + (ks - 54) * 40960) + (4 * cq + (fid & 3)) * 32;
      glds16n(ks < 54 ? v2 : vo, src, S.ldsbase + K1_RT + (HP * 6 + i) * 1024);
    }
  }
  WAIT_VM(11);
  h_step<HP, 9, 0>(S, wa, wb, cs, ones);
#pragma unroll
  for (int c = 0; c < 5; ++c) cs[c] = mfma(wb[c], ones, cs[c]);
  __builtin_amdgcn_s_setprio(0);
}

__global__ __launch_bounds__(512, 2) void hedgehog_k1(const u16* __restrict__ x, const u16* __restrict__ wqkv,
                                                      const u16* __restrict__ wphi, const u16* __restrict__ wo,
                                                      const u16* __restrict__ w1, const u16* __restrict__ b1,
                                                      const u16* __restrict__ w2, char* __restrict__ ws) {
  extern __shared__ __attribute__((aligned(16))) char smem[];
  LDSP char* lds = (LDSP char*)smem;
  const unsigned long long kpc = __builtin_amdgcn_s_getpc();
  const int tid = threadIdx.x, lane = tid & 63;
  const int wave = __builtin_amdgcn_readfirstlane(tid >> 6);
  const int b = blockIdx.x, xcd = b & 7, jx = b >> 3;
  const unsigned ldsbase = (unsigned)(size_t)lds;
  const bool helper = wave >= 6;
  DG(unsigned long long T[8]; T[0] = stamp(); T[7] = rstamp(); T[1] = T[6] = 0;)
  if (helper) {
    const char* pc = (const char*)(kpc & ~(unsigned long long)127) + min(((jx % 3) * 2 + (wave & 1)) * 8192 + lane * 128, CODE_BYTES);
    asm volatile("s_mov_b32 m0, %1\n\ts_nop 0\n\tglobal_load_lds_dword %0, off" ::"v"(pc), "s"(ldsbase + K1_SCR) : "memory");
  }
  if (tid == 0) *(LDSP u32x2*)(lds + K1_MB) = u32x2{0u, 0u};
  WAIT_LGKM0();
  asm volatile("" ::"s"(x), "s"(wqkv), "s"(wphi), "s"(wo), "s"(w1), "s"(b1), "s"(w2), "s"(ws));
  __builtin_amdgcn_s_barrier();
  const int mh = wave & 1;
  const int nj = wave < 4 ? wave >> 1 : 2;
  const int cj = helper ? 2 * mh : nj;
  const int g = lane >> 4, m = lane & 15, q4 = m >> 2, p4 = lane & 3;
  const int rb = 8 * (xcd >> 2) + (jx & 7);
  const int cgi = jx >> 3;
  const bool atype = cgi < 2;
  const int grp = 2 * (xcd & 3) + (cgi & 1);
  const u16* W = atype ? wqkv : w1;
  const int ldw = atype ? 1920 : 1728;
  const int c0 = atype ? 80 * grp : (grp < 6 ? 224 * grp : 1344 + 192 * (grp - 6));
  const int sub = atype ? 640 : (grp < 6 ? 80 : 64);
  const unsigned incw = 64 * ldw;
  const char* xrow = (const char*)x + (size_t)rb * 128 * 1280;
  const int t1 = 1 + 3 * (((jx & 7) + cgi) % 3);
  auto wvoff = [&](int id) {
    const int sj = id / 5, ss = id - 5 * sj;
    const int c = 64 * ss + lane, rho = c / 10, cc = c - 10 * rho;
    const int k = (rho & 0x13) | ((rho & 4) << 1) | ((rho & 8) >> 1);
    const bool narrow = !atype && (grp >= 6 || sj == 2);
    return (unsigned)(k * ldw + c0 + sub * sj + 8 * ((narrow && cc >= 8) ? cc - 2 : cc)) * 2;
  };
  auto xvoff = [&](int pc) {
    const int row = pc * 8 + (lane >> 3);
    return (unsigned)(row * 1280 + (((lane & 7) ^ ((row >> 1) & 7)) << 4));
  };
  const unsigned wa0 = K1_W + cj * WSUB + (16 * (g >> 1) + 4 * (g & 1) + q4) * 160 + p4 * (atype ? 40 : 8);
  const unsigned cstr = atype ? 8 : 32;
  const __amdgpu_buffer_rsrc_t rs = __builtin_amdgcn_make_buffer_rsrc((void*)ws, 0, WS_END, 0x00020000);
  f32x4 acc[4][5];

  if (!helper) {
    const unsigned x0 = xvoff(wave), x1 = xvoff(wave < 2 ? wave + 6 : 7);
    auto xgrp = [&](int u, int xt) {
      const char* px = xrow + xt * 128 + (u & 1) * (64 * 1280);
      const unsigned d = ldsbase + u * 8192 + wave * 1024;
      glds16n(x0, px, d);
      if (wave < 2) glds16n(x1, px, d + 6144);
    };
    xgrp(0, 0);
    xgrp(1, 0);
    const unsigned dw = ldsbase + K1_W + wave * 1024;
    const unsigned v0 = wvoff(wave);
    glds16n(v0, W, dw);
    const unsigned v1 = wvoff(wave + 6);
    glds16n(v1, W, dw + 6144);
    const unsigned v2 = wvoff(wave < 3 ? wave + 12 : 14);
    if (wave < 3) glds16n(v2, W, dw + 12288);
    glds16n(v0, (const char*)W + incw, dw + WSLOT);
    glds16n(v1, (const char*)W + incw, dw + WSLOT + 6144);
    if (wave < 3) glds16n(v2, (const char*)W + incw, dw + WSLOT + 12288);
    xgrp(2, t1);
    K1C S;
    S.lds = lds;
    S.xa = (mh * 64 + m) * 128 + ((g ^ (m >> 1)) << 4);
#pragma unroll
    for (int c = 0; c < 5; ++c) S.wa[c] = wa0 + c * cstr;
    S.wr = 0;
    DG(S.t1 = 0;)
    f32x4 cs[3], s1[4], s2[4];
#pragma unroll
    for (int i = 0; i < 4; ++i) {
      s1[i] = f32x4{0.f, 0.f, 0.f, 0.f}; s2[i] = s1[i]; cs[i % 3] = s1[i];
#pragma unroll
      for (int c = 0; c < 5; ++c) acc[i][c] = s1[i];
    }
    if (wave == 4 && lane < 48) *(LDSP u32x4*)(lds + K1_ZERO + lane * 16) = u32x4{0u, 0u, 0u, 0u};
    const bool five = atype || (grp < 6 && nj < 2);
    if (wave < 2) WAIT_VM(5); else if (wave == 2) WAIT_VM(4); else WAIT_VM(3);
    mb_arrive(lds, 0);
    {
      const int col = c0 + sub * (lane / 10) + 8 * (lane % 10);
      const unsigned vo = atype ? tid * 16 : (col > 1720 ? 1720 : col) * 2;
      if (tid < (atype ? 200 : 30)) glds16n(vo, atype ? (const char*)wphi + grp * 3200 : (const char*)b1, ldsbase + K1_WPHI + wave * 1024);
    }
    mb_wait(lds, 0);
    DG(T[1] = stamp();)
    if (nj == 1) {
      if (five) { if (mh == 0) c_main<5, 1>(S, acc, cs, s1, s2); else c_main<5, 2>(S, acc, cs, s1, s2); }
      else { if (mh == 0) c_main<4, 1>(S, acc, cs, s1, s2); else c_main<4, 2>(S, acc, cs, s1, s2); }
      const int cb = mh == 0 ? 0 : (five ? 3 : 2), cn = mh == 0 ? (five ? 3 : 2) : 2;
      if (m == 0) {
#pragma unroll
        for (int c = 0; c < 3; ++c)
          if (c < cn) *(LDSP f32x4*)(lds + K1_CS + (80 + (cb + c) * 16 + 4 * g) * 4) = cs[c];
      }
      if (g == q4) {
#pragma unroll
        for (int r = 0; r < 4; ++r) {
          const float sq = (m & 2) ? ((m & 1) ? s2[r][3] : s2[r][2]) : ((m & 1) ? s2[r][1] : s2[r][0]);
          const float mean = s1[r][0] * (1.0f / 640.0f);
          const float var = fmaxf(sq * (1.0f / 640.0f) - mean * mean, 0.f);
          float rsd = __builtin_amdgcn_rsqf(var + 1e-5f);
          if (!atype) rsd *= L2E;
          *(LDSP f32x2*)(lds + K1_STAT + (mh * 64 + r * 16 + m) * 8) = f32x2{rsd, -mean * rsd};
        }
      }
      if (atype) {
#pragma unroll
        for (int r = 2; r < 4; ++r)
#pragma unroll
          for (int c = 0; c < 5; ++c) *(LDSP f32x4*)(lds + K1_HO + (mh * 2 + r - 2) * 5120 + c * 1024 + lane * 16) = acc[r][c];
      }
    } else {
      if (five) c_main<5, 0>(S, acc, cs, s1, s2); else c_main<4, 0>(S, acc, cs, s1, s2);
      if (!atype) {
#pragma unroll
        for (int c = 0; c < 5; ++c) *(LDSP f32x4*)(lds + K1_HO + (mh * 2 + (nj >> 1)) * 5120 + c * 1024 + lane * 16) = acc[3][c];
        if (nj == 0 && tid < 120) {
          const unsigned w = *(LDSP unsigned*)(lds + K1_WPHI + tid * 4);
          *(LDSP f32x2*)(lds + K1_BL + tid * 8) = f32x2{bflo(w) * L2E, bfhi(w) * L2E};
        }
      }
    }
    DG(T[6] = S.t1;)
  } else {
    __builtin_amdgcn_s_setprio(3);
    if (mh == 0) {
      const char* p = lane < 12 ? ws + (size_t)lane * (2u << 20) : lane == 12 ? (const char*)w2 : lane == 13 ? (const char*)w2 + (2u << 20) : lane == 14 ? (const char*)wo : (const char*)wphi;
      if (lane < 16) asm volatile("s_mov_b32 m0, %1\n\ts_nop 0\n\tglobal_load_lds_dword %0, off" ::"v"(p), "s"(ldsbase + K1_SCR) : "memory");
    }
    K1H S;
    S.lds = lds; S.ldsbase = ldsbase;
    S.incw = incw;
    S.pw = (const char*)W + 2 * t1 * incw;
    S.wrapw = (unsigned)(size_t)W + 20 * incw;
    S.px = xrow + t1 * 128 + 64 * 1280;
    S.wrapx = (unsigned)(size_t)xrow + 1280;
    S.dw = 2 * WSLOT; S.dx = 3 * 8192; S.wr = 0;
    DG(S.t1 = 0;)
#pragma unroll
    for (int i = 0; i < 4; ++i) S.vx[i] = xvoff(4 * mh + i);
#pragma unroll
    for (int i = 0; i < 8; ++i) S.vw[i] = wvoff(8 * mh + i < 15 ? 8 * mh + i : 14);
#pragma unroll
    for (int c = 0; c < 5; ++c) { S.wa[c] = wa0 + c * cstr; acc[0][c] = f32x4{0.f, 0.f, 0.f, 0.f}; }
    S.pc2 = jx < 2 ? (const char*)(kpc & ~(unsigned long long)127) + CODE2_OFF : nullptr;
    S.rs = rs; S.w2 = w2; S.wo = wo; S.first = b * 12 + mh * 6;
    if (mh == 0) h_main<0>(S, acc[0]); else h_main<1>(S, acc[0]);
    DG(T[6] = S.t1;)
    if (m == 0) {
#pragma unroll
      for (int c = 0; c < 5; ++c) *(LDSP f32x4*)(lds + K1_CS + (cj * 80 + c * 16 + 4 * g) * 4) = acc[0][c];
    }
  }
  DG(T[2] = stamp();)
  WAIT_LGKM0();
  __builtin_amdgcn_s_barrier();
  asm volatile("" ::: "memory");
  DG(T[3] = stamp();)
#define DG_OUT1() DG({ T[5] = stamp(); unsigned long long re = rstamp(); if (lane == 0) { unsigned long long* dp = (unsigned long long*)(ws + OFF_DBG) + (size_t)(b * 8 + wave) * 16; for (int i = 0; i < 8; ++i) dp[i] = T[i]; dp[8] = re; } if (lane < 20) ((unsigned*)(ws + OFF_DBG + (2u << 20)))[(b * 8 + wave) * 32 + lane] = *(LDSP unsigned*)(lds + K1_LDS + wave * 128 + lane * 4) - (unsigned)T[0]; })
  auto side_store = [&]() {
    WAIT_VM(0);
    LDSP char* pr = lds + K1_RT + mh * 6144 + (4 * g + q4) * 32 + p4 * 8;
#pragma unroll
    for (int i = 0; i < 6; ++i) {
      const int gi = b * 12 + mh * 6 + i, cq = (gi >> 2) / 74, ks = (gi >> 2) - 74 * cq;
      const u32x4 v = trread2(pr + i * 1024, pr + i * 1024 + 512);
      if (gi < 2960) st16(rs, v, lane * 16, OFF_WT + ((4 * cq + (gi & 3)) * 74 + ks) * 1024);
    }
  };

  if (!atype) {
    auto round = [&](const f32x4 (&a)[5], int ej, int rt) {
      const bool five = grp < 6 && ej < 2;
      const f32x2 st = *(LDSP f32x2*)(lds + K1_STAT + (rt * 16 + m) * 8);
      u32x2 pk[5];
#pragma unroll
      for (int c = 0; c < 5; ++c) {
        if (c == 4 && !five) { pk[4] = u32x2{0u, 0u}; break; }
        const f32x4 cs = *(LDSP f32x4*)(lds + K1_CS + (ej * 80 + c * 16 + 4 * g) * 4);
        const f32x4 bl = *(LDSP f32x4*)(lds + K1_BL + (ej * 80 + c * 16 + 4 * g) * 4);
        float res[4];
#pragma unroll
        for (int e = 0; e < 4; ++e) {
          const float hl = fmaf(a[c][e], st[0], fmaf(st[1], cs[e], bl[e]));
          const float ex = __builtin_amdgcn_exp2f(hl);
          const float rc = __builtin_amdgcn_rcpf(fmaf(ex, ex + 2.f, 2.f));
          res[e] = hl * fmaf(rc, -2.f * LN2, LN2);
        }
        pk[c] = u32x2{pk2(res[0], res[1]), pk2(res[2], res[3])};
      }
      const int T0 = (c0 + sub * ej) >> 4;
      const unsigned base = OFF_ACT + ((rb * 8 + rt) * 54 + (T0 >> 1)) * 1024;
      if (!(T0 & 1)) {
        st16(rs, u32x4{pk[0][0], pk[0][1], pk[1][0], pk[1][1]}, lane * 16, base);
        st16(rs, u32x4{pk[2][0], pk[2][1], pk[3][0], pk[3][1]}, lane * 16, base + 1024);
        if (five) st8(rs, pk[4], lane * 16, base + 2048);
      } else {
        st8(rs, pk[0], lane * 16, base + 8);
        st16(rs, u32x4{pk[1][0], pk[1][1], pk[2][0], pk[2][1]}, lane * 16, base + 1024);
        st16(rs, u32x4{pk[3][0], pk[3][1], pk[4][0], pk[4][1]}, lane * 16, base + 2048);
      }
    };
    if (helper) {
#pragma unroll
      for (int r = 0; r < 2; ++r)
#pragma unroll
        for (int c = 0; c < 5; ++c) acc[r][c] = *(LDSP f32x4*)(lds + K1_HO + (mh * 2 + r) * 5120 + c * 1024 + lane * 16);
    }
#pragma unroll 1
    for (int it = 0; it < (helper ? 1 : 2); ++it) {
      round(acc[0], helper ? 0 : nj, mh * 4 + (helper ? 3 : 2 * it));
      if (it == 0 || nj == 1) round(acc[1], helper ? 2 : nj, mh * 4 + (helper ? 3 : 2 * it + 1));
#pragma unroll
      for (int c = 0; c < 5; ++c) { acc[0][c] = acc[2][c]; acc[1][c] = acc[3][c]; }
    }
    DG(T[4] = stamp();)
    if (helper) side_store();
    DG_OUT1();
    return;
  }

  auto normalise = [&](const f32x4 (&a)[5], const f32x4 (&cs)[5], int rt, u32x4 (&bq)[3], unsigned one) {
    const f32x2 st = *(LDSP f32x2*)(lds + K1_STAT + (rt * 16 + m) * 8);
#pragma unroll
    for (int c = 0; c < 5; ++c) {
      float v[4];
#pragma unroll
      for (int e = 0; e < 4; ++e) v[e] = fmaf(a[c][e], st[0], st[1] * cs[c][e]);
      bq[c >> 1][(c & 1) * 2] = pk2(v[0], v[1]);
      bq[c >> 1][(c & 1) * 2 + 1] = pk2(v[2], v[3]);
    }
    bq[2][2] = one;
    bq[2][3] = 0u;
  };
  f32x4 csv[5];
  if (!helper && nj != 1) {
#pragma unroll
    for (int c = 0; c < 5; ++c) csv[c] = *(LDSP f32x4*)(lds + K1_CS + (nj * 80 + c * 16 + 4 * g) * 4);
#pragma unroll
    for (int r = 0; r < 4; ++r) {
      u32x4 bq[3];
      normalise(acc[r], csv, mh * 4 + r, bq, nj == 2 ? 0x00003f80u : 0u);
      if (nj == 0) {
        const unsigned so = OFF_QT + (4 * grp * 128 + rb * 8 + mh * 4 + r) * 768;
#pragma unroll
        for (int i = 0; i < 3; ++i) st16(rs, bq[i], g * (128 * 768) + m * 16, so + i * 256);
      } else {
#pragma unroll
        for (int i = 0; i < 3; ++i) *(LDSP u32x4*)(lds + K1_VIMG + (mh * 64 + r * 16 + m) * V_PITCH + g * 48 + i * 16) = bq[i];
      }
    }
  }
  if (helper || nj != 2) {
    const int kr0 = helper ? 3 : (nj == 1 ? 0 : 2);
    if (kr0) {
#pragma unroll
      for (int c = 0; c < 5; ++c) acc[0][c] = *(LDSP f32x4*)(lds + K1_HO + (mh * 2 + kr0 - 2) * 5120 + c * 1024 + lane * 16);
    }
#pragma unroll
    for (int c = 0; c < 5; ++c) csv[c] = *(LDSP f32x4*)(lds + K1_CS + (80 + c * 16 + 4 * g) * 4);
    LDSP char* wl = lds + ((p4 == g) ? K1_WPHI + g * 800 + q4 * 40 : K1_ZERO);
#pragma unroll 1
    for (int kr = kr0; kr < (kr0 ? kr0 + 1 : 2); ++kr) {
      u32x4 bq[3];
      normalise(acc[0], csv, mh * 4 + kr, bq, 0u);
      f32x4 pk[5];
      head_proj(wl, bq, pk);
      unsigned ph[20];
      feature_map(pk, ph);
      LDSP char* dst = lds + (mh * 64 + kr * 16 + m) * PK_PITCH + g * 80;
#pragma unroll
      for (int i = 0; i < 5; ++i) *(LDSP u32x4*)(dst + i * 16) = u32x4{ph[4 * i], ph[4 * i + 1], ph[4 * i + 2], ph[4 * i + 3]};
#pragma unroll
      for (int c = 0; c < 5; ++c) acc[0][c] = acc[1][c];
    }
  }
  DG(T[4] = stamp();)
  WAIT_LGKM0();
  __builtin_amdgcn_s_barrier();
  asm volatile("" ::: "memory");
  if (!helper) {
#pragma unroll 1
    for (int job = wave; job < 12; job += 6) {
      const int hh = job & 3, ft = job >> 2;
      LDSP char* pa = lds + (4 * g + q4) * PK_PITCH + hh * 80 + ft * 32 + p4 * 8;
      LDSP char* pb = lds + K1_VIMG + (4 * g + q4) * V_PITCH + hh * 48 + p4 * 8;
      f32x4 d0 = {0.f, 0.f, 0.f, 0.f}, d1 = {0.f, 0.f, 0.f, 0.f};
#pragma unroll
      for (int kk = 0; kk < 4; ++kk) {
        u32x4 a = trread2(pa + (32 * kk) * PK_PITCH, pa + (32 * kk + 16) * PK_PITCH);
        u32x4 v0 = trread2(pb + (32 * kk) * V_PITCH, pb + (32 * kk + 16) * V_PITCH);
        u32x4 v1 = trread2(pb + (32 * kk) * V_PITCH + 32, pb + (32 * kk + 16) * V_PITCH + 32);
        d0 = mfma(a, v0, d0);
        d1 = mfma(a, v1, d1);
      }
      const unsigned so = OFF_KVP + ((4 * grp + hh) * 16 + rb) * KVP_B + 1344 * ft;
      st16(rs, __builtin_bit_cast(u32x4, d0), m * 64 + g * 16, so);
      if (m < 5) st16(rs, __builtin_bit_cast(u32x4, d1), (16 + m) * 64 + g * 16, so);
    }
  } else {
    side_store();
  }
  DG_OUT1();
}

constexpr unsigned K2_YD = 0;
constexpr unsigned K2_SD = 4 * 15360;
constexpr unsigned K2_KVT = K2_SD + 4 * 20480;
constexpr unsigned K2_ZERO = K2_KVT + 1920;
constexpr unsigned K2_WPHI = K2_ZERO + 1792;
constexpr unsigned K2_FLAG = K2_WPHI + 3200;
constexpr unsigned K2_OWN = K2_FLAG + 16;
constexpr unsigned K2_LDS = K2_OWN + 8192;

DEVI u32x4 bld(__amdgpu_buffer_rsrc_t rs, unsigned voff, unsigned soff) {
  return __builtin_amdgcn_raw_buffer_load_b128(rs, voff, soff, 0);
}
DEVI u32x4 bld_sc1(__amdgpu_buffer_rsrc_t rs, unsigned voff, unsigned soff) {
  return __builtin_amdgcn_raw_buffer_load_b128(rs, voff, soff, 16);
}
template <int N>
DEVI void y_stream(__amdgpu_buffer_rsrc_t rs, unsigned act, unsigned wt, int rot, f32x4 (&acc)[4][5]) {
  const unsigned l16 = (threadIdx.x & 63) * 16;
  unsigned ab[4];
#pragma unroll
  for (int l = 0; l < 4; ++l) ab[l] = act + ((l + rot) & 3) * (54 * 1024);
  u32x4 A[3][4], B[3][5];
  auto load = [&](int i) {
#pragma unroll
    for (int l = 0; l < 4; ++l) A[i % 3][l] = bld(rs, l16, ab[l] + i * 1024);
#pragma unroll
    for (int c = 0; c < 5; ++c) B[i % 3][c] = bld(rs, l16, wt + c * (74 * 1024) + i * 1024);
  };
  load(0);
  load(1);
#pragma unroll
  for (int i = 0; i < N; ++i) {
    if (i + 2 < N) load(i + 2);
    __builtin_amdgcn_sched_barrier(0);
#pragma unroll
    for (int l = 0; l < 4; ++l)
#pragma unroll
      for (int c = 0; c < 5; ++c) acc[l][c] = mfma(B[i % 3][c], A[i % 3][l], acc[l][c]);
    __builtin_amdgcn_sched_barrier(0);
  }
}

template <int C0, int C1>
DEVI void combine(LDSP char* lds, const u32x2 (&xv)[3], u16* out, int rt, int rb2, int cb, const f32x4 (&own)[5]) {
  const int lane = threadIdx.x & 63;
  const unsigned l16 = lane * 16;
  const size_t orow = (size_t)(64 * rb2 + 16 * rt + (lane & 15)) * 640 + 80 * cb + 4 * (lane >> 4);
  WAIT_LGKM0();
  __builtin_amdgcn_s_barrier();
  asm volatile("" ::: "memory");
#pragma unroll
  for (int c = C0; c < C1; ++c) {
    f32x4 y = C0 == 0 ? own[c] : *(LDSP f32x4*)(lds + K2_OWN + (rt * 2 + c - 3) * 1024 + l16);
#pragma unroll
    for (int l = 1; l < 4; ++l)
      y += *(LDSP f32x4*)(lds + K2_YD + ((rt - l) & 3) * 15360 + ((l - 1) * 5 + c) * 1024 + l16);
    LDSP f32x4* sp = (LDSP f32x4*)(lds + K2_SD + (rt * 5 + c) * 1024 + l16);
    const f32x4 s = sp[0] + sp[1280];
    y += sp[2560] + sp[3840];
    float o[4];
#pragma unroll
    for (int e = 0; e < 4; ++e) {
      const float gate = y[e] * __builtin_amdgcn_rcpf(1.f + __builtin_amdgcn_exp2f(-y[e] * L2E));
      const unsigned xw = xv[c - C0][e >> 1];
      o[e] = fmaf(s[e], gate, (e & 1) ? bfhi(xw) : bflo(xw));
    }
    *(u32x2*)(out + orow + 16 * c) = u32x2{pk2(o[0], o[1]), pk2(o[2], o[3])};
  }
}

__global__ __launch_bounds__(512, 2) void hedgehog_k2(const u16* __restrict__ x, const u16* __restrict__ wphi,
                                                      char* __restrict__ ws, u16* __restrict__ out) {
  extern __shared__ __attribute__((aligned(16))) char smem[];
  LDSP char* lds = (LDSP char*)smem;
  const int tid = threadIdx.x, lane = tid & 63;
  const int wave = __builtin_amdgcn_readfirstlane(tid >> 6);
  const int g = lane >> 4, m = lane & 15, q4 = m >> 2, p4 = lane & 3;
  const int b = blockIdx.x, xcd = b & 7, jx = b >> 3;
  const int rb2 = 8 * (xcd >> 1) + (jx & 7), cb = 4 * (xcd & 1) + (jx >> 3);
  const __amdgpu_buffer_rsrc_t rs = __builtin_amdgcn_make_buffer_rsrc((void*)ws, 0, WS_END, 0x00020000);
  const unsigned l16 = lane * 16;
  DG(unsigned long long T[8]; T[0] = stamp(); T[7] = rstamp(); T[1] = T[2] = T[3] = T[4] = T[5] = T[6] = 0;)
#define DG_OUT2() DG({ unsigned long long re = rstamp(); if (lane == 0) { unsigned long long* dp = (unsigned long long*)(ws + OFF_DBG + (1u << 20)) + (size_t)(b * 8 + wave) * 16; for (int i = 0; i < 8; ++i) dp[i] = T[i]; dp[8] = re; } })
  asm volatile("" ::"s"(x), "s"(wphi), "s"(out));
  const unsigned act = OFF_ACT + 4 * rb2 * (54 * 1024), wt = OFF_WT + 5 * cb * (74 * 1024);
  const u16* xt = x + (size_t)(64 * rb2 + 16 * (wave & 3) + m) * 640 + 80 * cb + 4 * g;
  f32x4 acc[4][5];
#pragma unroll
  for (int r = 0; r < 4; ++r)
#pragma unroll
    for (int c = 0; c < 5; ++c) acc[r][c] = f32x4{0.f, 0.f, 0.f, 0.f};

  if (wave < 4) {
    __builtin_amdgcn_s_barrier();
    DG(T[1] = stamp();)
    u32x2 xv[3];
#pragma unroll
    for (int c = 0; c < 3; ++c) xv[c] = *(const u32x2*)(xt + 16 * c);
    y_stream<K2_NY>(rs, act + wave * (K2_NY * 1024), wt + wave * (K2_NY * 1024), wave, acc);
    DG(T[3] = stamp();)
#pragma unroll
    for (int l = 1; l < 4; ++l)
#pragma unroll
      for (int c = 0; c < 5; ++c)
        *(LDSP f32x4*)(lds + K2_YD + wave * 15360 + ((l - 1) * 5 + c) * 1024 + l16) = acc[l][c];
#pragma unroll
    for (int c = 3; c < 5; ++c) *(LDSP f32x4*)(lds + K2_OWN + (wave * 2 + c - 3) * 1024 + l16) = acc[0][c];
    DG(T[4] = stamp();)
    combine<0, 3>(lds, xv, out, wave, rb2, cb, acc[0]);
    DG(T[6] = stamp();)
    DG_OUT2();
    return;
  }

  const int aw = wave - 4;
  const int h = 4 * xcd + (jx & 3), sb = jx >> 2;
  const int tok = sb * 256 + aw * 64 + lane;
  u32x4 wv = *(const u32x4*)(wphi + h * 400 + (lane < 50 ? lane : 49) * 8);
  u32x4 bq[3];
#pragma unroll
  for (int s = 0; s < 3; ++s) bq[s] = bld(rs, (unsigned)(tok >> 4) * 768 + m * 16, OFF_QT + h * (128 * 768) + s * 256);
  int item = aw * 53 + (lane < 53 ? lane : 52);
  item = item < 210 ? item : 209;
  const int d = item / 10, fq = item - 10 * d;
  u32x4 part[16];
#pragma unroll
  for (int r = 0; r < 16; ++r) part[r] = bld(rs, (unsigned)((fq >> 2) * 1344 + d * 64 + (fq & 3) * 16), OFF_KVP + (h * 16 + r) * KVP_B);
  if (aw == 0) {
    *(LDSP u32x4*)(lds + K2_ZERO + l16) = u32x4{0u, 0u, 0u, 0u};
    if (lane < 48) *(LDSP u32x4*)(lds + K2_ZERO + 1024 + l16) = u32x4{0u, 0u, 0u, 0u};
    if (lane == 0) *(LDSP u32x4*)(lds + K2_FLAG) = u32x4{0u, 0u, 0u, 0u};
  }
  WAIT_LGKM0();
  __builtin_amdgcn_s_barrier();
  __builtin_amdgcn_s_setprio(2);
  *(LDSP u32x4*)(lds + K2_WPHI + aw * 800 + (lane < 50 ? lane : 49) * 16) = wv;
  unsigned ph[20];
  {
    LDSP char* wl = lds + ((p4 == g) ? K2_WPHI + aw * 800 + q4 * 40 : K2_ZERO);
    f32x4 pq[5];
    head_proj(wl, bq, pq);
    feature_map(pq, ph);
#pragma unroll
    for (int i = 0; i < 20; ++i) asm volatile("" : "+v"(ph[i]));
  }
  DG(T[1] = stamp();)
  {
    f32x4 sum = __builtin_bit_cast(f32x4, part[0]);
#pragma unroll
    for (int r = 1; r < 16; ++r) sum += __builtin_bit_cast(f32x4, part[r]);
    *(LDSP u32x2*)(lds + K2_KVT + d * 80 + fq * 8) = u32x2{pk2(sum[0], sum[1]), pk2(sum[2], sum[3])};
    WAIT_LGKM0();
    if (lane == 0) *(volatile LDSP unsigned*)(lds + K2_FLAG + aw * 4) = 1u;
    for (int spin = 0; spin < (1 << 22); ++spin) {
      const u32x4 f = *(volatile LDSP u32x4*)(lds + K2_FLAG);
      if (__builtin_amdgcn_readfirstlane(f[0] & f[1] & f[2] & f[3])) break;
      __builtin_amdgcn_s_sleep(1);
    }
    asm volatile("" ::: "memory");
  }
  DG(T[2] = stamp();)
  {
    LDSP char* kl = lds + (((m >> 2) == g) ? K2_KVT + (m & 3) * 80 : K2_ZERO);
    f32x4 nd[6];
#pragma unroll
    for (int t = 0; t < 6; ++t) {
      f32x4 c = {0.f, 0.f, 0.f, 0.f};
#pragma unroll
      for (int s = 0; s < 5; ++s)
        c = mfma(*(LDSP u32x4*)(kl + t * 320 + s * 16), u32x4{ph[4 * s], ph[4 * s + 1], ph[4 * s + 2], ph[4 * s + 3]}, c);
      nd[t] = c;
    }
    const float rd = __builtin_amdgcn_rcpf(nd[5][0] + 1e-6f);
    unsigned ow[10];
#pragma unroll
    for (int t = 0; t < 5; ++t) {
      ow[2 * t] = min(pk2(nd[t][0] * rd, nd[t][1] * rd), 0xFFFFFFFEu);
      ow[2 * t + 1] = min(pk2(nd[t][2] * rd, nd[t][3] * rd), 0xFFFFFFFEu);
    }
    const int e0 = (5 * h + 1) >> 1, e8 = (h & 1) ? e0 - 1 : e0 + 2;
    const unsigned vo = (unsigned)(tok >> 4) * 20480 + m * 16;
    const bool odd = h & 1;
    st16(rs, odd ? u32x4{ow[2], ow[3], ow[4], ow[5]} : u32x4{ow[0], ow[1], ow[2], ow[3]}, vo, OFF_OT + e0 * 256);
    st16(rs, odd ? u32x4{ow[6], ow[7], ow[8], ow[9]} : u32x4{ow[4], ow[5], ow[6], ow[7]}, vo, OFF_OT + (e0 + 1) * 256);
    st8(rs, odd ? u32x2{ow[0], ow[1]} : u32x2{ow[8], ow[9]}, vo, OFF_OT + e8 * 256 + (h & 1) * 8);
  }
  DG(T[3] = stamp();)
  const unsigned ring = K2_SD + aw * 20480;
  u32x2 xv[3];
#pragma unroll
  for (int c = 0; c < 2; ++c) xv[c] = *(const u32x2*)(xt + 48 + 16 * c);
  xv[2] = xv[1];
  if (aw >= 2) {
    __builtin_amdgcn_s_setprio(0);
    constexpr int NA = (54 - 4 * K2_NY) / 2;
    y_stream<NA>(rs, act + (4 * K2_NY + NA * (aw - 2)) * 1024, wt + (4 * K2_NY + NA * (aw - 2)) * 1024, 0, acc);
  } else {
    u32x4 B[3][5];
    auto loadb = [&](int i) {
#pragma unroll
      for (int c = 0; c < 5; ++c) B[i % 3][c] = bld(rs, l16, wt + c * (74 * 1024) + (54 + 10 * aw + i) * 1024);
    };
    loadb(0);
    loadb(1);
    const unsigned ob = OFF_OT + (4 * rb2 * 20 + 10 * aw) * 1024;
    {
      const int l2 = lane < 40 ? lane : 39;
      const unsigned pv = 3 * 20480 + l2 * 256 + 15 * 16 + 4;
      for (int spin = 0; spin < (1 << 20); ++spin) {
        asm volatile("" ::: "memory");
        const u32x2 w = __builtin_amdgcn_raw_buffer_load_b64(rs, pv, ob, 16);
        if (!__any(w[0] == 0xFFFFFFFFu || w[1] == 0xFFFFFFFFu)) break;
        __builtin_amdgcn_s_sleep(4);
      }
    }
    DG(T[4] = stamp();)
#pragma unroll
    for (int half = 0; half < 2; ++half) {
      u32x4 A[5][4];
      for (int spin = 0; spin < (1 << 16); ++spin) {
        asm volatile("" ::: "memory");
        unsigned mx = 0;
#pragma unroll
        for (int i = 0; i < 5; ++i)
#pragma unroll
          for (int l = 0; l < 4; ++l) A[i][l] = bld_sc1(rs, l16, ob + l * 20480 + (5 * half + i) * 1024);
#pragma unroll
        for (int i = 0; i < 5; ++i)
#pragma unroll
          for (int l = 0; l < 4; ++l) mx = max(max(mx, max(A[i][l][0], A[i][l][1])), max(A[i][l][2], A[i][l][3]));
        if (!__any(mx == 0xFFFFFFFFu)) break;
        __builtin_amdgcn_s_sleep(4);
      }
      DG(if (half == 0) T[5] = stamp();)
#pragma unroll
      for (int i = 0; i < 5; ++i) {
        const int k = 5 * half + i;
        if (k + 2 < 10) loadb(k + 2);
        __builtin_amdgcn_sched_barrier(0);
#pragma unroll
        for (int l = 0; l < 4; ++l)
#pragma unroll
          for (int c = 0; c < 5; ++c) acc[l][c] = mfma(B[k % 3][c], A[i][l], acc[l][c]);
        __builtin_amdgcn_sched_barrier(0);
      }
    }
  }
#pragma unroll
  for (int l = 0; l < 4; ++l)
#pragma unroll
    for (int c = 0; c < 5; ++c) *(LDSP f32x4*)(lds + ring + (l * 5 + c) * 1024 + l16) = acc[l][c];
  DG(T[6] = stamp();)
  combine<3, 5>(lds, xv, out, aw, rb2, cb, acc[0]);
  DG_OUT2();
}

extern "C" void kernel_launch(void* const* d_in, const int* in_sizes, int n_in, void* d_out, int out_size, void* d_ws,
                              size_t ws_size, hipStream_t stream) {
  const u16* x = (const u16*)d_in[0];
  const u16* wqkv = (const u16*)d_in[1];
  const u16* wphi = (const u16*)d_in[2];
  const u16* wo = (const u16*)d_in[3];
  const u16* w1 = (const u16*)d_in[5];
  const u16* b1 = (const u16*)d_in[6];
  const u16* w2 = (const u16*)d_in[7];
  (void)hipFuncSetAttribute((const void*)hedgehog_k1, hipFuncAttributeMaxDynamicSharedMemorySize, K1_LDS DG(+ 1024));
  (void)hipFuncSetAttribute((const void*)hedgehog_k2, hipFuncAttributeMaxDynamicSharedMemorySize, K2_LDS);
  hedgehog_k1<<<256, 512, K1_LDS DG(+ 1024), stream>>>(x, wqkv, wphi, wo, w1, b1, w2, (char*)d_ws);
  hedgehog_k2<<<256, 512, K2_LDS, stream>>>(x, wphi, (char*)d_ws, (u16*)d_out);
}
```

```cpp
#define ROT3
#include <hip/hip_runtime.h>
#include <stdint.h>

typedef unsigned short u16;
typedef __attribute__((ext_vector_type(2))) __bf16 bf16x2;
typedef __attribute__((ext_vector_type(8))) __bf16 bf16x8;
typedef __attribute__((ext_vector_type(4))) short s16x4;
typedef __attribute__((ext_vector_type(2))) float f32x2;
typedef __attribute__((ext_vector_type(4))) float f32x4;
typedef __attribute__((ext_vector_type(2))) unsigned u32x2;
typedef __attribute__((ext_vector_type(4))) unsigned u32x4;
#define LDSP __attribute__((address_space(3)))
#define DEVI __device__ __forceinline__

constexpr unsigned OFF_ACT = 0;
constexpr unsigned OFF_WT = OFF_ACT + 128u * 54u * 1024u;
constexpr unsigned OFF_OT = OFF_WT + 40u * 74u * 1024u;
constexpr unsigned OFF_QT = OFF_OT + 128u * 20u * 1024u;
constexpr unsigned OFF_KVP = OFF_QT + 32u * 2048u * 48u;
constexpr unsigned KVP_B = 3 * 1344;
constexpr unsigned OFF_DUMMY = OFF_KVP + 32u * 16u * KVP_B;
constexpr unsigned WS_END = OFF_DUMMY + 640u * 1024u;

#define CODE_BYTES (46 * 1024)
#define CODE2_OFF (36 * 1024)
#define K2_NY 11
#define K1_MAP 1
constexpr float L2E = 1.4426950408889634f, LN2 = 0.6931471805599453f;

DEVI unsigned pk2(float a, float b) {
  bf16x2 v = {(__bf16)a, (__bf16)b};
  return __builtin_bit_cast(unsigned, v);
}
DEVI float bflo(unsigned u) { return __uint_as_float(u << 16); }
DEVI float bfhi(unsigned u) { return __uint_as_float(u & 0xffff0000u); }
DEVI f32x4 mfma(u32x4 a, u32x4 b, f32x4 c) {
  return __builtin_amdgcn_mfma_f32_16x16x32_bf16(__builtin_bit_cast(bf16x8, a), __builtin_bit_cast(bf16x8, b), c, 0, 0, 0);
}
DEVI u32x2 trread(LDSP char* p) {
  return __builtin_bit_cast(u32x2, __builtin_amdgcn_ds_read_tr16_b64_v4i16((LDSP s16x4*)p));
}
DEVI u32x4 trread2(LDSP char* p0, LDSP char* p1) {
  u32x2 a = trread(p0), b = trread(p1);
  return u32x4{a[0], a[1], b[0], b[1]};
}
DEVI float dot2(unsigned a, unsigned b, float c) {
  return __builtin_amdgcn_fdot2_f32_bf16(__builtin_bit_cast(bf16x2, a), __builtin_bit_cast(bf16x2, b), c, false);
}
DEVI float dot_ones(u32x4 a, float c) {
  c = dot2(a[0], 0x3f803f80u, c); c = dot2(a[1], 0x3f803f80u, c);
  c = dot2(a[2], 0x3f803f80u, c); c = dot2(a[3], 0x3f803f80u, c);
  return c;
}
DEVI float dot_self(u32x4 a, float c) {
  c = dot2(a[0], a[0], c); c = dot2(a[1], a[1], c); c = dot2(a[2], a[2], c); c = dot2(a[3], a[3], c);
  return c;
}
DEVI float red_g(float v) {
  v += __shfl_xor(v, 16);
  v += __shfl_xor(v, 32);
  return v;
}
DEVI void glds16(unsigned voff, const void* sbase, unsigned lds_dst) {
  unsigned keep;
  asm volatile("s_mov_b32 %0, m0\n\ts_mov_b32 m0, %3\n\ts_nop 0\n\tglobal_load_lds_dwordx4 %1, %2\n\ts_mov_b32 m0, %0"
               : "=&s"(keep) : "v"(voff), "s"(sbase), "s"(lds_dst) : "memory");
}
#define DG(...)
#define WAIT_VM(n) asm volatile("s_waitcnt vmcnt(" #n ")" ::: "memory")
#define WAIT_LGKM0() asm volatile("s_waitcnt lgkmcnt(0)" ::: "memory")

DEVI void feature_map(const f32x4 (&p)[5], unsigned (&o)[20]) {
  float mx = p[0][0], mn = p[0][0];
#pragma unroll
  for (int t = 0; t < 5; ++t)
#pragma unroll
    for (int r = 0; r < 4; ++r) { mx = fmaxf(mx, p[t][r]); mn = fminf(mn, p[t][r]); }
  const float a = -mx * L2E, b = mn * L2E;
  float e1[20], e2[20], s1 = 0.f, s2 = 0.f;
#pragma unroll
  for (int t = 0; t < 5; ++t)
#pragma unroll
    for (int r = 0; r < 4; ++r) {
      e1[4 * t + r] = __builtin_amdgcn_exp2f(fmaf(p[t][r], L2E, a));
      e2[4 * t + r] = __builtin_amdgcn_exp2f(fmaf(p[t][r], -L2E, b));
      s1 += e1[4 * t + r]; s2 += e2[4 * t + r];
    }
  const float r1 = __builtin_amdgcn_rcpf(s1), r2 = __builtin_amdgcn_rcpf(s2);
#pragma unroll
  for (int i = 0; i < 10; ++i) {
    o[i] = pk2(e1[2 * i] * r1, e1[2 * i + 1] * r1);
    o[10 + i] = pk2(e2[2 * i] * r2, e2[2 * i + 1] * r2);
  }
}
DEVI void head_proj(LDSP char* wl, const u32x4 (&b)[3], f32x4 (&p)[5]) {
#pragma unroll
  for (int t = 0; t < 5; ++t) {
    u32x4 a0 = trread2(wl + 0 * 40 + t * 8, wl + 4 * 40 + t * 8);
    u32x4 a1 = trread2(wl + 8 * 40 + t * 8, wl + 12 * 40 + t * 8);
    u32x2 h2 = trread(wl + 16 * 40 + t * 8);
    u32x4 a2 = {h2[0], h2[1], 0u, 0u};
    f32x4 c = {0.f, 0.f, 0.f, 0.f};
    c = mfma(a0, b[0], c);
    c = mfma(a1, b[1], c);
    c = mfma(a2, b[2], c);
    p[t] = c;
  }
}

constexpr unsigned XSLOT = 16384, WSUB = 5120, WSLOT = 3 * WSUB;
constexpr unsigned K1_W = 4 * XSLOT;
constexpr unsigned K1_WPHI = K1_W + 5 * WSLOT;
constexpr unsigned K1_ZERO = K1_WPHI + 3200;
constexpr unsigned K1_STAT = K1_ZERO + 768;
constexpr unsigned K1_CS = K1_STAT + 1024;
constexpr unsigned K1_BL = K1_CS + 960;
constexpr unsigned K1_SCR = K1_BL + 960;
constexpr unsigned K1_MB = K1_SCR + 256;
constexpr unsigned K1_LDS = K1_MB + 16;
constexpr unsigned PK_PITCH = 352, V_PITCH = 224, K1_VIMG = 128 * PK_PITCH;
constexpr unsigned K1_HO = K1_VIMG + 128 * V_PITCH;
constexpr unsigned K1_RT = K1_W + 2 * WSLOT;

DEVI void glds16n(unsigned voff, const void* sbase, unsigned lds_dst) {
  asm volatile("s_mov_b32 m0, %2\n\ts_nop 0\n\tglobal_load_lds_dwordx4 %0, %1" ::"v"(voff), "s"(sbase), "s"(lds_dst) : "memory");
}
DEVI void st16(__amdgpu_buffer_rsrc_t rs, u32x4 v, unsigned voff, unsigned soff) {
  __builtin_amdgcn_raw_buffer_store_b128(v, rs, voff + soff, 0, 16);
}
DEVI void st8(__amdgpu_buffer_rsrc_t rs, u32x2 v, unsigned voff, unsigned soff) {
  __builtin_amdgcn_raw_buffer_store_b64(v, rs, voff + soff, 0, 16);
}
#define STEP_SYNC() do { __builtin_amdgcn_s_waitcnt(0xC07F); __builtin_amdgcn_s_barrier(); __builtin_amdgcn_sched_barrier(0); } while (0)

DEVI void mb_arrive(LDSP char* lds, int i) {
  WAIT_LGKM0();
  if ((threadIdx.x & 63) == 0) __hip_atomic_fetch_add((LDSP unsigned*)(lds + K1_MB + 4 * i), 1u, __ATOMIC_RELAXED, __HIP_MEMORY_SCOPE_WORKGROUP);
}
DEVI void mb_wait(LDSP char* lds, int i) {
  for (int spin = 0; spin < (1 << 22); ++spin) {
    if (__builtin_amdgcn_readfirstlane(*(volatile LDSP unsigned*)(lds + K1_MB + 4 * i)) >= 6u) break;
    __builtin_amdgcn_s_sleep(1);
  }
  asm volatile("" ::: "memory");
  __builtin_amdgcn_sched_barrier(0);
}
struct K1C {
  LDSP char* lds;
  unsigned xa, wa[5], wr;
  DG(unsigned long long t1;)
};
template <int NC>
DEVI void c_read(K1C& S, int s, u32x4 (&xf)[4], u32x4 (&wf)[5]) {
  LDSP char* px = S.lds + ((s >> 1) & 3) * XSLOT + (S.xa ^ ((s & 1) << 6));
  LDSP char* pw = S.lds + S.wr;
  xf[0] = *(LDSP u32x4*)(px);
  wf[0] = trread2(pw + S.wa[0], pw + S.wa[0] + 1280);
#pragma unroll
  for (int r = 1; r < 4; ++r) xf[r] = *(LDSP u32x4*)(px + r * 2048);
#pragma unroll
  for (int c = 1; c < NC; ++c) wf[c] = trread2(pw + S.wa[c], pw + S.wa[c] + 1280);
  S.wr = S.wr == 4 * WSLOT ? 0 : S.wr + WSLOT;
}
template <int NC, int ROLE, bool READ, bool MINI = false>
DEVI void c_step(K1C& S, int s, const u32x4 (&xc)[4], const u32x4 (&wc)[5], u32x4 (&xn)[4], u32x4 (&wn)[5], f32x4 (&acc)[4][5],
                 f32x4 (&cs)[3], f32x4 (&s1)[4], f32x4 (&s2)[4], const u32x4& ones) {
  if (READ) {
    if (MINI) { WAIT_VM(0); mb_arrive(S.lds, 1); mb_wait(S.lds, 1); } else STEP_SYNC();
    c_read<NC>(S, s + 1, xn, wn);
  }
#pragma unroll
  for (int c = 0; c < NC; ++c)
#pragma unroll
    for (int r = 0; r < 4; ++r) acc[r][c] = mfma(wc[c], xc[r], acc[r][c]);
  if (ROLE) {
    constexpr int C0 = ROLE == 1 ? 0 : NC - 2, C1 = ROLE == 1 ? NC - 2 : NC;
#pragma unroll
    for (int r = 0; r < 4; ++r) {
      s1[r] = mfma(ones, xc[r], s1[r]);
      s2[r] = mfma(xc[r], xc[r], s2[r]);
    }
#pragma unroll
    for (int c = C0; c < C1; ++c) cs[c - C0] = mfma(wc[c], ones, cs[c - C0]);
  }
  if (READ) {
#pragma unroll
    for (int i = 0; i < 4 + 2 * NC; ++i) {
      __builtin_amdgcn_sched_group_barrier(0x8, 1, 0);
      __builtin_amdgcn_sched_group_barrier(0x100, 1, 0);
    }
  }
  __builtin_amdgcn_sched_barrier(0);
}
template <int NC, int ROLE>
DEVI void c_main(K1C& S, f32x4 (&acc)[4][5], f32x4 (&cs)[3], f32x4 (&s1)[4], f32x4 (&s2)[4]) {
  u32x4 xa[4], wa[5], xb[4], wb[5];
  u32x4 ones = {0x3f803f80u, 0x3f803f80u, 0x3f803f80u, 0x3f803f80u};
  asm volatile("" : "+v"(ones));
  c_read<NC>(S, 0, xa, wa);
  c_step<NC, ROLE, true, true>(S, 0, xa, wa, xb, wb, acc, cs, s1, s2, ones);
#pragma unroll 1
  for (int s = 1; s < 19; s += 2) {
    c_step<NC, ROLE, true>(S, s, xb, wb, xa, wa, acc, cs, s1, s2, ones);
    c_step<NC, ROLE, true>(S, s + 1, xa, wa, xb, wb, acc, cs, s1, s2, ones);
  }
  c_step<NC, ROLE, false>(S, 19, xb, wb, xa, wa, acc, cs, s1, s2, ones);
}

struct K1H {
  LDSP char* lds;
  unsigned ldsbase;
  unsigned vw[8], vx[4];
  const char* pw; const char* px;
  unsigned incw, wrapw, wrapx;
  unsigned dw, dx;
  unsigned wa[5], wr;
  __amdgpu_buffer_rsrc_t rs;
  const u16* w2; const u16* wo;
  int first;
  const char* pc2;
  DG(unsigned long long t1;)
};
template <int HP, int ODD>
DEVI void h_issue(K1H& S) {
#pragma unroll
  for (int i = 0; i < 8 - HP; ++i) glds16n(S.vw[i], S.pw, S.ldsbase + K1_W + HP * 8192 + i * 1024 + S.dw);
#pragma unroll
  for (int i = 0; i < 4; ++i) glds16n(S.vx[i], S.px, S.ldsbase + HP * 4096 + i * 1024 + S.dx);
  S.pw += S.incw;
  S.pw -= (unsigned)(size_t)S.pw == S.wrapw ? 18 * S.incw : 0u;
  S.px += ODD ? 128 - 64 * 1280 : 64 * 1280;
  S.px -= (unsigned)(size_t)S.px == S.wrapx ? 1152u : 0u;
  S.dw = S.dw == 4 * WSLOT ? 0 : S.dw + WSLOT;
  S.dx = (S.dx + 8192) & 0xffff;
}
template <int HP, int G> DEVI void h_wait() {
  constexpr int N = G * (12 - HP);
  asm volatile("s_waitcnt vmcnt(%0)" ::"i"(N) : "memory");
}
DEVI void h_read(K1H& S, u32x4 (&wf)[5]) {
  LDSP char* pw = S.lds + S.wr;
#pragma unroll
  for (int c = 0; c < 5; ++c) wf[c] = trread2(pw + S.wa[c], pw + S.wa[c] + 1280);
  S.wr = S.wr == 4 * WSLOT ? 0 : S.wr + WSLOT;
}
template <int HP, int G, int ISSUE>
DEVI void h_step(K1H& S, const u32x4 (&wc)[5], u32x4 (&wn)[5], f32x4 (&cs)[5], const u32x4& ones) {
  if (G >= 0 && G < 9) h_wait<HP, (G >= 0 && G < 9 ? G : 0)>();
  if (G >= 0) STEP_SYNC();
#pragma unroll
  for (int c = 0; c < 5; ++c) cs[c] = mfma(wc[c], ones, cs[c]);
  h_read(S, wn);
  __builtin_amdgcn_sched_barrier(0);
  if (ISSUE) h_issue<HP, ISSUE - 1>(S);
  __builtin_amdgcn_sched_barrier(0);
}
template <int HP>
DEVI void h_main(K1H& S, f32x4 (&cs)[5]) {
  u32x4 wa[5], wb[5];
  u32x4 ones = {0x3f803f80u, 0x3f803f80u, 0x3f803f80u, 0x3f803f80u};
  asm volatile("" : "+v"(ones));
  h_issue<HP, 1>(S);
  h_issue<HP, 0>(S);
  h_issue<HP, 1>(S);
  mb_wait(S.lds, 0);
  h_read(S, wa);
  mb_wait(S.lds, 1);
  h_step<HP, -1, 1>(S, wa, wb, cs, ones);
  h_step<HP, 3, 2>(S, wb, wa, cs, ones);
#pragma unroll 1
  for (int s = 2; s < 14; s += 2) {
    h_step<HP, 3, 1>(S, wa, wb, cs, ones);
    h_step<HP, 3, 2>(S, wb, wa, cs, ones);
  }
  h_step<HP, 3, 1>(S, wa, wb, cs, ones);
  constexpr int n = 12 - HP;
  const int lane = threadIdx.x & 63;
  h_step<HP, 3, 0>(S, wb, wa, cs, ones);
  if (S.pc2) {
    asm volatile("s_mov_b32 m0, %1\n\ts_nop 0\n\tglobal_load_lds_dword %0, off" ::"v"(S.pc2 + HP * 8192 + lane * 128), "s"(S.ldsbase + K1_SCR) : "memory");
  }
#pragma unroll
  for (int i = 0; i < 5; ++i) st16(S.rs, u32x4{~0u, ~0u, ~0u, ~0u}, lane * 16, OFF_OT + (blockIdx.x * 10 + HP * 5 + i) * 1024);
  asm volatile("s_waitcnt vmcnt(%0)" ::"i"(2 * n + 5) : "memory");
  h_step<HP, 9, 0>(S, wa, wb, cs, ones);
  asm volatile("s_waitcnt vmcnt(%0)" ::"i"(n + 5) : "memory");
  h_step<HP, 9, 0>(S, wb, wa, cs, ones);
  {
    const int rho = lane >> 1;
    const unsigned v2 = rho * 1280 + (lane & 1) * 16;
    const unsigned vo = (((rho & 12) << 1) | ((rho & 16) >> 2) | (rho & 3)) * 1280 + (lane & 1) * 16;
#pragma unroll
    for (int i = 0; i < 6; ++i) {
      const int gi = S.first + i, fid = gi < 2960 ? gi : 2959;
      const int cq = (fid >> 2) / 74, ks = (fid >> 2) - 74 * cq;
      const char* src = (ks < 54 ? (const char*)S.w2 + ks * 40960 : (const char*)S.wo + (ks - 54) * 40960) + (4 * cq + (fid & 3)) * 32;
      glds16n(ks < 54 ? v2 : vo, src, S.ldsbase + K1_RT + (HP * 6 + i) * 1024);
    }
  }
  WAIT_VM(11);
  h_step<HP, 9, 0>(S, wa, wb, cs, ones);
#pragma unroll
  for (int c = 0; c < 5; ++c) cs[c] = mfma(wb[c], ones, cs[c]);
  __builtin_amdgcn_s_setprio(0);
}

__global__ __launch_bounds__(512, 2) void hedgehog_k1(const u16* __restrict__ x, const u16* __restrict__ wqkv,
                                                      const u16* __restrict__ wphi, const u16* __restrict__ wo,
                                                      const u16* __restrict__ w1, const u16* __restrict__ b1,
                                                      const u16* __restrict__ w2, char* __restrict__ ws) {
  extern __shared__ __attribute__((aligned(16))) char smem[];
  LDSP char* lds = (LDSP char*)smem;
  const unsigned long long kpc = __builtin_amdgcn_s_getpc();
  const int tid = threadIdx.x, lane = tid & 63;
  const int wave = __builtin_amdgcn_readfirstlane(tid >> 6);
  const int b = blockIdx.x, xcd = b & 7, jx = b >> 3;
  const unsigned ldsbase = (unsigned)(size_t)lds;
  const bool helper = wave >= 6;
  DG(unsigned long long T[8]; T[0] = stamp(); T[7] = rstamp(); T[1] = T[6] = 0;)
  if (helper) {
    const char* pc = (const char*)(kpc & ~(unsigned long long)127) + min(((jx % 3) * 2 + (wave & 1)) * 8192 + lane * 128, CODE_BYTES);
    asm volatile("s_mov_b32 m0, %1\n\ts_nop 0\n\tglobal_load_lds_dword %0, off" ::"v"(pc), "s"(ldsbase + K1_SCR) : "memory");
  }
  if (tid == 0) *(LDSP u32x2*)(lds + K1_MB) = u32x2{0u, 0u};
  WAIT_LGKM0();
  asm volatile("" ::"s"(x), "s"(wqkv), "s"(wphi), "s"(wo), "s"(w1), "s"(b1), "s"(w2), "s"(ws));
  __builtin_amdgcn_s_barrier();
  const int mh = wave & 1;
  const int nj = wave < 4 ? wave >> 1 : 2;
  const int cj = helper ? 2 * mh : nj;
  const int g = lane >> 4, m = lane & 15, q4 = m >> 2, p4 = lane & 3;
  const int rb = 8 * (xcd >> 2) + (jx & 7);
  const int cgi = jx >> 3;
  const bool atype = cgi < 2;
  const int grp = 2 * (xcd & 3) + (cgi & 1);
  const u16* W = atype ? wqkv : w1;
  const int ldw = atype ? 1920 : 1728;
  const int c0 = atype ? 80 * grp : (grp < 6 ? 224 * grp : 1344 + 192 * (grp - 6));
  const int sub = atype ? 640 : (grp < 6 ? 80 : 64);
  const unsigned incw = 64 * ldw;
  const char* xrow = (const char*)x + (size_t)rb * 128 * 1280;
  const int t1 = 1 + 3 * (((jx & 7) + cgi) % 3);
  auto wvoff = [&](int id) {
    const int sj = id / 5, ss = id - 5 * sj;
    const int c = 64 * ss + lane, rho = c / 10, cc = c - 10 * rho;
    const int k = (rho & 0x13) | ((rho & 4) << 1) | ((rho & 8) >> 1);
    const bool narrow = !atype && (grp >= 6 || sj == 2);
    return (unsigned)(k * ldw + c0 + sub * sj + 8 * ((narrow && cc >= 8) ? cc - 2 : cc)) * 2;
  };
  auto xvoff = [&](int pc) {
    const int row = pc * 8 + (lane >> 3);
    return (unsigned)(row * 1280 + (((lane & 7) ^ ((row >> 1) & 7)) << 4));
  };
  const unsigned wa0 = K1_W + cj * WSUB + (16 * (g >> 1) + 4 * (g & 1) + q4) * 160 + p4 * (atype ? 40 : 8);
  const unsigned cstr = atype ? 8 : 32;
  const __amdgpu_buffer_rsrc_t rs = __builtin_amdgcn_make_buffer_rsrc((void*)ws, 0, WS_END, 0x00020000);
  f32x4 acc[4][5];

  if (!helper) {
    const unsigned x0 = xvoff(wave), x1 = xvoff(wave < 2 ? wave + 6 : 7);
    auto xgrp = [&](int u, int xt) {
      const char* px = xrow + xt * 128 + (u & 1) * (64 * 1280);
      const unsigned d = ldsbase + u * 8192 + wave * 1024;
      glds16n(x0, px, d);
      if (wave < 2) glds16n(x1, px, d + 6144);
    };
    xgrp(0, 0);
    xgrp(1, 0);
    const unsigned dw = ldsbase + K1_W + wave * 1024;
    const unsigned v0 = wvoff(wave);
    glds16n(v0, W, dw);
    const unsigned v1 = wvoff(wave + 6);
    glds16n(v1, W, dw + 6144);
    const unsigned v2 = wvoff(wave < 3 ? wave + 12 : 14);
    if (wave < 3) glds16n(v2, W, dw + 12288);
    glds16n(v0, (const char*)W + incw, dw + WSLOT);
    glds16n(v1, (const char*)W + incw, dw + WSLOT + 6144);
    if (wave < 3) glds16n(v2, (const char*)W + incw, dw + WSLOT + 12288);
    xgrp(2, t1);
    K1C S;
    S.lds = lds;
    S.xa = (mh * 64 + m) * 128 + ((g ^ (m >> 1)) << 4);
#pragma unroll
    for (int c = 0; c < 5; ++c) S.wa[c] = wa0 + c * cstr;
    S.wr = 0;
    DG(S.t1 = 0;)
    f32x4 cs[3], s1[4], s2[4];
#pragma unroll
    for (int i = 0; i < 4; ++i) {
      s1[i] = f32x4{0.f, 0.f, 0.f, 0.f}; s2[i] = s1[i]; cs[i % 3] = s1[i];
#pragma unroll
      for (int c = 0; c < 5; ++c) acc[i][c] = s1[i];
    }
    if (wave == 4 && lane < 48) *(LDSP u32x4*)(lds + K1_ZERO + lane * 16) = u32x4{0u, 0u, 0u, 0u};
    const bool five = atype || (grp < 6 && nj < 2);
    if (wave < 2) WAIT_VM(5); else if (wave == 2) WAIT_VM(4); else WAIT_VM(3);
    mb_arrive(lds, 0);
    {
      const int col = c0 + sub * (lane / 10) + 8 * (lane % 10);
      const unsigned vo = atype ? tid * 16 : (col > 1720 ? 1720 : col) * 2;
      if (tid < (atype ? 200 : 30)) glds16n(vo, atype ? (const char*)wphi + grp * 3200 : (const char*)b1, ldsbase + K1_WPHI + wave * 1024);
    }
    mb_wait(lds, 0);
    DG(T[1] = stamp();)
    if (nj == 1) {
      if (five) { if (mh == 0) c_main<5, 1>(S, acc, cs, s1, s2); else c_main<5, 2>(S, acc, cs, s1, s2); }
      else { if (mh == 0) c_main<4, 1>(S, acc, cs, s1, s2); else c_main<4, 2>(S, acc, cs, s1, s2); }
      const int cb = mh == 0 ? 0 : (five ? 3 : 2), cn = mh == 0 ? (five ? 3 : 2) : 2;
      if (m == 0) {
#pragma unroll
        for (int c = 0; c < 3; ++c)
          if (c < cn) *(LDSP f32x4*)(lds + K1_CS + (80 + (cb + c) * 16 + 4 * g) * 4) = cs[c];
      }
      if (g == q4) {
#pragma unroll
        for (int r = 0; r < 4; ++r) {
          const float sq = (m & 2) ? ((m & 1) ? s2[r][3] : s2[r][2]) : ((m & 1) ? s2[r][1] : s2[r][0]);
          const float mean = s1[r][0] * (1.0f / 640.0f);
          const float var = fmaxf(sq * (1.0f / 640.0f) - mean * mean, 0.f);
          float rsd = __builtin_amdgcn_rsqf(var + 1e-5f);
          if (!atype) rsd *= L2E;
          *(LDSP f32x2*)(lds + K1_STAT + (mh * 64 + r * 16 + m) * 8) = f32x2{rsd, -mean * rsd};
        }
      }
      if (atype) {
#pragma unroll
        for (int r = 2; r < 4; ++r)
#pragma unroll
          for (int c = 0; c < 5; ++c) *(LDSP f32x4*)(lds + K1_HO + (mh * 2 + r - 2) * 5120 + c * 1024 + lane * 16) = acc[r][c];
      }
    } else {
      if (five) c_main<5, 0>(S, acc, cs, s1, s2); else c_main<4, 0>(S, acc, cs, s1, s2);
      if (!atype) {
#pragma unroll
        for (int c = 0; c < 5; ++c) *(LDSP f32x4*)(lds + K1_HO + (mh * 2 + (nj >> 1)) * 5120 + c * 1024 + lane * 16) = acc[3][c];
        if (nj == 0 && tid < 120) {
          const unsigned w = *(LDSP unsigned*)(lds + K1_WPHI + tid * 4);
          *(LDSP f32x2*)(lds + K1_BL + tid * 8) = f32x2{bflo(w) * L2E, bfhi(w) * L2E};
        }
      }
    }
    DG(T[6] = S.t1;)
  } else {
    __builtin_amdgcn_s_setprio(3);
    if (mh == 0) {
      const char* p = lane < 12 ? ws + (size_t)lane * (2u << 20) : lane == 12 ? (const char*)w2 : lane == 13 ? (const char*)w2 + (2u << 20) : lane == 14 ? (const char*)wo : (const char*)wphi;
      if (lane < 16) asm volatile("s_mov_b32 m0, %1\n\ts_nop 0\n\tglobal_load_lds_dword %0, off" ::"v"(p), "s"(ldsbase + K1_SCR) : "memory");
    }
    K1H S;
    S.lds = lds; S.ldsbase = ldsbase;
    S.incw = incw;
    S.pw = (const char*)W + 2 * t1 * incw;
    S.wrapw = (unsigned)(size_t)W + 20 * incw;
    S.px = xrow + t1 * 128 + 64 * 1280;
    S.wrapx = (unsigned)(size_t)xrow + 1280;
    S.dw = 2 * WSLOT; S.dx = 3 * 8192; S.wr = 0;
    DG(S.t1 = 0;)
#pragma unroll
    for (int i = 0; i < 4; ++i) S.vx[i] = xvoff(4 * mh + i);
#pragma unroll
    for (int i = 0; i < 8; ++i) S.vw[i] = wvoff(8 * mh + i < 15 ? 8 * mh + i : 14);
#pragma unroll
    for (int c = 0; c < 5; ++c) { S.wa[c] = wa0 + c * cstr; acc[0][c] = f32x4{0.f, 0.f, 0.f, 0.f}; }
    S.pc2 = jx < 2 ? (const char*)(kpc & ~(unsigned long long)127) + CODE2_OFF : nullptr;
    S.rs = rs; S.w2 = w2; S.wo = wo; S.first = b * 12 + mh * 6;
    if (mh == 0) h_main<0>(S, acc[0]); else h_main<1>(S, acc[0]);
    DG(T[6] = S.t1;)
    if (m == 0) {
#pragma unroll
      for (int c = 0; c < 5; ++c) *(LDSP f32x4*)(lds + K1_CS + (cj * 80 + c * 16 + 4 * g) * 4) = acc[0][c];
    }
  }
  DG(T[2] = stamp();)
  WAIT_LGKM0();
  __builtin_amdgcn_s_barrier();
  asm volatile("" ::: "memory");
  DG(T[3] = stamp();)
#define DG_OUT1() DG({ T[5] = stamp(); unsigned long long re = rstamp(); if (lane == 0) { unsigned long long* dp = (unsigned long long*)(ws + OFF_DBG) + (size_t)(b * 8 + wave) * 16; for (int i = 0; i < 8; ++i) dp[i] = T[i]; dp[8] = re; } if (lane < 20) ((unsigned*)(ws + OFF_DBG + (2u << 20)))[(b * 8 + wave) * 32 + lane] = *(LDSP unsigned*)(lds + K1_LDS + wave * 128 + lane * 4) - (unsigned)T[0]; })
  auto side_store = [&]() {
    WAIT_VM(0);
    LDSP char* pr = lds + K1_RT + mh * 6144 + (4 * g + q4) * 32 + p4 * 8;
#pragma unroll
    for (int i = 0; i < 6; ++i) {
      const int gi = b * 12 + mh * 6 + i, cq = (gi >> 2) / 74, ks = (gi >> 2) - 74 * cq;
      const u32x4 v = trread2(pr + i * 1024, pr + i * 1024 + 512);
      if (gi < 2960) st16(rs, v, lane * 16, OFF_WT + ((4 * cq + (gi & 3)) * 74 + ks) * 1024);
    }
  };

  if (!atype) {
    auto round = [&](const f32x4 (&a)[5], int ej, int rt) {
      const bool five = grp < 6 && ej < 2;
      const f32x2 st = *(LDSP f32x2*)(lds + K1_STAT + (rt * 16 + m) * 8);
      u32x2 pk[5];
#pragma unroll
      for (int c = 0; c < 5; ++c) {
        if (c == 4 && !five) { pk[4] = u32x2{0u, 0u}; break; }
        const f32x4 cs = *(LDSP f32x4*)(lds + K1_CS + (ej * 80 + c * 16 + 4 * g) * 4);
        const f32x4 bl = *(LDSP f32x4*)(lds + K1_BL + (ej * 80 + c * 16 + 4 * g) * 4);
        float res[4];
#pragma unroll
        for (int e = 0; e < 4; ++e) {
          const float hl = fmaf(a[c][e], st[0], fmaf(st[1], cs[e], bl[e]));
          const float ex = __builtin_amdgcn_exp2f(hl);
          const float rc = __builtin_amdgcn_rcpf(fmaf(ex, ex + 2.f, 2.f));
          res[e] = hl * fmaf(rc, -2.f * LN2, LN2);
        }
        pk[c] = u32x2{pk2(res[0], res[1]), pk2(res[2], res[3])};
      }
      const int T0 = (c0 + sub * ej) >> 4;
      const unsigned base = OFF_ACT + ((rb * 8 + rt) * 54 + (T0 >> 1)) * 1024;
      if (!(T0 & 1)) {
        st16(rs, u32x4{pk[0][0], pk[0][1], pk[1][0], pk[1][1]}, lane * 16, base);
        st16(rs, u32x4{pk[2][0], pk[2][1], pk[3][0], pk[3][1]}, lane * 16, base + 1024);
        if (five) st8(rs, pk[4], lane * 16, base + 2048);
      } else {
        st8(rs, pk[0], lane * 16, base + 8);
        st16(rs, u32x4{pk[1][0], pk[1][1], pk[2][0], pk[2][1]}, lane * 16, base + 1024);
        st16(rs, u32x4{pk[3][0], pk[3][1], pk[4][0], pk[4][1]}, lane * 16, base + 2048);
      }
    };
    if (helper) {
#pragma unroll
      for (int r = 0; r < 2; ++r)
#pragma unroll
        for (int c = 0; c < 5; ++c) acc[r][c] = *(LDSP f32x4*)(lds + K1_HO + (mh * 2 + r) * 5120 + c * 1024 + lane * 16);
    }
#pragma unroll 1
    for (int it = 0; it < (helper ? 1 : 2); ++it) {
      round(acc[0], helper ? 0 : nj, mh * 4 + (helper ? 3 : 2 * it));
      if (it == 0 || nj == 1) round(acc[1], helper ? 2 : nj, mh * 4 + (helper ? 3 : 2 * it + 1));
#pragma unroll
      for (int c = 0; c < 5; ++c) { acc[0][c] = acc[2][c]; acc[1][c] = acc[3][c]; }
    }
    DG(T[4] = stamp();)
    if (helper) side_store();
    DG_OUT1();
    return;
  }

  auto normalise = [&](const f32x4 (&a)[5], const f32x4 (&cs)[5], int rt, u32x4 (&bq)[3], unsigned one) {
    const f32x2 st = *(LDSP f32x2*)(lds + K1_STAT + (rt * 16 + m) * 8);
#pragma unroll
    for (int c = 0; c < 5; ++c) {
      float v[4];
#pragma unroll
      for (int e = 0; e < 4; ++e) v[e] = fmaf(a[c][e], st[0], st[1] * cs[c][e]);
      bq[c >> 1][(c & 1) * 2] = pk2(v[0], v[1]);
      bq[c >> 1][(c & 1) * 2 + 1] = pk2(v[2], v[3]);
    }
    bq[2][2] = one;
    bq[2][3] = 0u;
  };
  f32x4 csv[5];
  if (!helper && nj != 1) {
#pragma unroll
    for (int c = 0; c < 5; ++c) csv[c] = *(LDSP f32x4*)(lds + K1_CS + (nj * 80 + c * 16 + 4 * g) * 4);
#pragma unroll
    for (int r = 0; r < 4; ++r) {
      u32x4 bq[3];
      normalise(acc[r], csv, mh * 4 + r, bq, nj == 2 ? 0x00003f80u : 0u);
      if (nj == 0) {
        const unsigned so = OFF_QT + (4 * grp * 128 + rb * 8 + mh * 4 + r) * 768;
#pragma unroll
        for (int i = 0; i < 3; ++i) st16(rs, bq[i], g * (128 * 768) + m * 16, so + i * 256);
      } else {
#pragma unroll
        for (int i = 0; i < 3; ++i) *(LDSP u32x4*)(lds + K1_VIMG + (mh * 64 + r * 16 + m) * V_PITCH + g * 48 + i * 16) = bq[i];
      }
    }
  }
  if (helper || nj != 2) {
    const int kr0 = helper ? 3 : (nj == 1 ? 0 : 2);
    if (kr0) {
#pragma unroll
      for (int c = 0; c < 5; ++c) acc[0][c] = *(LDSP f32x4*)(lds + K1_HO + (mh * 2 + kr0 - 2) * 5120 + c * 1024 + lane * 16);
    }
#pragma unroll
    for (int c = 0; c < 5; ++c) csv[c] = *(LDSP f32x4*)(lds + K1_CS + (80 + c * 16 + 4 * g) * 4);
    LDSP char* wl = lds + ((p4 == g) ? K1_WPHI + g * 800 + q4 * 40 : K1_ZERO);
#pragma unroll 1
    for (int kr = kr0; kr < (kr0 ? kr0 + 1 : 2); ++kr) {
      u32x4 bq[3];
      normalise(acc[0], csv, mh * 4 + kr, bq, 0u);
      f32x4 pk[5];
      head_proj(wl, bq, pk);
      unsigned ph[20];
      feature_map(pk, ph);
      LDSP char* dst = lds + (mh * 64 + kr * 16 + m) * PK_PITCH + g * 80;
#pragma unroll
      for (int i = 0; i < 5; ++i) *(LDSP u32x4*)(dst + i * 16) = u32x4{ph[4 * i], ph[4 * i + 1], ph[4 * i + 2], ph[4 * i + 3]};
#pragma unroll
      for (int c = 0; c < 5; ++c) acc[0][c] = acc[1][c];
    }
  }
  DG(T[4] = stamp();)
  WAIT_LGKM0();
  __builtin_amdgcn_s_barrier();
  asm volatile("" ::: "memory");
  if (!helper) {
#pragma unroll 1
    for (int job = wave; job < 12; job += 6) {
      const int hh = job & 3, ft = job >> 2;
      LDSP char* pa = lds + (4 * g + q4) * PK_PITCH + hh * 80 + ft * 32 + p4 * 8;
      LDSP char* pb = lds + K1_VIMG + (4 * g + q4) * V_PITCH + hh * 48 + p4 * 8;
      f32x4 d0 = {0.f, 0.f, 0.f, 0.f}, d1 = {0.f, 0.f, 0.f, 0.f};
#pragma unroll
      for (int kk = 0; kk < 4; ++kk) {
        u32x4 a = trread2(pa + (32 * kk) * PK_PITCH, pa + (32 * kk + 16) * PK_PITCH);
        u32x4 v0 = trread2(pb + (32 * kk) * V_PITCH, pb + (32 * kk + 16) * V_PITCH);
        u32x4 v1 = trread2(pb + (32 * kk) * V_PITCH + 32, pb + (32 * kk + 16) * V_PITCH + 32);
        d0 = mfma(a, v0, d0);
        d1 = mfma(a, v1, d1);
      }
      const unsigned so = OFF_KVP + ((4 * grp + hh) * 16 + rb) * KVP_B + 1344 * ft;
      st16(rs, __builtin_bit_cast(u32x4, d0), m * 64 + g * 16, so);
      if (m < 5) st16(rs, __builtin_bit_cast(u32x4, d1), (16 + m) * 64 + g * 16, so);
    }
  } else {
    side_store();
  }
  DG_OUT1();
}

constexpr unsigned K2_YD = 0;
constexpr unsigned K2_SD = 4 * 15360;
constexpr unsigned K2_KVT = K2_SD + 4 * 20480;
constexpr unsigned K2_ZERO = K2_KVT + 1920;
constexpr unsigned K2_WPHI = K2_ZERO + 1792;
constexpr unsigned K2_FLAG = K2_WPHI + 3200;
constexpr unsigned K2_OWN = K2_FLAG + 16;
constexpr unsigned K2_LDS = K2_OWN + 8192;

DEVI u32x4 bld(__amdgpu_buffer_rsrc_t rs, unsigned voff, unsigned soff) {
  return __builtin_amdgcn_raw_buffer_load_b128(rs, voff, soff, 0);
}
DEVI u32x4 bld_sc1(__amdgpu_buffer_rsrc_t rs, unsigned voff, unsigned soff) {
  return __builtin_amdgcn_raw_buffer_load_b128(rs, voff, soff, 16);
}
template <int N>
DEVI void y_stream(__amdgpu_buffer_rsrc_t rs, unsigned act, unsigned wt, int rot, f32x4 (&acc)[4][5]) {
  const unsigned l16 = (threadIdx.x & 63) * 16;
  unsigned ab[4];
#pragma unroll
  for (int l = 0; l < 4; ++l) ab[l] = act + ((l + rot) & 3) * (54 * 1024);
  u32x4 A[3][4], B[3][5];
  auto load = [&](int i) {
#pragma unroll
    for (int l = 0; l < 4; ++l) A[i % 3][l] = bld(rs, l16, ab[l] + i * 1024);
#pragma unroll
    for (int c = 0; c < 5; ++c) B[i % 3][c] = bld(rs, l16, wt + c * (74 * 1024) + i * 1024);
  };
  load(0);
  load(1);
#pragma unroll
  for (int i = 0; i < N; ++i) {
    if (i + 2 < N) load(i + 2);
    __builtin_amdgcn_sched_barrier(0);
#pragma unroll
    for (int l = 0; l < 4; ++l)
#pragma unroll
      for (int c = 0; c < 5; ++c) acc[l][c] = mfma(B[i % 3][c], A[i % 3][l], acc[l][c]);
    __builtin_amdgcn_sched_barrier(0);
  }
}

template <int C0, int C1>
DEVI void combine(LDSP char* lds, const u32x2 (&xv)[3], u16* out, int rt, int rb2, int cb, const f32x4 (&own)[5]) {
  const int lane = threadIdx.x & 63;
  const unsigned l16 = lane * 16;
  const size_t orow = (size_t)(64 * rb2 + 16 * rt + (lane & 15)) * 640 + 80 * cb + 4 * (lane >> 4);
  WAIT_LGKM0();
  __builtin_amdgcn_s_barrier();
  asm volatile("" ::: "memory");
#pragma unroll
  for (int c = C0; c < C1; ++c) {
    f32x4 y = C0 == 0 ? own[c] : *(LDSP f32x4*)(lds + K2_OWN + (rt * 2 + c - 3) * 1024 + l16);
#pragma unroll
    for (int l = 1; l < 4; ++l)
      y += *(LDSP f32x4*)(lds + K2_YD + ((rt - l) & 3) * 15360 + ((l - 1) * 5 + c) * 1024 + l16);
    LDSP f32x4* sp = (LDSP f32x4*)(lds + K2_SD + (rt * 5 + c) * 1024 + l16);
    const f32x4 s = sp[0] + sp[1280];
    y += sp[2560] + sp[3840];
    float o[4];
#pragma unroll
    for (int e = 0; e < 4; ++e) {
      const float gate = y[e] * __builtin_amdgcn_rcpf(1.f + __builtin_amdgcn_exp2f(-y[e] * L2E));
      const unsigned xw = xv[c - C0][e >> 1];
      o[e] = fmaf(s[e], gate, (e & 1) ? bfhi(xw) : bflo(xw));
    }
    *(u32x2*)(out + orow + 16 * c) = u32x2{pk2(o[0], o[1]), pk2(o[2], o[3])};
  }
}

__global__ __launch_bounds__(512, 2) void hedgehog_k2(const u16* __restrict__ x, const u16* __restrict__ wphi,
                                                      char* __restrict__ ws, u16* __restrict__ out) {
  extern __shared__ __attribute__((aligned(16))) char smem[];
  LDSP char* lds = (LDSP char*)smem;
  const int tid = threadIdx.x, lane = tid & 63;
  const int wave = __builtin_amdgcn_readfirstlane(tid >> 6);
  const int g = lane >> 4, m = lane & 15, q4 = m >> 2, p4 = lane & 3;
  const int b = blockIdx.x, xcd = b & 7, jx = b >> 3;
  const int rb2 = 8 * (xcd >> 1) + (jx & 7), cb = 4 * (xcd & 1) + (jx >> 3);
  const __amdgpu_buffer_rsrc_t rs = __builtin_amdgcn_make_buffer_rsrc((void*)ws, 0, WS_END, 0x00020000);
  const unsigned l16 = lane * 16;
  DG(unsigned long long T[8]; T[0] = stamp(); T[7] = rstamp(); T[1] = T[2] = T[3] = T[4] = T[5] = T[6] = 0;)
#define DG_OUT2() DG({ unsigned long long re = rstamp(); if (lane == 0) { unsigned long long* dp = (unsigned long long*)(ws + OFF_DBG + (1u << 20)) + (size_t)(b * 8 + wave) * 16; for (int i = 0; i < 8; ++i) dp[i] = T[i]; dp[8] = re; } })
  asm volatile("" ::"s"(x), "s"(wphi), "s"(out));
  if (wave == 0) {
    const unsigned long long pc = __builtin_amdgcn_s_getpc();
    const char* p = lane < 62 ? (const char*)(pc & ~(unsigned long long)127) + lane * 256 : (const char*)out + (lane - 62) * (2u << 20);
    asm volatile("s_mov_b32 m0, %1\n\ts_nop 0\n\tglobal_load_lds_dword %0, off" ::"v"(p), "s"((unsigned)(size_t)lds + K2_OWN) : "memory");
  }
  const unsigned act = OFF_ACT + 4 * rb2 * (54 * 1024), wt = OFF_WT + 5 * cb * (74 * 1024);
  const u16* xt = x + (size_t)(64 * rb2 + 16 * (wave & 3) + m) * 640 + 80 * cb + 4 * g;
  f32x4 acc[4][5];
#pragma unroll
  for (int r = 0; r < 4; ++r)
#pragma unroll
    for (int c = 0; c < 5; ++c) acc[r][c] = f32x4{0.f, 0.f, 0.f, 0.f};

  if (wave < 4) {
    __builtin_amdgcn_s_barrier();
    DG(T[1] = stamp();)
    u32x2 xv[3];
#pragma unroll
    for (int c = 0; c < 3; ++c) xv[c] = *(const u32x2*)(xt + 16 * c);
    y_stream<K2_NY>(rs, act + wave * (K2_NY * 1024), wt + wave * (K2_NY * 1024), wave, acc);
    DG(T[3] = stamp();)
#pragma unroll
    for (int l = 1; l < 4; ++l)
#pragma unroll
      for (int c = 0; c < 5; ++c)
        *(LDSP f32x4*)(lds + K2_YD + wave * 15360 + ((l - 1) * 5 + c) * 1024 + l16) = acc[l][c];
#pragma unroll
    for (int c = 3; c < 5; ++c) *(LDSP f32x4*)(lds + K2_OWN + (wave * 2 + c - 3) * 1024 + l16) = acc[0][c];
    DG(T[4] = stamp();)
    combine<0, 3>(lds, xv, out, wave, rb2, cb, acc[0]);
    DG(T[6] = stamp();)
    DG_OUT2();
    return;
  }

  const int aw = wave - 4;
  const int h = 4 * xcd + (jx & 3), sb = jx >> 2;
  const int tok = sb * 256 + aw * 64 + lane;
  u32x4 wv = *(const u32x4*)(wphi + h * 400 + (lane < 50 ? lane : 49) * 8);
  u32x4 bq[3];
#pragma unroll
  for (int s = 0; s < 3; ++s) bq[s] = bld(rs, (unsigned)(tok >> 4) * 768 + m * 16, OFF_QT + h * (128 * 768) + s * 256);
  int item = aw * 53 + (lane < 53 ? lane : 52);
  item = item < 210 ? item : 209;
  const int d = item / 10, fq = item - 10 * d;
  u32x4 part[16];
#pragma unroll
  for (int r = 0; r < 16; ++r) part[r] = bld(rs, (unsigned)((fq >> 2) * 1344 + d * 64 + (fq & 3) * 16), OFF_KVP + (h * 16 + r) * KVP_B);
  if (aw == 0) {
    *(LDSP u32x4*)(lds + K2_ZERO + l16) = u32x4{0u, 0u, 0u, 0u};
    if (lane < 48) *(LDSP u32x4*)(lds + K2_ZERO + 1024 + l16) = u32x4{0u, 0u, 0u, 0u};
    if (lane == 0) *(LDSP u32x4*)(lds + K2_FLAG) = u32x4{0u, 0u, 0u, 0u};
  }
  WAIT_LGKM0();
  __builtin_amdgcn_s_barrier();
  __builtin_amdgcn_s_setprio(2);
  *(LDSP u32x4*)(lds + K2_WPHI + aw * 800 + (lane < 50 ? lane : 49) * 16) = wv;
  unsigned ph[20];
  {
    LDSP char* wl = lds + ((p4 == g) ? K2_WPHI + aw * 800 + q4 * 40 : K2_ZERO);
    f32x4 pq[5];
    head_proj(wl, bq, pq);
    feature_map(pq, ph);
#pragma unroll
    for (int i = 0; i < 20; ++i) asm volatile("" : "+v"(ph[i]));
  }
  DG(T[1] = stamp();)
  {
    f32x4 sum = __builtin_bit_cast(f32x4, part[0]);
#pragma unroll
    for (int r = 1; r < 16; ++r) sum += __builtin_bit_cast(f32x4, part[r]);
    *(LDSP u32x2*)(lds + K2_KVT + d * 80 + fq * 8) = u32x2{pk2(sum[0], sum[1]), pk2(sum[2], sum[3])};
    WAIT_LGKM0();
    if (lane == 0) *(volatile LDSP unsigned*)(lds + K2_FLAG + aw * 4) = 1u;
    for (int spin = 0; spin < (1 << 22); ++spin) {
      const u32x4 f = *(volatile LDSP u32x4*)(lds + K2_FLAG);
      if (__builtin_amdgcn_readfirstlane(f[0] & f[1] & f[2] & f[3])) break;
      __builtin_amdgcn_s_sleep(1);
    }
    asm volatile("" ::: "memory");
  }
  DG(T[2] = stamp();)
  {
    LDSP char* kl = lds + (((m >> 2) == g) ? K2_KVT + (m & 3) * 80 : K2_ZERO);
    f32x4 nd[6];
#pragma unroll
    for (int t = 0; t < 6; ++t) {
      f32x4 c = {0.f, 0.f, 0.f, 0.f};
#pragma unroll
      for (int s = 0; s < 5; ++s)
        c = mfma(*(LDSP u32x4*)(kl + t * 320 + s * 16), u32x4{ph[4 * s], ph[4 * s + 1], ph[4 * s + 2], ph[4 * s + 3]}, c);
      nd[t] = c;
    }
    const float rd = __builtin_amdgcn_rcpf(nd[5][0] + 1e-6f);
    unsigned ow[10];
#pragma unroll
    for (int t = 0; t < 5; ++t) {
      ow[2 * t] = min(pk2(nd[t][0] * rd, nd[t][1] * rd), 0xFFFFFFFEu);
      ow[2 * t + 1] = min(pk2(nd[t][2] * rd, nd[t][3] * rd), 0xFFFFFFFEu);
    }
    const int e0 = (5 * h + 1) >> 1, e8 = (h & 1) ? e0 - 1 : e0 + 2;
    const unsigned vo = (unsigned)(tok >> 4) * 20480 + m * 16;
    const bool odd = h & 1;
    st16(rs, odd ? u32x4{ow[2], ow[3], ow[4], ow[5]} : u32x4{ow[0], ow[1], ow[2], ow[3]}, vo, OFF_OT + e0 * 256);
    st16(rs, odd ? u32x4{ow[6], ow[7], ow[8], ow[9]} : u32x4{ow[4], ow[5], ow[6], ow[7]}, vo, OFF_OT + (e0 + 1) * 256);
    st8(rs, odd ? u32x2{ow[0], ow[1]} : u32x2{ow[8], ow[9]}, vo, OFF_OT + e8 * 256 + (h & 1) * 8);
  }
  DG(T[3] = stamp();)
  const unsigned ring = K2_SD + aw * 20480;
  u32x2 xv[3];
#pragma unroll
  for (int c = 0; c < 2; ++c) xv[c] = *(const u32x2*)(xt + 48 + 16 * c);
  xv[2] = xv[1];
  if (aw >= 2) {
    __builtin_amdgcn_s_setprio(0);
    constexpr int NA = (54 - 4 * K2_NY) / 2;
    y_stream<NA>(rs, act + (4 * K2_NY + NA * (aw - 2)) * 1024, wt + (4 * K2_NY + NA * (aw - 2)) * 1024, 0, acc);
  } else {
    u32x4 B[3][5];
    auto loadb = [&](int i) {
#pragma unroll
      for (int c = 0; c < 5; ++c) B[i % 3][c] = bld(rs, l16, wt + c * (74 * 1024) + (54 + 10 * aw + i) * 1024);
    };
    loadb(0);
    loadb(1);
    const unsigned ob = OFF_OT + (4 * rb2 * 20 + 10 * aw) * 1024;
    {
      const int l2 = lane < 40 ? lane : 39;
      const unsigned pv = 3 * 20480 + l2 * 256 + 15 * 16 + 4;
      for (int spin = 0; spin < (1 << 20); ++spin) {
        asm volatile("" ::: "memory");
        const u32x2 w = __builtin_amdgcn_raw_buffer_load_b64(rs, pv, ob, 16);
        if (!__any(w[0] == 0xFFFFFFFFu || w[1] == 0xFFFFFFFFu)) break;
        __builtin_amdgcn_s_sleep(4);
      }
    }
    DG(T[4] = stamp();)
#pragma unroll
    for (int half = 0; half < 2; ++half) {
      u32x4 A[5][4];
      for (int spin = 0; spin < (1 << 16); ++spin) {
        asm volatile("" ::: "memory");
        unsigned mx = 0;
#pragma unroll
        for (int i = 0; i < 5; ++i)
#pragma unroll
          for (int l = 0; l < 4; ++l) A[i][l] = bld_sc1(rs, l16, ob + l * 20480 + (5 * half + i) * 1024);
#pragma unroll
        for (int i = 0; i < 5; ++i)
#pragma unroll
          for (int l = 0; l < 4; ++l) mx = max(max(mx, max(A[i][l][0], A[i][l][1])), max(A[i][l][2], A[i][l][3]));
        if (!__any(mx == 0xFFFFFFFFu)) break;
        __builtin_amdgcn_s_sleep(4);
      }
      DG(if (half == 0) T[5] = stamp();)
#pragma unroll
      for (int i = 0; i < 5; ++i) {
        const int k = 5 * half + i;
        if (k + 2 < 10) loadb(k + 2);
        __builtin_amdgcn_sched_barrier(0);
#pragma unroll
        for (int l = 0; l < 4; ++l)
#pragma unroll
          for (int c = 0; c < 5; ++c) acc[l][c] = mfma(B[k % 3][c], A[i][l], acc[l][c]);
        __builtin_amdgcn_sched_barrier(0);
      }
    }
  }
#pragma unroll
  for (int l = 0; l < 4; ++l)
#pragma unroll
    for (int c = 0; c < 5; ++c) *(LDSP f32x4*)(lds + ring + (l * 5 + c) * 1024 + l16) = acc[l][c];
  DG(T[6] = stamp();)
  combine<3, 5>(lds, xv, out, aw, rb2, cb, acc[0]);
  DG_OUT2();
}

extern "C" void kernel_launch(void* const* d_in, const int* in_sizes, int n_in, void* d_out, int out_size, void* d_ws,
                              size_t ws_size, hipStream_t stream) {
  const u16* x = (const u16*)d_in[0];
  const u16* wqkv = (const u16*)d_in[1];
  const u16* wphi = (const u16*)d_in[2];
  const u16* wo = (const u16*)d_in[3];
  const u16* w1 = (const u16*)d_in[5];
  const u16* b1 = (const u16*)d_in[6];
  const u16* w2 = (const u16*)d_in[7];
  (void)hipFuncSetAttribute((const void*)hedgehog_k1, hipFuncAttributeMaxDynamicSharedMemorySize, K1_LDS DG(+ 1024));
  (void)hipFuncSetAttribute((const void*)hedgehog_k2, hipFuncAttributeMaxDynamicSharedMemorySize, K2_LDS);
  hedgehog_k1<<<256, 512, K1_LDS DG(+ 1024), stream>>>(x, wqkv, wphi, wo, w1, b1, w2, (char*)d_ws);
  hedgehog_k2<<<256, 512, K2_LDS, stream>>>(x, wphi, (char*)d_ws, (u16*)d_out);
}
```

```cpp
#define ROT3
#include <hip/hip_runtime.h>
#include <stdint.h>

typedef unsigned short u16;
typedef __attribute__((ext_vector_type(2))) __bf16 bf16x2;
typedef __attribute__((ext_vector_type(8))) __bf16 bf16x8;
typedef __attribute__((ext_vector_type(4))) short s16x4;
typedef __attribute__((ext_vector_type(2))) float f32x2;
typedef __attribute__((ext_vector_type(4))) float f32x4;
typedef __attribute__((ext_vector_type(2))) unsigned u32x2;
typedef __attribute__((ext_vector_type(4))) unsigned u32x4;
#define LDSP __attribute__((address_space(3)))
#define DEVI __device__ __forceinline__

constexpr unsigned OFF_ACT = 0;
constexpr unsigned OFF_WT = OFF_ACT + 128u * 54u * 1024u;
constexpr unsigned OFF_OT = OFF_WT + 40u * 74u * 1024u;
constexpr unsigned OFF_QT = OFF_OT + 128u * 20u * 1024u;
constexpr unsigned OFF_KVP = OFF_QT + 32u * 2048u * 48u;
constexpr unsigned KVP_B = 3 * 1344;
constexpr unsigned OFF_DUMMY = OFF_KVP + 32u * 16u * KVP_B;
constexpr unsigned WS_END = OFF_DUMMY + 640u * 1024u;

#define CODE_BYTES (46 * 1024)
#define CODE2_OFF (36 * 1024)
#define K2_NY 11
#define K1_MAP 1
constexpr float L2E = 1.4426950408889634f, LN2 = 0.6931471805599453f;

DEVI unsigned pk2(float a, float b) {
  bf16x2 v = {(__bf16)a, (__bf16)b};
  return __builtin_bit_cast(unsigned, v);
}
DEVI float bflo(unsigned u) { return __uint_as_float(u << 16); }
DEVI float bfhi(unsigned u) { return __uint_as_float(u & 0xffff0000u); }
DEVI f32x4 mfma(u32x4 a, u32x4 b, f32x4 c) {
  return __builtin_amdgcn_mfma_f32_16x16x32_bf16(__builtin_bit_cast(bf16x8, a), __builtin_bit_cast(bf16x8, b), c, 0, 0, 0);
}
DEVI u32x2 trread(LDSP char* p) {
  return __builtin_bit_cast(u32x2, __builtin_amdgcn_ds_read_tr16_b64_v4i16((LDSP s16x4*)p));
}
DEVI u32x4 trread2(LDSP char* p0, LDSP char* p1) {
  u32x2 a = trread(p0), b = trread(p1);
  return u32x4{a[0], a[1], b[0], b[1]};
}
DEVI float dot2(unsigned a, unsigned b, float c) {
  return __builtin_amdgcn_fdot2_f32_bf16(__builtin_bit_cast(bf16x2, a), __builtin_bit_cast(bf16x2, b), c, false);
}
DEVI float dot_ones(u32x4 a, float c) {
  c = dot2(a[0], 0x3f803f80u, c); c = dot2(a[1], 0x3f803f80u, c);
  c = dot2(a[2], 0x3f803f80u, c); c = dot2(a[3], 0x3f803f80u, c);
  return c;
}
DEVI float dot_self(u32x4 a, float c) {
  c = dot2(a[0], a[0], c); c = dot2(a[1], a[1], c); c = dot2(a[2], a[2], c); c = dot2(a[3], a[3], c);
  return c;
}
DEVI float red_g(float v) {
  v += __shfl_xor(v, 16);
  v += __shfl_xor(v, 32);
  return v;
}
DEVI void glds16(unsigned voff, const void* sbase, unsigned lds_dst) {
  unsigned keep;
  asm volatile("s_mov_b32 %0, m0\n\ts_mov_b32 m0, %3\n\ts_nop 0\n\tglobal_load_lds_dwordx4 %1, %2\n\ts_mov_b32 m0, %0"
               : "=&s"(keep) : "v"(voff), "s"(sbase), "s"(lds_dst) : "memory");
}
#define DG(...)
#define WAIT_VM(n) asm volatile("s_waitcnt vmcnt(" #n ")" ::: "memory")
#define WAIT_LGKM0() asm volatile("s_waitcnt lgkmcnt(0)" ::: "memory")

DEVI void feature_map(const f32x4 (&p)[5], unsigned (&o)[20]) {
  float mx = p[0][0], mn = p[0][0];
#pragma unroll
  for (int t = 0; t < 5; ++t)
#pragma unroll
    for (int r = 0; r < 4; ++r) { mx = fmaxf(mx, p[t][r]); mn = fminf(mn, p[t][r]); }
  const float a = -mx * L2E, b = mn * L2E;
  float e1[20], e2[20], s1 = 0.f, s2 = 0.f;
#pragma unroll
  for (int t = 0; t < 5; ++t)
#pragma unroll
    for (int r = 0; r < 4; ++r) {
      e1[4 * t + r] = __builtin_amdgcn_exp2f(fmaf(p[t][r], L2E, a));
      e2[4 * t + r] = __builtin_amdgcn_exp2f(fmaf(p[t][r], -L2E, b));
      s1 += e1[4 * t + r]; s2 += e2[4 * t + r];
    }
  const float r1 = __builtin_amdgcn_rcpf(s1), r2 = __builtin_amdgcn_rcpf(s2);
#pragma unroll
  for (int i = 0; i < 10; ++i) {
    o[i] = pk2(e1[2 * i] * r1, e1[2 * i + 1] * r1);
    o[10 + i] = pk2(e2[2 * i] * r2, e2[2 * i + 1] * r2);
  }
}
DEVI void head_proj(LDSP char* wl, const u32x4 (&b)[3], f32x4 (&p)[5]) {
#pragma unroll
  for (int t = 0; t < 5; ++t) {
    u32x4 a0 = trread2(wl + 0 * 40 + t * 8, wl + 4 * 40 + t * 8);
    u32x4 a1 = trread2(wl + 8 * 40 + t * 8, wl + 12 * 40 + t * 8);
    u32x2 h2 = trread(wl + 16 * 40 + t * 8);
    u32x4 a2 = {h2[0], h2[1], 0u, 0u};
    f32x4 c = {0.f, 0.f, 0.f, 0.f};
    c = mfma(a0, b[0], c);
    c = mfma(a1, b[1], c);
    c = mfma(a2, b[2], c);
    p[t] = c;
  }
}

constexpr unsigned XSLOT = 16384, WSUB = 5120, WSLOT = 3 * WSUB;
constexpr unsigned K1_W = 4 * XSLOT;
constexpr unsigned K1_WPHI = K1_W + 5 * WSLOT;
constexpr unsigned K1_ZERO = K1_WPHI + 3200;
constexpr unsigned K1_STAT = K1_ZERO + 768;
constexpr unsigned K1_CS = K1_STAT + 1024;
constexpr unsigned K1_BL = K1_CS + 960;
constexpr unsigned K1_SCR = K1_BL + 960;
constexpr unsigned K1_MB = K1_SCR + 256;
constexpr unsigned K1_LDS = K1_MB + 16;
constexpr unsigned PK_PITCH = 352, V_PITCH = 224, K1_VIMG = 128 * PK_PITCH;
constexpr unsigned K1_HO = K1_VIMG + 128 * V_PITCH;
constexpr unsigned K1_RT = K1_W + 2 * WSLOT;

DEVI void glds16n(unsigned voff, const void* sbase, unsigned lds_dst) {
  asm volatile("s_mov_b32 m0, %2\n\ts_nop 0\n\tglobal_load_lds_dwordx4 %0, %1" ::"v"(voff), "s"(sbase), "s"(lds_dst) : "memory");
}
DEVI void st16(__amdgpu_buffer_rsrc_t rs, u32x4 v, unsigned voff, unsigned soff) {
  __builtin_amdgcn_raw_buffer_store_b128(v, rs, voff + soff, 0, 16);
}
DEVI void st8(__amdgpu_buffer_rsrc_t rs, u32x2 v, unsigned voff, unsigned soff) {
  __builtin_amdgcn_raw_buffer_store_b64(v, rs, voff + soff, 0, 16);
}
#define STEP_SYNC() do { __builtin_amdgcn_s_waitcnt(0xC07F); __builtin_amdgcn_s_barrier(); __builtin_amdgcn_sched_barrier(0); } while (0)

DEVI void mb_arrive(LDSP char* lds, int i) {
  WAIT_LGKM0();
  if ((threadIdx.x & 63) == 0) __hip_atomic_fetch_add((LDSP unsigned*)(lds + K1_MB + 4 * i), 1u, __ATOMIC_RELAXED, __HIP_MEMORY_SCOPE_WORKGROUP);
}
DEVI void mb_wait(LDSP char* lds, int i) {
  for (int spin = 0; spin < (1 << 22); ++spin) {
    if (__builtin_amdgcn_readfirstlane(*(volatile LDSP unsigned*)(lds + K1_MB + 4 * i)) >= 6u) break;
    __builtin_amdgcn_s_sleep(1);
  }
  asm volatile("" ::: "memory");
  __builtin_amdgcn_sched_barrier(0);
}
struct K1C {
  LDSP char* lds;
  unsigned xa, wa[5], wr;
  DG(unsigned long long t1;)
};
template <int NC>
DEVI void c_read(K1C& S, int s, u32x4 (&xf)[4], u32x4 (&wf)[5]) {
  LDSP char* px = S.lds + ((s >> 1) & 3) * XSLOT + (S.xa ^ ((s & 1) << 6));
  LDSP char* pw = S.lds + S.wr;
  xf[0] = *(LDSP u32x4*)(px);
  wf[0] = trread2(pw + S.wa[0], pw + S.wa[0] + 1280);
#pragma unroll
  for (int r = 1; r < 4; ++r) xf[r] = *(LDSP u32x4*)(px + r * 2048);
#pragma unroll
  for (int c = 1; c < NC; ++c) wf[c] = trread2(pw + S.wa[c], pw + S.wa[c] + 1280);
  S.wr = S.wr == 4 * WSLOT ? 0 : S.wr + WSLOT;
}
template <int NC, int ROLE, bool READ, bool MINI = false>
DEVI void c_step(K1C& S, int s, const u32x4 (&xc)[4], const u32x4 (&wc)[5], u32x4 (&xn)[4], u32x4 (&wn)[5], f32x4 (&acc)[4][5],
                 f32x4 (&cs)[3], f32x4 (&s1)[4], f32x4 (&s2)[4], const u32x4& ones) {
  if (READ) {
    if (MINI) { WAIT_VM(0); mb_arrive(S.lds, 1); mb_wait(S.lds, 1); } else STEP_SYNC();
    c_read<NC>(S, s + 1, xn, wn);
  }
#pragma unroll
  for (int c = 0; c < NC; ++c)
#pragma unroll
    for (int r = 0; r < 4; ++r) acc[r][c] = mfma(wc[c], xc[r], acc[r][c]);
  if (ROLE) {
    constexpr int C0 = ROLE == 1 ? 0 : NC - 2, C1 = ROLE == 1 ? NC - 2 : NC;
#pragma unroll
    for (int r = 0; r < 4; ++r) {
      s1[r] = mfma(ones, xc[r], s1[r]);
      s2[r] = mfma(xc[r], xc[r], s2[r]);
    }
#pragma unroll
    for (int c = C0; c < C1; ++c) cs[c - C0] = mfma(wc[c], ones, cs[c - C0]);
  }
  if (READ) {
#pragma unroll
    for (int i = 0; i < 4 + 2 * NC; ++i) {
      __builtin_amdgcn_sched_group_barrier(0x8, 1, 0);
      __builtin_amdgcn_sched_group_barrier(0x100, 1, 0);
    }
  }
  __builtin_amdgcn_sched_barrier(0);
}
template <int NC, int ROLE>
DEVI void c_main(K1C& S, f32x4 (&acc)[4][5], f32x4 (&cs)[3], f32x4 (&s1)[4], f32x4 (&s2)[4]) {
  u32x4 xa[4], wa[5], xb[4], wb[5];
  u32x4 ones = {0x3f803f80u, 0x3f803f80u, 0x3f803f80u, 0x3f803f80u};
  asm volatile("" : "+v"(ones));
  c_read<NC>(S, 0, xa, wa);
  c_step<NC, ROLE, true, true>(S, 0, xa, wa, xb, wb, acc, cs, s1, s2, ones);
#pragma unroll 1
  for (int s = 1; s < 19; s += 2) {
    c_step<NC, ROLE, true>(S, s, xb, wb, xa, wa, acc, cs, s1, s2, ones);
    c_step<NC, ROLE, true>(S, s + 1, xa, wa, xb, wb, acc, cs, s1, s2, ones);
  }
  c_step<NC, ROLE, false>(S, 19, xb, wb, xa, wa, acc, cs, s1, s2, ones);
}

struct K1H {
  LDSP char* lds;
  unsigned ldsbase;
  unsigned vw[8], vx[4];
  const char* pw; const char* px;
  unsigned incw, wrapw, wrapx;
  unsigned dw, dx;
  unsigned wa[5], wr;
  __amdgpu_buffer_rsrc_t rs;
  const u16* w2; const u16* wo;
  int first;
  const char* pc2;
  const char* touch;
  DG(unsigned long long t1;)
};
template <int HP, int ODD, bool SKIP = false>
DEVI void h_issue(K1H& S) {
  if (!SKIP) {
#pragma unroll
    for (int i = 0; i < 8 - HP; ++i) glds16n(S.vw[i], S.pw, S.ldsbase + K1_W + HP * 8192 + i * 1024 + S.dw);
#pragma unroll
    for (int i = 0; i < 4; ++i) glds16n(S.vx[i], S.px, S.ldsbase + HP * 4096 + i * 1024 + S.dx);
  }
  S.pw += S.incw;
  S.pw -= (unsigned)(size_t)S.pw == S.wrapw ? 18 * S.incw : 0u;
  S.px += ODD ? 128 - 64 * 1280 : 64 * 1280;
  S.px -= (unsigned)(size_t)S.px == S.wrapx ? 1152u : 0u;
  S.dw = S.dw == 4 * WSLOT ? 0 : S.dw + WSLOT;
  S.dx = (S.dx + 8192) & 0xffff;
}
template <int HP, int G> DEVI void h_wait() {
  constexpr int N = G * (12 - HP);
  asm volatile("s_waitcnt vmcnt(%0)" ::"i"(N) : "memory");
}
DEVI void h_read(K1H& S, u32x4 (&wf)[5]) {
  LDSP char* pw = S.lds + S.wr;
#pragma unroll
  for (int c = 0; c < 5; ++c) wf[c] = trread2(pw + S.wa[c], pw + S.wa[c] + 1280);
  S.wr = S.wr == 4 * WSLOT ? 0 : S.wr + WSLOT;
}
template <int HP, int G, int ISSUE>
DEVI void h_step(K1H& S, const u32x4 (&wc)[5], u32x4 (&wn)[5], f32x4 (&cs)[5], const u32x4& ones) {
  if (G >= 0 && G < 9) h_wait<HP, (G >= 0 && G < 9 ? G : 0)>();
  if (G >= 0) STEP_SYNC();
#pragma unroll
  for (int c = 0; c < 5; ++c) cs[c] = mfma(wc[c], ones, cs[c]);
  h_read(S, wn);
  __builtin_amdgcn_sched_barrier(0);
  if (ISSUE) h_issue<HP, ISSUE - 1>(S);
  __builtin_amdgcn_sched_barrier(0);
}
template <int HP>
DEVI void h_main(K1H& S, f32x4 (&cs)[5]) {
  u32x4 wa[5], wb[5];
  u32x4 ones = {0x3f803f80u, 0x3f803f80u, 0x3f803f80u, 0x3f803f80u};
  asm volatile("" : "+v"(ones));
  h_issue<HP, 1, true>(S);
  h_issue<HP, 0>(S);
  h_issue<HP, 1>(S);
  if (HP == 0) {
    if ((threadIdx.x & 63) < 16) asm volatile("s_mov_b32 m0, %1\n\ts_nop 0\n\tglobal_load_lds_dword %0, off" ::"v"(S.touch), "s"(S.ldsbase + K1_SCR) : "memory");
  }
  mb_wait(S.lds, 0);
  h_read(S, wa);
  mb_wait(S.lds, 1);
  h_step<HP, -1, 1>(S, wa, wb, cs, ones);
  h_step<HP, 3, 2>(S, wb, wa, cs, ones);
#pragma unroll 1
  for (int s = 2; s < 14; s += 2) {
    h_step<HP, 3, 1>(S, wa, wb, cs, ones);
    h_step<HP, 3, 2>(S, wb, wa, cs, ones);
  }
  h_step<HP, 3, 1>(S, wa, wb, cs, ones);
  constexpr int n = 12 - HP;
  const int lane = threadIdx.x & 63;
  h_step<HP, 3, 0>(S, wb, wa, cs, ones);
  if (S.pc2) {
    asm volatile("s_mov_b32 m0, %1\n\ts_nop 0\n\tglobal_load_lds_dword %0, off" ::"v"(S.pc2 + HP * 8192 + lane * 128), "s"(S.ldsbase + K1_SCR) : "memory");
  }
#pragma unroll
  for (int i = 0; i < 5; ++i) st16(S.rs, u32x4{~0u, ~0u, ~0u, ~0u}, lane * 16, OFF_OT + (blockIdx.x * 10 + HP * 5 + i) * 1024);
  asm volatile("s_waitcnt vmcnt(%0)" ::"i"(2 * n + 5) : "memory");
  h_step<HP, 9, 0>(S, wa, wb, cs, ones);
  asm volatile("s_waitcnt vmcnt(%0)" ::"i"(n + 5) : "memory");
  h_step<HP, 9, 0>(S, wb, wa, cs, ones);
  {
    const int rho = lane >> 1;
    const unsigned v2 = rho * 1280 + (lane & 1) * 16;
    const unsigned vo = (((rho & 12) << 1) | ((rho & 16) >> 2) | (rho & 3)) * 1280 + (lane & 1) * 16;
#pragma unroll
    for (int i = 0; i < 6; ++i) {
      const int gi = S.first + i, fid = gi < 2960 ? gi : 2959;
      const int cq = (fid >> 2) / 74, ks = (fid >> 2) - 74 * cq;
      const char* src = (ks < 54 ? (const char*)S.w2 + ks * 40960 : (const char*)S.wo + (ks - 54) * 40960) + (4 * cq + (fid & 3)) * 32;
      glds16n(ks < 54 ? v2 : vo, src, S.ldsbase + K1_RT + (HP * 6 + i) * 1024);
    }
  }
  WAIT_VM(11);
  h_step<HP, 9, 0>(S, wa, wb, cs, ones);
#pragma unroll
  for (int c = 0; c < 5; ++c) cs[c] = mfma(wb[c], ones, cs[c]);
  __builtin_amdgcn_s_setprio(0);
}

__global__ __launch_bounds__(512, 2) void hedgehog_k1(const u16* __restrict__ x, const u16* __restrict__ wqkv,
                                                      const u16* __restrict__ wphi, const u16* __restrict__ wo,
                                                      const u16* __restrict__ w1, const u16* __restrict__ b1,
                                                      const u16* __restrict__ w2, char* __restrict__ ws) {
  extern __shared__ __attribute__((aligned(16))) char smem[];
  LDSP char* lds = (LDSP char*)smem;
  const unsigned long long kpc = __builtin_amdgcn_s_getpc();
  const int tid = threadIdx.x, lane = tid & 63;
  const int wave = __builtin_amdgcn_readfirstlane(tid >> 6);
  const int b = blockIdx.x, xcd = b & 7, jx = b >> 3;
  const unsigned ldsbase = (unsigned)(size_t)lds;
  const bool helper = wave >= 6;
  DG(unsigned long long T[8]; T[0] = stamp(); T[7] = rstamp(); T[1] = T[6] = 0;)
  if (helper) {
    const char* pc = (const char*)(kpc & ~(unsigned long long)127) + min(((jx % 3) * 2 + (wave & 1)) * 8192 + lane * 128, CODE_BYTES);
    asm volatile("s_mov_b32 m0, %1\n\ts_nop 0\n\tglobal_load_lds_dword %0, off" ::"v"(pc), "s"(ldsbase + K1_SCR) : "memory");
  }
  if (tid == 0) *(LDSP u32x2*)(lds + K1_MB) = u32x2{0u, 0u};
  WAIT_LGKM0();
  asm volatile("" ::"s"(x), "s"(wqkv), "s"(wphi), "s"(wo), "s"(w1), "s"(b1), "s"(w2), "s"(ws));
  const int rb = 8 * (xcd >> 2) + (jx & 7);
  const char* xrow = (const char*)x + (size_t)rb * 128 * 1280;
  auto xvoff = [&](int pc) {
    const int row = pc * 8 + (lane >> 3);
    return (unsigned)(row * 1280 + (((lane & 7) ^ ((row >> 1) & 7)) << 4));
  };
  const unsigned x0 = xvoff(wave), x1 = x0 + 48 * 1280;
  if (!helper) {
    glds16n(x0, xrow, ldsbase + wave * 1024);
    glds16n(x0, xrow + 64 * 1280, ldsbase + 8192 + wave * 1024);
    if (wave < 2) {
      glds16n(x1, xrow, ldsbase + wave * 1024 + 6144);
      glds16n(x1, xrow + 64 * 1280, ldsbase + 8192 + wave * 1024 + 6144);
    }
  }
  __builtin_amdgcn_s_barrier();
  const int mh = wave & 1;
  const int nj = wave < 4 ? wave >> 1 : 2;
  const int cj = helper ? 2 * mh : nj;
  const int g = lane >> 4, m = lane & 15, q4 = m >> 2, p4 = lane & 3;
  const int cgi = jx >> 3;
  const bool atype = cgi < 2;
  const int grp = 2 * (xcd & 3) + (cgi & 1);
  const u16* W = atype ? wqkv : w1;
  const int ldw = atype ? 1920 : 1728;
  const int c0 = atype ? 80 * grp : (grp < 6 ? 224 * grp : 1344 + 192 * (grp - 6));
  const int sub = atype ? 640 : (grp < 6 ? 80 : 64);
  const unsigned incw = 64 * ldw;
  const int t1 = 1 + 3 * (((jx & 7) + cgi) % 3);
  auto wvoff = [&](int id) {
    const int sj = id / 5, ss = id - 5 * sj;
    const int c = 64 * ss + lane, rho = c / 10, cc = c - 10 * rho;
    const int k = (rho & 0x13) | ((rho & 4) << 1) | ((rho & 8) >> 1);
    const bool narrow = !atype && (grp >= 6 || sj == 2);
    return (unsigned)(k * ldw + c0 + sub * sj + 8 * ((narrow && cc >= 8) ? cc - 2 : cc)) * 2;
  };
  const unsigned wa0 = K1_W + cj * WSUB + (16 * (g >> 1) + 4 * (g & 1) + q4) * 160 + p4 * (atype ? 40 : 8);
  const unsigned cstr = atype ? 8 : 32;
  const __amdgpu_buffer_rsrc_t rs = __builtin_amdgcn_make_buffer_rsrc((void*)ws, 0, WS_END, 0x00020000);
  f32x4 acc[4][5];

  if (!helper) {
    auto xgrp = [&](int u, int xt) {
      const char* px = xrow + xt * 128 + (u & 1) * (64 * 1280);
      const unsigned d = ldsbase + u * 8192 + wave * 1024;
      glds16n(x0, px, d);
      if (wave < 2) glds16n(x1, px, d + 6144);
    };
    const unsigned dw = ldsbase + K1_W + wave * 1024;
    const unsigned v0 = wvoff(wave);
    glds16n(v0, W, dw);
    const unsigned v1 = wvoff(wave + 6);
    glds16n(v1, W, dw + 6144);
    const unsigned v2 = wvoff(wave < 3 ? wave + 12 : 14);
    if (wave < 3) glds16n(v2, W, dw + 12288);
    glds16n(v0, (const char*)W + incw, dw + WSLOT);
    glds16n(v1, (const char*)W + incw, dw + WSLOT + 6144);
    if (wave < 3) glds16n(v2, (const char*)W + incw, dw + WSLOT + 12288);
    xgrp(2, t1);
    K1C S;
    S.lds = lds;
    S.xa = (mh * 64 + m) * 128 + ((g ^ (m >> 1)) << 4);
#pragma unroll
    for (int c = 0; c < 5; ++c) S.wa[c] = wa0 + c * cstr;
    S.wr = 0;
    DG(S.t1 = 0;)
    f32x4 cs[3], s1[4], s2[4];
#pragma unroll
    for (int i = 0; i < 4; ++i) {
      s1[i] = f32x4{0.f, 0.f, 0.f, 0.f}; s2[i] = s1[i]; cs[i % 3] = s1[i];
#pragma unroll
      for (int c = 0; c < 5; ++c) acc[i][c] = s1[i];
    }
    if (wave == 4 && lane < 48) *(LDSP u32x4*)(lds + K1_ZERO + lane * 16) = u32x4{0u, 0u, 0u, 0u};
    const bool five = atype || (grp < 6 && nj < 2);
    if (wave < 2) WAIT_VM(5); else if (wave == 2) WAIT_VM(4); else WAIT_VM(3);
    mb_arrive(lds, 0);
    {
      const int col = c0 + sub * (lane / 10) + 8 * (lane % 10);
      const unsigned vo = atype ? tid * 16 : (col > 1720 ? 1720 : col) * 2;
      if (tid < (atype ? 200 : 30)) glds16n(vo, atype ? (const char*)wphi + grp * 3200 : (const char*)b1, ldsbase + K1_WPHI + wave * 1024);
    }
    mb_wait(lds, 0);
    DG(T[1] = stamp();)
    if (nj == 1) {
      if (five) { if (mh == 0) c_main<5, 1>(S, acc, cs, s1, s2); else c_main<5, 2>(S, acc, cs, s1, s2); }
      else { if (mh == 0) c_main<4, 1>(S, acc, cs, s1, s2); else c_main<4, 2>(S, acc, cs, s1, s2); }
      const int cb = mh == 0 ? 0 : (five ? 3 : 2), cn = mh == 0 ? (five ? 3 : 2) : 2;
      if (m == 0) {
#pragma unroll
        for (int c = 0; c < 3; ++c)
          if (c < cn) *(LDSP f32x4*)(lds + K1_CS + (80 + (cb + c) * 16 + 4 * g) * 4) = cs[c];
      }
      if (g == q4) {
#pragma unroll
        for (int r = 0; r < 4; ++r) {
          const float sq = (m & 2) ? ((m & 1) ? s2[r][3] : s2[r][2]) : ((m & 1) ? s2[r][1] : s2[r][0]);
          const float mean = s1[r][0] * (1.0f / 640.0f);
          const float var = fmaxf(sq * (1.0f / 640.0f) - mean * mean, 0.f);
          float rsd = __builtin_amdgcn_rsqf(var + 1e-5f);
          if (!atype) rsd *= L2E;
          *(LDSP f32x2*)(lds + K1_STAT + (mh * 64 + r * 16 + m) * 8) = f32x2{rsd, -mean * rsd};
        }
      }
      if (atype) {
#pragma unroll
        for (int r = 2; r < 4; ++r)
#pragma unroll
          for (int c = 0; c < 5; ++c) *(LDSP f32x4*)(lds + K1_HO + (mh * 2 + r - 2) * 5120 + c * 1024 + lane * 16) = acc[r][c];
      }
    } else {
      if (five) c_main<5, 0>(S, acc, cs, s1, s2); else c_main<4, 0>(S, acc, cs, s1, s2);
      if (!atype) {
#pragma unroll
        for (int c = 0; c < 5; ++c) *(LDSP f32x4*)(lds + K1_HO + (mh * 2 + (nj >> 1)) * 5120 + c * 1024 + lane * 16) = acc[3][c];
        if (nj == 0 && tid < 120) {
          const unsigned w = *(LDSP unsigned*)(lds + K1_WPHI + tid * 4);
          *(LDSP f32x2*)(lds + K1_BL + tid * 8) = f32x2{bflo(w) * L2E, bfhi(w) * L2E};
        }
      }
    }
    DG(T[6] = S.t1;)
  } else {
    __builtin_amdgcn_s_setprio(3);
    K1H S;
    S.lds = lds; S.ldsbase = ldsbase;
    S.incw = incw;
    S.pw = (const char*)W + 2 * t1 * incw;
    S.wrapw = (unsigned)(size_t)W + 20 * incw;
    S.px = xrow + t1 * 128 + 64 * 1280;
    S.wrapx = (unsigned)(size_t)xrow + 1280;
    S.dw = 2 * WSLOT; S.dx = 3 * 8192; S.wr = 0;
    DG(S.t1 = 0;)
#pragma unroll
    for (int i = 0; i < 4; ++i) {
      S.vx[i] = xvoff(4 * mh + i);
      glds16n(S.vx[i], S.px, ldsbase + mh * 4096 + i * 1024 + 3 * 8192);
    }
#pragma unroll
    for (int i = 0; i < 8; ++i) {
      S.vw[i] = wvoff(8 * mh + i < 15 ? 8 * mh + i : 14);
      if (8 * mh + i < 15) glds16n(S.vw[i], S.pw, ldsbase + K1_W + mh * 8192 + i * 1024 + 2 * WSLOT);
    }
    S.touch = lane < 12 ? ws + (size_t)lane * (2u << 20) : lane == 12 ? (const char*)w2 : lane == 13 ? (const char*)w2 + (2u << 20) : lane == 14 ? (const char*)wo : (const char*)wphi;
#pragma unroll
    for (int c = 0; c < 5; ++c) { S.wa[c] = wa0 + c * cstr; acc[0][c] = f32x4{0.f, 0.f, 0.f, 0.f}; }
    S.pc2 = jx < 2 ? (const char*)(kpc & ~(unsigned long long)127) + CODE2_OFF : nullptr;
    S.rs = rs; S.w2 = w2; S.wo = wo; S.first = b * 12 + mh * 6;
    if (mh == 0) h_main<0>(S, acc[0]); else h_main<1>(S, acc[0]);
    DG(T[6] = S.t1;)
    if (m == 0) {
#pragma unroll
      for (int c = 0; c < 5; ++c) *(LDSP f32x4*)(lds + K1_CS + (cj * 80 + c * 16 + 4 * g) * 4) = acc[0][c];
    }
  }
  DG(T[2] = stamp();)
  WAIT_LGKM0();
  __builtin_amdgcn_s_barrier();
  asm volatile("" ::: "memory");
  DG(T[3] = stamp();)
#define DG_OUT1() DG({ T[5] = stamp(); unsigned long long re = rstamp(); if (lane == 0) { unsigned long long* dp = (unsigned long long*)(ws + OFF_DBG) + (size_t)(b * 8 + wave) * 16; for (int i = 0; i < 8; ++i) dp[i] = T[i]; dp[8] = re; } if (lane < 20) ((unsigned*)(ws + OFF_DBG + (2u << 20)))[(b * 8 + wave) * 32 + lane] = *(LDSP unsigned*)(lds + K1_LDS + wave * 128 + lane * 4) - (unsigned)T[0]; })
  auto side_store = [&]() {
    WAIT_VM(0);
    LDSP char* pr = lds + K1_RT + mh * 6144 + (4 * g + q4) * 32 + p4 * 8;
#pragma unroll
    for (int i = 0; i < 6; ++i) {
      const int gi = b * 12 + mh * 6 + i, cq = (gi >> 2) / 74, ks = (gi >> 2) - 74 * cq;
      const u32x4 v = trread2(pr + i * 1024, pr + i * 1024 + 512);
      if (gi < 2960) st16(rs, v, lane * 16, OFF_WT + ((4 * cq + (gi & 3)) * 74 + ks) * 1024);
    }
  };

  if (!atype) {
    auto round = [&](const f32x4 (&a)[5], int ej, int rt) {
      const bool five = grp < 6 && ej < 2;
      const f32x2 st = *(LDSP f32x2*)(lds + K1_STAT + (rt * 16 + m) * 8);
      u32x2 pk[5];
#pragma unroll
      for (int c = 0; c < 5; ++c) {
        if (c == 4 && !five) { pk[4] = u32x2{0u, 0u}; break; }
        const f32x4 cs = *(LDSP f32x4*)(lds + K1_CS + (ej * 80 + c * 16 + 4 * g) * 4);
        const f32x4 bl = *(LDSP f32x4*)(lds + K1_BL + (ej * 80 + c * 16 + 4 * g) * 4);
        float res[4];
#pragma unroll
        for (int e = 0; e < 4; ++e) {
          const float hl = fmaf(a[c][e], st[0], fmaf(st[1], cs[e], bl[e]));
          const float ex = __builtin_amdgcn_exp2f(hl);
          const float rc = __builtin_amdgcn_rcpf(fmaf(ex, ex + 2.f, 2.f));
          res[e] = hl * fmaf(rc, -2.f * LN2, LN2);
        }
        pk[c] = u32x2{pk2(res[0], res[1]), pk2(res[2], res[3])};
      }
      const int T0 = (c0 + sub * ej) >> 4;
      const unsigned base = OFF_ACT + ((rb * 8 + rt) * 54 + (T0 >> 1)) * 1024;
      if (!(T0 & 1)) {
        st16(rs, u32x4{pk[0][0], pk[0][1], pk[1][0], pk[1][1]}, lane * 16, base);
        st16(rs, u32x4{pk[2][0], pk[2][1], pk[3][0], pk[3][1]}, lane * 16, base + 1024);
        if (five) st8(rs, pk[4], lane * 16, base + 2048);
      } else {
        st8(rs, pk[0], lane * 16, base + 8);
        st16(rs, u32x4{pk[1][0], pk[1][1], pk[2][0], pk[2][1]}, lane * 16, base + 1024);
        st16(rs, u32x4{pk[3][0], pk[3][1], pk[4][0], pk[4][1]}, lane * 16, base + 2048);
      }
    };
    if (helper) {
#pragma unroll
      for (int r = 0; r < 2; ++r)
#pragma unroll
        for (int c = 0; c < 5; ++c) acc[r][c] = *(LDSP f32x4*)(lds + K1_HO + (mh * 2 + r) * 5120 + c * 1024 + lane * 16);
    }
#pragma unroll 1
    for (int it = 0; it < (helper ? 1 : 2); ++it) {
      round(acc[0], helper ? 0 : nj, mh * 4 + (helper ? 3 : 2 * it));
      if (it == 0 || nj == 1) round(acc[1], helper ? 2 : nj, mh * 4 + (helper ? 3 : 2 * it + 1));
#pragma unroll
      for (int c = 0; c < 5; ++c) { acc[0][c] = acc[2][c]; acc[1][c] = acc[3][c]; }
    }
    DG(T[4] = stamp();)
    if (helper) side_store();
    DG_OUT1();
    return;
  }

  auto normalise = [&](const f32x4 (&a)[5], const f32x4 (&cs)[5], int rt, u32x4 (&bq)[3], unsigned one) {
    const f32x2 st = *(LDSP f32x2*)(lds + K1_STAT + (rt * 16 + m) * 8);
#pragma unroll
    for (int c = 0; c < 5; ++c) {
      float v[4];
#pragma unroll
      for (int e = 0; e < 4; ++e) v[e] = fmaf(a[c][e], st[0], st[1] * cs[c][e]);
      bq[c >> 1][(c & 1) * 2] = pk2(v[0], v[1]);
      bq[c >> 1][(c & 1) * 2 + 1] = pk2(v[2], v[3]);
    }
    bq[2][2] = one;
    bq[2][3] = 0u;
  };
  f32x4 csv[5];
  if (!helper && nj != 1) {
#pragma unroll
    for (int c = 0; c < 5; ++c) csv[c] = *(LDSP f32x4*)(lds + K1_CS + (nj * 80 + c * 16 + 4 * g) * 4);
#pragma unroll
    for (int r = 0; r < 4; ++r) {
      u32x4 bq[3];
      normalise(acc[r], csv, mh * 4 + r, bq, nj == 2 ? 0x00003f80u : 0u);
      if (nj == 0) {
        const unsigned so = OFF_QT + (4 * grp * 128 + rb * 8 + mh * 4 + r) * 768;
#pragma unroll
        for (int i = 0; i < 3; ++i) st16(rs, bq[i], g * (128 * 768) + m * 16, so + i * 256);
      } else {
#pragma unroll
        for (int i = 0; i < 3; ++i) *(LDSP u32x4*)(lds + K1_VIMG + (mh * 64 + r * 16 + m) * V_PITCH + g * 48 + i * 16) = bq[i];
      }
    }
  }
  if (helper || nj != 2) {
    const int kr0 = helper ? 3 : (nj == 1 ? 0 : 2);
    if (kr0) {
#pragma unroll
      for (int c = 0; c < 5; ++c) acc[0][c] = *(LDSP f32x4*)(lds + K1_HO + (mh * 2 + kr0 - 2) * 5120 + c * 1024 + lane * 16);
    }
#pragma unroll
    for (int c = 0; c < 5; ++c) csv[c] = *(LDSP f32x4*)(lds + K1_CS + (80 + c * 16 + 4 * g) * 4);
    LDSP char* wl = lds + ((p4 == g) ? K1_WPHI + g * 800 + q4 * 40 : K1_ZERO);
#pragma unroll 1
    for (int kr = kr0; kr < (kr0 ? kr0 + 1 : 2); ++kr) {
      u32x4 bq[3];
      normalise(acc[0], csv, mh * 4 + kr, bq, 0u);
      f32x4 pk[5];
      head_proj(wl, bq, pk);
      unsigned ph[20];
      feature_map(pk, ph);
      LDSP char* dst = lds + (mh * 64 + kr * 16 + m) * PK_PITCH + g * 80;
#pragma unroll
      for (int i = 0; i < 5; ++i) *(LDSP u32x4*)(dst + i * 16) = u32x4{ph[4 * i], ph[4 * i + 1], ph[4 * i + 2], ph[4 * i + 3]};
#pragma unroll
      for (int c = 0; c < 5; ++c) acc[0][c] = acc[1][c];
    }
  }
  DG(T[4] = stamp();)
  WAIT_LGKM0();
  __builtin_amdgcn_s_barrier();
  asm volatile("" ::: "memory");
  if (!helper) {
#pragma unroll 1
    for (int job = wave; job < 12; job += 6) {
      const int hh = job & 3, ft = job >> 2;
      LDSP char* pa = lds + (4 * g + q4) * PK_PITCH + hh * 80 + ft * 32 + p4 * 8;
      LDSP char* pb = lds + K1_VIMG + (4 * g + q4) * V_PITCH + hh * 48 + p4 * 8;
      f32x4 d0 = {0.f, 0.f, 0.f, 0.f}, d1 = {0.f, 0.f, 0.f, 0.f};
#pragma unroll
      for (int kk = 0; kk < 4; ++kk) {
        u32x4 a = trread2(pa + (32 * kk) * PK_PITCH, pa + (32 * kk + 16) * PK_PITCH);
        u32x4 v0 = trread2(pb + (32 * kk) * V_PITCH, pb + (32 * kk + 16) * V_PITCH);
        u32x4 v1 = trread2(pb + (32 * kk) * V_PITCH + 32, pb + (32 * kk + 16) * V_PITCH + 32);
        d0 = mfma(a, v0, d0);
        d1 = mfma(a, v1, d1);
      }
      const unsigned so = OFF_KVP + ((4 * grp + hh) * 16 + rb) * KVP_B + 1344 * ft;
      st16(rs, __builtin_bit_cast(u32x4, d0), m * 64 + g * 16, so);
      if (m < 5) st16(rs, __builtin_bit_cast(u32x4, d1), (16 + m) * 64 + g * 16, so);
    }
  } else {
    side_store();
  }
  DG_OUT1();
}

constexpr unsigned K2_YD = 0;
constexpr unsigned K2_SD = 4 * 15360;
constexpr unsigned K2_KVT = K2_SD + 4 * 20480;
constexpr unsigned K2_ZERO = K2_KVT + 1920;
constexpr unsigned K2_WPHI = K2_ZERO + 1792;
constexpr unsigned K2_FLAG = K2_WPHI + 3200;
constexpr unsigned K2_OWN = K2_FLAG + 16;
constexpr unsigned K2_LDS = K2_OWN + 8192;

DEVI u32x4 bld(__amdgpu_buffer_rsrc_t rs, unsigned voff, unsigned soff) {
  return __builtin_amdgcn_raw_buffer_load_b128(rs, voff, soff, 0);
}
DEVI u32x4 bld_sc1(__amdgpu_buffer_rsrc_t rs, unsigned voff, unsigned soff) {
  return __builtin_amdgcn_raw_buffer_load_b128(rs, voff, soff, 16);
}
template <int N>
DEVI void y_stream(__amdgpu_buffer_rsrc_t rs, unsigned act, unsigned wt, int rot, f32x4 (&acc)[4][5]) {
  const unsigned l16 = (threadIdx.x & 63) * 16;
  unsigned ab[4];
#pragma unroll
  for (int l = 0; l < 4; ++l) ab[l] = act + ((l + rot) & 3) * (54 * 1024);
  u32x4 A[3][4], B[3][5];
  auto load = [&](int i) {
#pragma unroll
    for (int l = 0; l < 4; ++l) A[i % 3][l] = bld(rs, l16, ab[l] + i * 1024);
#pragma unroll
    for (int c = 0; c < 5; ++c) B[i % 3][c] = bld(rs, l16, wt + c * (74 * 1024) + i * 1024);
  };
  load(0);
  load(1);
#pragma unroll
  for (int i = 0; i < N; ++i) {
    if (i + 2 < N) load(i + 2);
    __builtin_amdgcn_sched_barrier(0);
#pragma unroll
    for (int l = 0; l < 4; ++l)
#pragma unroll
      for (int c = 0; c < 5; ++c) acc[l][c] = mfma(B[i % 3][c], A[i % 3][l], acc[l][c]);
    __builtin_amdgcn_sched_barrier(0);
  }
}

template <int C0, int C1>
DEVI void combine(LDSP char* lds, const u32x2 (&xv)[3], u16* out, int rt, int rb2, int cb, const f32x4 (&own)[5]) {
  const int lane = threadIdx.x & 63;
  const unsigned l16 = lane * 16;
  const size_t orow = (size_t)(64 * rb2 + 16 * rt + (lane & 15)) * 640 + 80 * cb + 4 * (lane >> 4);
  WAIT_LGKM0();
  __builtin_amdgcn_s_barrier();
  asm volatile("" ::: "memory");
#pragma unroll
  for (int c = C0; c < C1; ++c) {
    f32x4 y = C0 == 0 ? own[c] : *(LDSP f32x4*)(lds + K2_OWN + (rt * 2 + c - 3) * 1024 + l16);
#pragma unroll
    for (int l = 1; l < 4; ++l)
      y += *(LDSP f32x4*)(lds + K2_YD + ((rt - l) & 3) * 15360 + ((l - 1) * 5 + c) * 1024 + l16);
    LDSP f32x4* sp = (LDSP f32x4*)(lds + K2_SD + (rt * 5 + c) * 1024 + l16);
    const f32x4 s = sp[0] + sp[1280];
    y += sp[2560] + sp[3840];
    float o[4];
#pragma unroll
    for (int e = 0; e < 4; ++e) {
      const float gate = y[e] * __builtin_amdgcn_rcpf(1.f + __builtin_amdgcn_exp2f(-y[e] * L2E));
      const unsigned xw = xv[c - C0][e >> 1];
      o[e] = fmaf(s[e], gate, (e & 1) ? bfhi(xw) : bflo(xw));
    }
    *(u32x2*)(out + orow + 16 * c) = u32x2{pk2(o[0], o[1]), pk2(o[2], o[3])};
  }
}

__global__ __launch_bounds__(512, 2) void hedgehog_k2(const u16* __restrict__ x, const u16* __restrict__ wphi,
                                                      char* __restrict__ ws, u16* __restrict__ out) {
  extern __shared__ __attribute__((aligned(16))) char smem[];
  LDSP char* lds = (LDSP char*)smem;
  const int tid = threadIdx.x, lane = tid & 63;
  const int wave = __builtin_amdgcn_readfirstlane(tid >> 6);
  const int g = lane >> 4, m = lane & 15, q4 = m >> 2, p4 = lane & 3;
  const int b = blockIdx.x, xcd = b & 7, jx = b >> 3;
  const int rb2 = 8 * (xcd >> 1) + (jx & 7), cb = 4 * (xcd & 1) + (jx >> 3);
  const __amdgpu_buffer_rsrc_t rs = __builtin_amdgcn_make_buffer_rsrc((void*)ws, 0, WS_END, 0x00020000);
  const unsigned l16 = lane * 16;
  DG(unsigned long long T[8]; T[0] = stamp(); T[7] = rstamp(); T[1] = T[2] = T[3] = T[4] = T[5] = T[6] = 0;)
#define DG_OUT2() DG({ unsigned long long re = rstamp(); if (lane == 0) { unsigned long long* dp = (unsigned long long*)(ws + OFF_DBG + (1u << 20)) + (size_t)(b * 8 + wave) * 16; for (int i = 0; i < 8; ++i) dp[i] = T[i]; dp[8] = re; } })
  asm volatile("" ::"s"(x), "s"(wphi), "s"(out));
  if (wave == 0) {
    const unsigned long long pc = __builtin_amdgcn_s_getpc();
    const char* p = lane < 62 ? (const char*)(pc & ~(unsigned long long)127) + lane * 256 : (const char*)out + (lane - 62) * (2u << 20);
    asm volatile("s_mov_b32 m0, %1\n\ts_nop 0\n\tglobal_load_lds_dword %0, off" ::"v"(p), "s"((unsigned)(size_t)lds + K2_OWN) : "memory");
  }
  const unsigned act = OFF_ACT + 4 * rb2 * (54 * 1024), wt = OFF_WT + 5 * cb * (74 * 1024);
  const u16* xt = x + (size_t)(64 * rb2 + 16 * (wave & 3) + m) * 640 + 80 * cb + 4 * g;
  f32x4 acc[4][5];
#pragma unroll
  for (int r = 0; r < 4; ++r)
#pragma unroll
    for (int c = 0; c < 5; ++c) acc[r][c] = f32x4{0.f, 0.f, 0.f, 0.f};

  if (wave < 4) {
    __builtin_amdgcn_s_barrier();
    DG(T[1] = stamp();)
    u32x2 xv[3];
#pragma unroll
    for (int c = 0; c < 3; ++c) xv[c] = *(const u32x2*)(xt + 16 * c);
    y_stream<K2_NY>(rs, act + wave * (K2_NY * 1024), wt + wave * (K2_NY * 1024), wave, acc);
    DG(T[3] = stamp();)
#pragma unroll
    for (int l = 1; l < 4; ++l)
#pragma unroll
      for (int c = 0; c < 5; ++c)
        *(LDSP f32x4*)(lds + K2_YD + wave * 15360 + ((l - 1) * 5 + c) * 1024 + l16) = acc[l][c];
#pragma unroll
    for (int c = 3; c < 5; ++c) *(LDSP f32x4*)(lds + K2_OWN + (wave * 2 + c - 3) * 1024 + l16) = acc[0][c];
    DG(T[4] = stamp();)
    combine<0, 3>(lds, xv, out, wave, rb2, cb, acc[0]);
    DG(T[6] = stamp();)
    DG_OUT2();
    return;
  }

  const int aw = wave - 4;
  const int h = 4 * xcd + (jx & 3), sb = jx >> 2;
  const int tok = sb * 256 + aw * 64 + lane;
  u32x4 wv = *(const u32x4*)(wphi + h * 400 + (lane < 50 ? lane : 49) * 8);
  u32x4 bq[3];
#pragma unroll
  for (int s = 0; s < 3; ++s) bq[s] = bld(rs, (unsigned)(tok >> 4) * 768 + m * 16, OFF_QT + h * (128 * 768) + s * 256);
  int item = aw * 53 + (lane < 53 ? lane : 52);
  item = item < 210 ? item : 209;
  const int d = item / 10, fq = item - 10 * d;
  u32x4 part[16];
#pragma unroll
  for (int r = 0; r < 16; ++r) part[r] = bld(rs, (unsigned)((fq >> 2) * 1344 + d * 64 + (fq & 3) * 16), OFF_KVP + (h * 16 + r) * KVP_B);
  if (aw == 0) {
    *(LDSP u32x4*)(lds + K2_ZERO + l16) = u32x4{0u, 0u, 0u, 0u};
    if (lane < 48) *(LDSP u32x4*)(lds + K2_ZERO + 1024 + l16) = u32x4{0u, 0u, 0u, 0u};
    if (lane == 0) *(LDSP u32x4*)(lds + K2_FLAG) = u32x4{0u, 0u, 0u, 0u};
  }
  WAIT_LGKM0();
  __builtin_amdgcn_s_barrier();
  __builtin_amdgcn_s_setprio(2);
  *(LDSP u32x4*)(lds + K2_WPHI + aw * 800 + (lane < 50 ? lane : 49) * 16) = wv;
  unsigned ph[20];
  {
    LDSP char* wl = lds + ((p4 == g) ? K2_WPHI + aw * 800 + q4 * 40 : K2_ZERO);
    f32x4 pq[5];
    head_proj(wl, bq, pq);
    feature_map(pq, ph);
#pragma unroll
    for (int i = 0; i < 20; ++i) asm volatile("" : "+v"(ph[i]));
  }
  DG(T[1] = stamp();)
  {
    f32x4 sum = __builtin_bit_cast(f32x4, part[0]);
#pragma unroll
    for (int r = 1; r < 16; ++r) sum += __builtin_bit_cast(f32x4, part[r]);
    *(LDSP u32x2*)(lds + K2_KVT + d * 80 + fq * 8) = u32x2{pk2(sum[0], sum[1]), pk2(sum[2], sum[3])};
    WAIT_LGKM0();
    if (lane == 0) *(volatile LDSP unsigned*)(lds + K2_FLAG + aw * 4) = 1u;
    for (int spin = 0; spin < (1 << 22); ++spin) {
      const u32x4 f = *(volatile LDSP u32x4*)(lds + K2_FLAG);
      if (__builtin_amdgcn_readfirstlane(f[0] & f[1] & f[2] & f[3])) break;
      __builtin_amdgcn_s_sleep(1);
    }
    asm volatile("" ::: "memory");
  }
  DG(T[2] = stamp();)
  {
    LDSP char* kl = lds + (((m >> 2) == g) ? K2_KVT + (m & 3) * 80 : K2_ZERO);
    f32x4 nd[6];
#pragma unroll
    for (int t = 0; t < 6; ++t) {
      f32x4 c = {0.f, 0.f, 0.f, 0.f};
#pragma unroll
      for (int s = 0; s < 5; ++s)
        c = mfma(*(LDSP u32x4*)(kl + t * 320 + s * 16), u32x4{ph[4 * s], ph[4 * s + 1], ph[4 * s + 2], ph[4 * s + 3]}, c);
      nd[t] = c;
    }
    const float rd = __builtin_amdgcn_rcpf(nd[5][0] + 1e-6f);
    unsigned ow[10];
#pragma unroll
    for (int t = 0; t < 5; ++t) {
      ow[2 * t] = min(pk2(nd[t][0] * rd, nd[t][1] * rd), 0xFFFFFFFEu);
      ow[2 * t + 1] = min(pk2(nd[t][2] * rd, nd[t][3] * rd), 0xFFFFFFFEu);
    }
    const int e0 = (5 * h + 1) >> 1, e8 = (h & 1) ? e0 - 1 : e0 + 2;
    const unsigned vo = (unsigned)(tok >> 4) * 20480 + m * 16;
    const bool odd = h & 1;
    st16(rs, odd ? u32x4{ow[2], ow[3], ow[4], ow[5]} : u32x4{ow[0], ow[1], ow[2], ow[3]}, vo, OFF_OT + e0 * 256);
    st16(rs, odd ? u32x4{ow[6], ow[7], ow[8], ow[9]} : u32x4{ow[4], ow[5], ow[6], ow[7]}, vo, OFF_OT + (e0 + 1) * 256);
    st8(rs, odd ? u32x2{ow[0], ow[1]} : u32x2{ow[8], ow[9]}, vo, OFF_OT + e8 * 256 + (h & 1) * 8);
  }
  DG(T[3] = stamp();)
  const unsigned ring = K2_SD + aw * 20480;
  u32x2 xv[3];
#pragma unroll
  for (int c = 0; c < 2; ++c) xv[c] = *(const u32x2*)(xt + 48 + 16 * c);
  xv[2] = xv[1];
  if (aw >= 2) {
    __builtin_amdgcn_s_setprio(0);
    constexpr int NA = (54 - 4 * K2_NY) / 2;
    y_stream<NA>(rs, act + (4 * K2_NY + NA * (aw - 2)) * 1024, wt + (4 * K2_NY + NA * (aw - 2)) * 1024, 0, acc);
  } else {
    u32x4 B[3][5];
    auto loadb = [&](int i) {
#pragma unroll
      for (int c = 0; c < 5; ++c) B[i % 3][c] = bld(rs, l16, wt + c * (74 * 1024) + (54 + 10 * aw + i) * 1024);
    };
    loadb(0);
    loadb(1);
    const unsigned ob = OFF_OT + (4 * rb2 * 20 + 10 * aw) * 1024;
    {
      const int l2 = lane < 40 ? lane : 39;
      const unsigned pv = 3 * 20480 + l2 * 256 + 15 * 16 + 4;
      for (int spin = 0; spin < (1 << 20); ++spin) {
        asm volatile("" ::: "memory");
        const u32x2 w = __builtin_amdgcn_raw_buffer_load_b64(rs, pv, ob, 16);
        if (!__any(w[0] == 0xFFFFFFFFu || w[1] == 0xFFFFFFFFu)) break;
        __builtin_amdgcn_s_sleep(4);
      }
    }
    DG(T[4] = stamp();)
#pragma unroll
    for (int half = 0; half < 2; ++half) {
      u32x4 A[5][4];
      for (int spin = 0; spin < (1 << 16); ++spin) {
        asm volatile("" ::: "memory");
        unsigned mx = 0;
#pragma unroll
        for (int i = 0; i < 5; ++i)
#pragma unroll
          for (int l = 0; l < 4; ++l) A[i][l] = bld_sc1(rs, l16, ob + l * 20480 + (5 * half + i) * 1024);
#pragma unroll
        for (int i = 0; i < 5; ++i)
#pragma unroll
          for (int l = 0; l < 4; ++l) mx = max(max(mx, max(A[i][l][0], A[i][l][1])), max(A[i][l][2], A[i][l][3]));
        if (!__any(mx == 0xFFFFFFFFu)) break;
        __builtin_amdgcn_s_sleep(4);
      }
      DG(if (half == 0) T[5] = stamp();)
#pragma unroll
      for (int i = 0; i < 5; ++i) {
        const int k = 5 * half + i;
        if (k + 2 < 10) loadb(k + 2);
        __builtin_amdgcn_sched_barrier(0);
#pragma unroll
        for (int l = 0; l < 4; ++l)
#pragma unroll
          for (int c = 0; c < 5; ++c) acc[l][c] = mfma(B[k % 3][c], A[i][l], acc[l][c]);
        __builtin_amdgcn_sched_barrier(0);
      }
    }
  }
#pragma unroll
  for (int l = 0; l < 4; ++l)
#pragma unroll
    for (int c = 0; c < 5; ++c) *(LDSP f32x4*)(lds + ring + (l * 5 + c) * 1024 + l16) = acc[l][c];
  DG(T[6] = stamp();)
  combine<3, 5>(lds, xv, out, aw, rb2, cb, acc[0]);
  DG_OUT2();
}

extern "C" void kernel_launch(void* const* d_in, const int* in_sizes, int n_in, void* d_out, int out_size, void* d_ws,
                              size_t ws_size, hipStream_t stream) {
  const u16* x = (const u16*)d_in[0];
  const u16* wqkv = (const u16*)d_in[1];
  const u16* wphi = (const u16*)d_in[2];
  const u16* wo = (const u16*)d_in[3];
  const u16* w1 = (const u16*)d_in[5];
  const u16* b1 = (const u16*)d_in[6];
  const u16* w2 = (const u16*)d_in[7];
  (void)hipFuncSetAttribute((const void*)hedgehog_k1, hipFuncAttributeMaxDynamicSharedMemorySize, K1_LDS DG(+ 1024));
  (void)hipFuncSetAttribute((const void*)hedgehog_k2, hipFuncAttributeMaxDynamicSharedMemorySize, K2_LDS);
  hedgehog_k1<<<256, 512, K1_LDS DG(+ 1024), stream>>>(x, wqkv, wphi, wo, w1, b1, w2, (char*)d_ws);
  hedgehog_k2<<<256, 512, K2_LDS, stream>>>(x, wphi, (char*)d_ws, (u16*)d_out);
}
```

```cpp
#define ROT3
#include <hip/hip_runtime.h>
#include <stdint.h>

typedef unsigned short u16;
typedef __attribute__((ext_vector_type(2))) __bf16 bf16x2;
typedef __attribute__((ext_vector_type(8))) __bf16 bf16x8;
typedef __attribute__((ext_vector_type(4))) short s16x4;
typedef __attribute__((ext_vector_type(2))) float f32x2;
typedef __attribute__((ext_vector_type(4))) float f32x4;
typedef __attribute__((ext_vector_type(2))) unsigned u32x2;
typedef __attribute__((ext_vector_type(4))) unsigned u32x4;
#define LDSP __attribute__((address_space(3)))
#define DEVI __device__ __forceinline__

constexpr unsigned OFF_ACT = 0;
constexpr unsigned OFF_WT = OFF_ACT + 128u * 54u * 1024u;
constexpr unsigned OFF_OT = OFF_WT + 40u * 74u * 1024u;
constexpr unsigned OFF_QT = OFF_OT + 128u * 20u * 1024u;
constexpr unsigned OFF_KVP = OFF_QT + 32u * 2048u * 48u;
constexpr unsigned KVP_B = 3 * 1344;
constexpr unsigned OFF_SYNC = OFF_KVP + 32u * 16u * KVP_B;
constexpr unsigned OFF_FL1 = OFF_SYNC + 128;
constexpr unsigned OFF_FLR = OFF_FL1 + 256 * 64;
constexpr unsigned OFF_FO = OFF_FLR + 256 * 64;
constexpr unsigned WS_END = OFF_FO + 1024 * 64;

#define CODE_BYTES (51 * 1024)
#define CODE2_OFF (36 * 1024)
#define K2_NY 11
#define K1_MAP 1
constexpr float L2E = 1.4426950408889634f, LN2 = 0.6931471805599453f;

DEVI unsigned pk2(float a, float b) {
  bf16x2 v = {(__bf16)a, (__bf16)b};
  return __builtin_bit_cast(unsigned, v);
}
DEVI float bflo(unsigned u) { return __uint_as_float(u << 16); }
DEVI float bfhi(unsigned u) { return __uint_as_float(u & 0xffff0000u); }
DEVI f32x4 mfma(u32x4 a, u32x4 b, f32x4 c) {
  return __builtin_amdgcn_mfma_f32_16x16x32_bf16(__builtin_bit_cast(bf16x8, a), __builtin_bit_cast(bf16x8, b), c, 0, 0, 0);
}
DEVI u32x2 trread(LDSP char* p) {
  return __builtin_bit_cast(u32x2, __builtin_amdgcn_ds_read_tr16_b64_v4i16((LDSP s16x4*)p));
}
DEVI u32x4 trread2(LDSP char* p0, LDSP char* p1) {
  u32x2 a = trread(p0), b = trread(p1);
  return u32x4{a[0], a[1], b[0], b[1]};
}
DEVI float dot2(unsigned a, unsigned b, float c) {
  return __builtin_amdgcn_fdot2_f32_bf16(__builtin_bit_cast(bf16x2, a), __builtin_bit_cast(bf16x2, b), c, false);
}
DEVI float dot_ones(u32x4 a, float c) {
  c = dot2(a[0], 0x3f803f80u, c); c = dot2(a[1], 0x3f803f80u, c);
  c = dot2(a[2], 0x3f803f80u, c); c = dot2(a[3], 0x3f803f80u, c);
  return c;
}
DEVI float dot_self(u32x4 a, float c) {
  c = dot2(a[0], a[0], c); c = dot2(a[1], a[1], c); c = dot2(a[2], a[2], c); c = dot2(a[3], a[3], c);
  return c;
}
DEVI float red_g(float v) {
  v += __shfl_xor(v, 16);
  v += __shfl_xor(v, 32);
  return v;
}
DEVI void glds16(unsigned voff, const void* sbase, unsigned lds_dst) {
  unsigned keep;
  asm volatile("s_mov_b32 %0, m0\n\ts_mov_b32 m0, %3\n\ts_nop 0\n\tglobal_load_lds_dwordx4 %1, %2\n\ts_mov_b32 m0, %0"
               : "=&s"(keep) : "v"(voff), "s"(sbase), "s"(lds_dst) : "memory");
}
#define DG(...)
#define WAIT_VM(n) asm volatile("s_waitcnt vmcnt(" #n ")" ::: "memory")
#define WAIT_LGKM0() asm volatile("s_waitcnt lgkmcnt(0)" ::: "memory")

DEVI void feature_map(const f32x4 (&p)[5], unsigned (&o)[20]) {
  float mx = p[0][0], mn = p[0][0];
#pragma unroll
  for (int t = 0; t < 5; ++t)
#pragma unroll
    for (int r = 0; r < 4; ++r) { mx = fmaxf(mx, p[t][r]); mn = fminf(mn, p[t][r]); }
  const float a = -mx * L2E, b = mn * L2E;
  float e1[20], e2[20], s1 = 0.f, s2 = 0.f;
#pragma unroll
  for (int t = 0; t < 5; ++t)
#pragma unroll
    for (int r = 0; r < 4; ++r) {
      e1[4 * t + r] = __builtin_amdgcn_exp2f(fmaf(p[t][r], L2E, a));
      e2[4 * t + r] = __builtin_amdgcn_exp2f(fmaf(p[t][r], -L2E, b));
      s1 += e1[4 * t + r]; s2 += e2[4 * t + r];
    }
  const float r1 = __builtin_amdgcn_rcpf(s1), r2 = __builtin_amdgcn_rcpf(s2);
#pragma unroll
  for (int i = 0; i < 10; ++i) {
    o[i] = pk2(e1[2 * i] * r1, e1[2 * i + 1] * r1);
    o[10 + i] = pk2(e2[2 * i] * r2, e2[2 * i + 1] * r2);
  }
}
DEVI void head_proj(LDSP char* wl, const u32x4 (&b)[3], f32x4 (&p)[5]) {
#pragma unroll
  for (int t = 0; t < 5; ++t) {
    u32x4 a0 = trread2(wl + 0 * 40 + t * 8, wl + 4 * 40 + t * 8);
    u32x4 a1 = trread2(wl + 8 * 40 + t * 8, wl + 12 * 40 + t * 8);
    u32x2 h2 = trread(wl + 16 * 40 + t * 8);
    u32x4 a2 = {h2[0], h2[1], 0u, 0u};
    f32x4 c = {0.f, 0.f, 0.f, 0.f};
    c = mfma(a0, b[0], c);
    c = mfma(a1, b[1], c);
    c = mfma(a2, b[2], c);
    p[t] = c;
  }
}

constexpr unsigned XSLOT = 16384, WSUB = 5120, WSLOT = 3 * WSUB;
constexpr unsigned K1_W = 4 * XSLOT;
constexpr unsigned K1_WPHI = K1_W + 5 * WSLOT;
constexpr unsigned K1_ZERO = K1_WPHI + 3200;
constexpr unsigned K1_STAT = K1_ZERO + 768;
constexpr unsigned K1_CS = K1_STAT + 1024;
constexpr unsigned K1_BL = K1_CS + 960;
constexpr unsigned K1_SCR = K1_BL + 960;
constexpr unsigned K1_MB = K1_SCR + 256;
constexpr unsigned K1_RT = K1_MB + 16;
constexpr unsigned K1_LDS = K1_RT + 6144;
constexpr unsigned F_TAG = 159744;
constexpr unsigned F_LDS = F_TAG + 16;
constexpr unsigned PK_PITCH = 352, V_PITCH = 224, K1_VIMG = 128 * PK_PITCH;
constexpr unsigned K1_HO = K1_VIMG + 128 * V_PITCH;

DEVI void glds16n(unsigned voff, const void* sbase, unsigned lds_dst) {
  asm volatile("s_mov_b32 m0, %2\n\ts_nop 0\n\tglobal_load_lds_dwordx4 %0, %1" ::"v"(voff), "s"(sbase), "s"(lds_dst) : "memory");
}
DEVI void st16(__amdgpu_buffer_rsrc_t rs, u32x4 v, unsigned voff, unsigned soff) {
  __builtin_amdgcn_raw_buffer_store_b128(v, rs, voff + soff, 0, 16);
}
DEVI void st8(__amdgpu_buffer_rsrc_t rs, u32x2 v, unsigned voff, unsigned soff) {
  __builtin_amdgcn_raw_buffer_store_b64(v, rs, voff + soff, 0, 16);
}
#define STEP_SYNC() do { __builtin_amdgcn_s_waitcnt(0xC07F); __builtin_amdgcn_s_barrier(); __builtin_amdgcn_sched_barrier(0); } while (0)

DEVI void mb_arrive(LDSP char* lds, int i) {
  WAIT_LGKM0();
  if ((threadIdx.x & 63) == 0) __hip_atomic_fetch_add((LDSP unsigned*)(lds + K1_MB + 4 * i), 1u, __ATOMIC_RELAXED, __HIP_MEMORY_SCOPE_WORKGROUP);
}
DEVI void mb_wait(LDSP char* lds, int i) {
  for (int spin = 0; spin < (1 << 22); ++spin) {
    if (__builtin_amdgcn_readfirstlane(*(volatile LDSP unsigned*)(lds + K1_MB + 4 * i)) >= 6u) break;
    __builtin_amdgcn_s_sleep(1);
  }
  asm volatile("" ::: "memory");
  __builtin_amdgcn_sched_barrier(0);
}
struct K1C {
  LDSP char* lds;
  unsigned xa, wa[5], wr;
  __amdgpu_buffer_rsrc_t rs;
  const char* rsrc[2];
  unsigned rvo[2], rdst[2], scr, rrd, l16;
  bool tagger;
  u32x2 epoch;
  DG(unsigned long long t1;)
};
DEVI void rt_stage(K1C& S, int s) {
  if (s == 1) {
    if (S.tagger && S.l16 == 0) {
      const unsigned long long t = (((unsigned long long)S.epoch[1] << 32) | S.epoch[0]) + 1ull;
      *(LDSP u32x2*)(S.lds + F_TAG) = u32x2{(unsigned)t, (unsigned)(t >> 32)};
    }
    glds16n(S.rvo[0], S.rsrc[0], S.scr);
  } else {
    WAIT_VM(0);
    if (s < 17) {
      const u32x4 v = trread2(S.lds + S.rrd, S.lds + S.rrd + 512);
      st16(S.rs, v, S.l16, s == 7 ? S.rdst[0] : S.rdst[1]);
      WAIT_LGKM0();
      if (s == 7) glds16n(S.rvo[1], S.rsrc[1], S.scr);
    } else if (S.l16 == 0) {
      if (__hip_atomic_fetch_add((LDSP unsigned*)(S.lds + K1_MB + 8), 1u, __ATOMIC_RELAXED, __HIP_MEMORY_SCOPE_WORKGROUP) == 5u)
        __builtin_amdgcn_raw_buffer_store_b64(*(LDSP u32x2*)(S.lds + F_TAG), S.rs, OFF_FLR + blockIdx.x * 64, 0, 16);
    }
  }
}
template <int NC>
DEVI void c_read(K1C& S, int s, u32x4 (&xf)[4], u32x4 (&wf)[5]) {
  LDSP char* px = S.lds + ((s >> 1) & 3) * XSLOT + (S.xa ^ ((s & 1) << 6));
  LDSP char* pw = S.lds + S.wr;
  xf[0] = *(LDSP u32x4*)(px);
  wf[0] = trread2(pw + S.wa[0], pw + S.wa[0] + 1280);
#pragma unroll
  for (int r = 1; r < 4; ++r) xf[r] = *(LDSP u32x4*)(px + r * 2048);
#pragma unroll
  for (int c = 1; c < NC; ++c) wf[c] = trread2(pw + S.wa[c], pw + S.wa[c] + 1280);
  S.wr = S.wr == 4 * WSLOT ? 0 : S.wr + WSLOT;
}
template <int NC, int ROLE, bool READ, bool MINI = false>
DEVI void c_step(K1C& S, int s, const u32x4 (&xc)[4], const u32x4 (&wc)[5], u32x4 (&xn)[4], u32x4 (&wn)[5], f32x4 (&acc)[4][5],
                 f32x4 (&cs)[3], f32x4 (&s1)[4], f32x4 (&s2)[4], const u32x4& ones) {
  if (READ) {
    if (MINI) { WAIT_VM(0); mb_arrive(S.lds, 1); mb_wait(S.lds, 1); } else STEP_SYNC();
    c_read<NC>(S, s + 1, xn, wn);
  }
#pragma unroll
  for (int c = 0; c < NC; ++c)
#pragma unroll
    for (int r = 0; r < 4; ++r) acc[r][c] = mfma(wc[c], xc[r], acc[r][c]);
  if (ROLE) {
    constexpr int C0 = ROLE == 1 ? 0 : NC - 2, C1 = ROLE == 1 ? NC - 2 : NC;
#pragma unroll
    for (int r = 0; r < 4; ++r) {
      s1[r] = mfma(ones, xc[r], s1[r]);
      s2[r] = mfma(xc[r], xc[r], s2[r]);
    }
#pragma unroll
    for (int c = C0; c < C1; ++c) cs[c - C0] = mfma(wc[c], ones, cs[c - C0]);
  }
  if (READ) {
#pragma unroll
    for (int i = 0; i < 4 + 2 * NC; ++i) {
      __builtin_amdgcn_sched_group_barrier(0x8, 1, 0);
      __builtin_amdgcn_sched_group_barrier(0x100, 1, 0);
    }
  }
  __builtin_amdgcn_sched_barrier(0);
}
template <int NC, int ROLE>
DEVI void c_main(K1C& S, f32x4 (&acc)[4][5], f32x4 (&cs)[3], f32x4 (&s1)[4], f32x4 (&s2)[4]) {
  u32x4 xa[4], wa[5], xb[4], wb[5];
  u32x4 ones = {0x3f803f80u, 0x3f803f80u, 0x3f803f80u, 0x3f803f80u};
  asm volatile("" : "+v"(ones));
  c_read<NC>(S, 0, xa, wa);
  c_step<NC, ROLE, true, true>(S, 0, xa, wa, xb, wb, acc, cs, s1, s2, ones);
#pragma unroll 1
  for (int s = 1; s < 19; s += 2) {
    c_step<NC, ROLE, true>(S, s, xb, wb, xa, wa, acc, cs, s1, s2, ones);
    c_step<NC, ROLE, true>(S, s + 1, xa, wa, xb, wb, acc, cs, s1, s2, ones);
    if (s == 1 || s == 7 || s == 13 || s == 17) rt_stage(S, s);
  }
  c_step<NC, ROLE, false>(S, 19, xb, wb, xa, wa, acc, cs, s1, s2, ones);
}

struct K1H {
  LDSP char* lds;
  unsigned ldsbase;
  unsigned vw[8], vx[4];
  const char* pw; const char* px;
  unsigned incw, wrapw, wrapx;
  unsigned dw, dx;
  unsigned wa[5], wr;
  const char* pc2;
  const char* touch;
  DG(unsigned long long t1;)
};
template <int HP, int ODD, bool SKIP = false>
DEVI void h_issue(K1H& S) {
  if (!SKIP) {
#pragma unroll
    for (int i = 0; i < 8 - HP; ++i) glds16n(S.vw[i], S.pw, S.ldsbase + K1_W + HP * 8192 + i * 1024 + S.dw);
#pragma unroll
    for (int i = 0; i < 4; ++i) glds16n(S.vx[i], S.px, S.ldsbase + HP * 4096 + i * 1024 + S.dx);
  }
  S.pw += S.incw;
  S.pw -= (unsigned)(size_t)S.pw == S.wrapw ? 18 * S.incw : 0u;
  S.px += ODD ? 128 - 64 * 1280 : 64 * 1280;
  S.px -= (unsigned)(size_t)S.px == S.wrapx ? 1152u : 0u;
  S.dw = S.dw == 4 * WSLOT ? 0 : S.dw + WSLOT;
  S.dx = (S.dx + 8192) & 0xffff;
}
template <int HP, int G> DEVI void h_wait() {
  constexpr int N = G * (12 - HP);
  asm volatile("s_waitcnt vmcnt(%0)" ::"i"(N) : "memory");
}
DEVI void h_read(K1H& S, u32x4 (&wf)[5]) {
  LDSP char* pw = S.lds + S.wr;
#pragma unroll
  for (int c = 0; c < 5; ++c) wf[c] = trread2(pw + S.wa[c], pw + S.wa[c] + 1280);
  S.wr = S.wr == 4 * WSLOT ? 0 : S.wr + WSLOT;
}
template <int HP, int G, int ISSUE>
DEVI void h_step(K1H& S, const u32x4 (&wc)[5], u32x4 (&wn)[5], f32x4 (&cs)[5], const u32x4& ones) {
  if (G >= 0 && G < 9) h_wait<HP, (G >= 0 && G < 9 ? G : 0)>();
  if (G >= 0) STEP_SYNC();
#pragma unroll
  for (int c = 0; c < 5; ++c) cs[c] = mfma(wc[c], ones, cs[c]);
  h_read(S, wn);
  __builtin_amdgcn_sched_barrier(0);
  if (ISSUE) h_issue<HP, ISSUE - 1>(S);
  __builtin_amdgcn_sched_barrier(0);
}
template <int HP>
DEVI void h_main(K1H& S, f32x4 (&cs)[5]) {
  u32x4 wa[5], wb[5];
  u32x4 ones = {0x3f803f80u, 0x3f803f80u, 0x3f803f80u, 0x3f803f80u};
  asm volatile("" : "+v"(ones));
  h_issue<HP, 1, true>(S);
  h_issue<HP, 0>(S);
  h_issue<HP, 1>(S);
  if (HP == 0) {
    if ((threadIdx.x & 63) < 16) asm volatile("s_mov_b32 m0, %1\n\ts_nop 0\n\tglobal_load_lds_dword %0, off" ::"v"(S.touch), "s"(S.ldsbase + K1_SCR) : "memory");
  }
  mb_wait(S.lds, 0);
  h_read(S, wa);
  mb_wait(S.lds, 1);
  h_step<HP, -1, 1>(S, wa, wb, cs, ones);
  h_step<HP, 3, 2>(S, wb, wa, cs, ones);
#pragma unroll 1
  for (int s = 2; s < 14; s += 2) {
    h_step<HP, 3, 1>(S, wa, wb, cs, ones);
    h_step<HP, 3, 2>(S, wb, wa, cs, ones);
  }
  h_step<HP, 3, 1>(S, wa, wb, cs, ones);
  const int lane = threadIdx.x & 63;
  h_step<HP, 3, 0>(S, wb, wa, cs, ones);
  if (S.pc2) {
    asm volatile("s_mov_b32 m0, %1\n\ts_nop 0\n\tglobal_load_lds_dword %0, off" ::"v"(S.pc2 + min(CODE2_OFF + HP * 8192 + lane * 128, CODE_BYTES)), "s"(S.ldsbase + K1_SCR) : "memory");
  }
  h_step<HP, 2, 0>(S, wa, wb, cs, ones);
  h_step<HP, 1, 0>(S, wb, wa, cs, ones);
  h_step<HP, 0, 0>(S, wa, wb, cs, ones);
#pragma unroll
  for (int c = 0; c < 5; ++c) cs[c] = mfma(wb[c], ones, cs[c]);
  __builtin_amdgcn_s_setprio(0);
}

DEVI void phase1(LDSP char* lds, unsigned long long kpc, const u16* __restrict__ x, const u16* __restrict__ wqkv,
                 const u16* __restrict__ wphi, const u16* __restrict__ wo, const u16* __restrict__ w1,
                 const u16* __restrict__ b1, const u16* __restrict__ w2, char* __restrict__ ws) {
  const int tid = threadIdx.x, lane = tid & 63;
  const int wave = __builtin_amdgcn_readfirstlane(tid >> 6);
  const int b = blockIdx.x, xcd = b & 7, jx = b >> 3;
  const unsigned ldsbase = (unsigned)(size_t)lds;
  const bool helper = wave >= 6;
  DG(unsigned long long T[8]; T[0] = stamp(); T[7] = rstamp(); T[1] = T[6] = 0;)
  if (helper) {
    const char* pc = (const char*)(kpc & ~(unsigned long long)127) + min(((jx & 3) * 2 + (wave & 1)) * 8192 + lane * 128, CODE_BYTES);
    asm volatile("s_mov_b32 m0, %1\n\ts_nop 0\n\tglobal_load_lds_dword %0, off" ::"v"(pc), "s"(ldsbase + K1_SCR) : "memory");
  }
  if (tid == 0) { *(LDSP u32x4*)(lds + K1_MB) = u32x4{0u, 0u, 0u, 0u}; *(LDSP u32x2*)(lds + F_TAG + 8) = u32x2{0u, 0u}; }
  WAIT_LGKM0();
  asm volatile("" ::"s"(x), "s"(wqkv), "s"(wphi), "s"(wo), "s"(w1), "s"(b1), "s"(w2), "s"(ws));
  const int rb = 8 * (xcd >> 2) + (jx & 7);
  const char* xrow = (const char*)x + (size_t)rb * 128 * 1280;
  auto xvoff = [&](int pc) {
    const int row = pc * 8 + (lane >> 3);
    return (unsigned)(row * 1280 + (((lane & 7) ^ ((row >> 1) & 7)) << 4));
  };
  const unsigned x0 = xvoff(wave), x1 = x0 + 48 * 1280;
  if (!helper) {
    glds16n(x0, xrow, ldsbase + wave * 1024);
    glds16n(x0, xrow + 64 * 1280, ldsbase + 8192 + wave * 1024);
    if (wave < 2) {
      glds16n(x1, xrow, ldsbase + wave * 1024 + 6144);
      glds16n(x1, xrow + 64 * 1280, ldsbase + 8192 + wave * 1024 + 6144);
    }
  }
  __builtin_amdgcn_s_barrier();
  const int mh = wave & 1;
  const int nj = wave < 4 ? wave >> 1 : 2;
  const int cj = helper ? 2 * mh : nj;
  const int g = lane >> 4, m = lane & 15, q4 = m >> 2, p4 = lane & 3;
  const int cgi = jx >> 3;
  const bool atype = cgi < 2;
  const int grp = 2 * (xcd & 3) + (cgi & 1);
  const u16* W = atype ? wqkv : w1;
  const int ldw = atype ? 1920 : 1728;
  const int c0 = atype ? 80 * grp : (grp < 6 ? 224 * grp : 1344 + 192 * (grp - 6));
  const int sub = atype ? 640 : (grp < 6 ? 80 : 64);
  const unsigned incw = 64 * ldw;
  const int t1 = 1 + 3 * (((jx & 7) + cgi) % 3);
  auto wvoff = [&](int id) {
    const int sj = id / 5, ss = id - 5 * sj;
    const int c = 64 * ss + lane, rho = c / 10, cc = c - 10 * rho;
    const int k = (rho & 0x13) | ((rho & 4) << 1) | ((rho & 8) >> 1);
    const bool narrow = !atype && (grp >= 6 || sj == 2);
    return (unsigned)(k * ldw + c0 + sub * sj + 8 * ((narrow && cc >= 8) ? cc - 2 : cc)) * 2;
  };
  const unsigned wa0 = K1_W + cj * WSUB + (16 * (g >> 1) + 4 * (g & 1) + q4) * 160 + p4 * (atype ? 40 : 8);
  const unsigned cstr = atype ? 8 : 32;
  const __amdgpu_buffer_rsrc_t rs = __builtin_amdgcn_make_buffer_rsrc((void*)ws, 0, WS_END, 0x00020000);
  f32x4 acc[4][5];

  if (!helper) {
    auto xgrp = [&](int u, int xt) {
      const char* px = xrow + xt * 128 + (u & 1) * (64 * 1280);
      const unsigned d = ldsbase + u * 8192 + wave * 1024;
      glds16n(x0, px, d);
      if (wave < 2) glds16n(x1, px, d + 6144);
    };
    const unsigned dw = ldsbase + K1_W + wave * 1024;
    const unsigned v0 = wvoff(wave);
    glds16n(v0, W, dw);
    const unsigned v1 = wvoff(wave + 6);
    glds16n(v1, W, dw + 6144);
    const unsigned v2 = wvoff(wave < 3 ? wave + 12 : 14);
    if (wave < 3) glds16n(v2, W, dw + 12288);
    glds16n(v0, (const char*)W + incw, dw + WSLOT);
    glds16n(v1, (const char*)W + incw, dw + WSLOT + 6144);
    if (wave < 3) glds16n(v2, (const char*)W + incw, dw + WSLOT + 12288);
    xgrp(2, t1);
    K1C S;
    S.lds = lds;
    S.xa = (mh * 64 + m) * 128 + ((g ^ (m >> 1)) << 4);
#pragma unroll
    for (int c = 0; c < 5; ++c) S.wa[c] = wa0 + c * cstr;
    S.wr = 0;
    S.rs = rs; S.tagger = wave == 5; S.l16 = lane * 16;
    S.scr = ldsbase + K1_RT + wave * 1024;
    S.rrd = K1_RT + wave * 1024 + (lane >> 2) * 32 + (lane & 3) * 8;
#pragma unroll
    for (int j = 0; j < 2; ++j) {
      const int gi = b * 12 + wave * 2 + j, fid = gi < 2960 ? gi : 2959;
      const int cq = (fid >> 2) / 74, ks = (fid >> 2) - 74 * cq, rho = lane >> 1;
      S.rvo[j] = (ks < 54 ? rho : (((rho & 12) << 1) | ((rho & 16) >> 2) | (rho & 3))) * 1280 + (lane & 1) * 16;
      S.rsrc[j] = (ks < 54 ? (const char*)w2 + ks * 40960 : (const char*)wo + (ks - 54) * 40960) + (4 * cq + (fid & 3)) * 32;
      S.rdst[j] = gi < 2960 ? OFF_WT + ((4 * cq + (gi & 3)) * 74 + ks) * 1024 : WS_END;
    }
    DG(S.t1 = 0;)
    f32x4 cs[3], s1[4], s2[4];
#pragma unroll
    for (int i = 0; i < 4; ++i) {
      s1[i] = f32x4{0.f, 0.f, 0.f, 0.f}; s2[i] = s1[i]; cs[i % 3] = s1[i];
#pragma unroll
      for (int c = 0; c < 5; ++c) acc[i][c] = s1[i];
    }
    if (wave == 4 && lane < 48) *(LDSP u32x4*)(lds + K1_ZERO + lane * 16) = u32x4{0u, 0u, 0u, 0u};
    const bool five = atype || (grp < 6 && nj < 2);
    if (wave < 2) WAIT_VM(5); else if (wave == 2) WAIT_VM(4); else WAIT_VM(3);
    mb_arrive(lds, 0);
    S.epoch = __builtin_amdgcn_raw_buffer_load_b64(rs, OFF_SYNC, 0, 16);
    {
      const int col = c0 + sub * (lane / 10) + 8 * (lane % 10);
      const unsigned vo = atype ? tid * 16 : (col > 1720 ? 1720 : col) * 2;
      if (tid < (atype ? 200 : 30)) glds16n(vo, atype ? (const char*)wphi + grp * 3200 : (const char*)b1, ldsbase + K1_WPHI + wave * 1024);
    }
    mb_wait(lds, 0);
    DG(T[1] = stamp();)
    if (nj == 1) {
      if (five) { if (mh == 0) c_main<5, 1>(S, acc, cs, s1, s2); else c_main<5, 2>(S, acc, cs, s1, s2); }
      else { if (mh == 0) c_main<4, 1>(S, acc, cs, s1, s2); else c_main<4, 2>(S, acc, cs, s1, s2); }
      const int cb = mh == 0 ? 0 : (five ? 3 : 2), cn = mh == 0 ? (five ? 3 : 2) : 2;
      if (m == 0) {
#pragma unroll
        for (int c = 0; c < 3; ++c)
          if (c < cn) *(LDSP f32x4*)(lds + K1_CS + (80 + (cb + c) * 16 + 4 * g) * 4) = cs[c];
      }
      if (g == q4) {
#pragma unroll
        for (int r = 0; r < 4; ++r) {
          const float sq = (m & 2) ? ((m & 1) ? s2[r][3] : s2[r][2]) : ((m & 1) ? s2[r][1] : s2[r][0]);
          const float mean = s1[r][0] * (1.0f / 640.0f);
          const float var = fmaxf(sq * (1.0f / 640.0f) - mean * mean, 0.f);
          float rsd = __builtin_amdgcn_rsqf(var + 1e-5f);
          if (!atype) rsd *= L2E;
          *(LDSP f32x2*)(lds + K1_STAT + (mh * 64 + r * 16 + m) * 8) = f32x2{rsd, -mean * rsd};
        }
      }
      if (atype) {
#pragma unroll
        for (int r = 2; r < 4; ++r)
#pragma unroll
          for (int c = 0; c < 5; ++c) *(LDSP f32x4*)(lds + K1_HO + (mh * 2 + r - 2) * 5120 + c * 1024 + lane * 16) = acc[r][c];
      }
    } else {
      if (five) c_main<5, 0>(S, acc, cs, s1, s2); else c_main<4, 0>(S, acc, cs, s1, s2);
      if (!atype) {
#pragma unroll
        for (int c = 0; c < 5; ++c) *(LDSP f32x4*)(lds + K1_HO + (mh * 2 + (nj >> 1)) * 5120 + c * 1024 + lane * 16) = acc[3][c];
        if (nj == 0 && tid < 120) {
          const unsigned w = *(LDSP unsigned*)(lds + K1_WPHI + tid * 4);
          *(LDSP f32x2*)(lds + K1_BL + tid * 8) = f32x2{bflo(w) * L2E, bfhi(w) * L2E};
        }
      }
    }
    DG(T[6] = S.t1;)
  } else {
    __builtin_amdgcn_s_setprio(3);
    K1H S;
    S.lds = lds; S.ldsbase = ldsbase;
    S.incw = incw;
    S.pw = (const char*)W + 2 * t1 * incw;
    S.wrapw = (unsigned)(size_t)W + 20 * incw;
    S.px = xrow + t1 * 128 + 64 * 1280;
    S.wrapx = (unsigned)(size_t)xrow + 1280;
    S.dw = 2 * WSLOT; S.dx = 3 * 8192; S.wr = 0;
    DG(S.t1 = 0;)
#pragma unroll
    for (int i = 0; i < 4; ++i) {
      S.vx[i] = xvoff(4 * mh + i);
      glds16n(S.vx[i], S.px, ldsbase + mh * 4096 + i * 1024 + 3 * 8192);
    }
#pragma unroll
    for (int i = 0; i < 8; ++i) {
      S.vw[i] = wvoff(8 * mh + i < 15 ? 8 * mh + i : 14);
      if (8 * mh + i < 15) glds16n(S.vw[i], S.pw, ldsbase + K1_W + mh * 8192 + i * 1024 + 2 * WSLOT);
    }
    S.touch = lane < 12 ? ws + (size_t)lane * (2u << 20) : lane == 12 ? (const char*)w2 : lane == 13 ? (const char*)w2 + (2u << 20) : lane == 14 ? (const char*)wo : (const char*)wphi;
#pragma unroll
    for (int c = 0; c < 5; ++c) { S.wa[c] = wa0 + c * cstr; acc[0][c] = f32x4{0.f, 0.f, 0.f, 0.f}; }
    S.pc2 = jx < 2 ? (const char*)(kpc & ~(unsigned long long)127) : nullptr;
    if (mh == 0) h_main<0>(S, acc[0]); else h_main<1>(S, acc[0]);
    DG(T[6] = S.t1;)
    if (m == 0) {
#pragma unroll
      for (int c = 0; c < 5; ++c) *(LDSP f32x4*)(lds + K1_CS + (cj * 80 + c * 16 + 4 * g) * 4) = acc[0][c];
    }
  }
  DG(T[2] = stamp();)
  WAIT_LGKM0();
  __builtin_amdgcn_s_barrier();
  asm volatile("" ::: "memory");
  DG(T[3] = stamp();)
#define DG_OUT1() DG({ T[5] = stamp(); unsigned long long re = rstamp(); if (lane == 0) { unsigned long long* dp = (unsigned long long*)(ws + OFF_DBG) + (size_t)(b * 8 + wave) * 16; for (int i = 0; i < 8; ++i) dp[i] = T[i]; dp[8] = re; } if (lane < 20) ((unsigned*)(ws + OFF_DBG + (2u << 20)))[(b * 8 + wave) * 32 + lane] = *(LDSP unsigned*)(lds + K1_LDS + wave * 128 + lane * 4) - (unsigned)T[0]; })

  if (!atype) {
    auto round = [&](const f32x4 (&a)[5], int ej, int rt) {
      const bool five = grp < 6 && ej < 2;
      const f32x2 st = *(LDSP f32x2*)(lds + K1_STAT + (rt * 16 + m) * 8);
      u32x2 pk[5];
#pragma unroll
      for (int c = 0; c < 5; ++c) {
        if (c == 4 && !five) { pk[4] = u32x2{0u, 0u}; break; }
        const f32x4 cs = *(LDSP f32x4*)(lds + K1_CS + (ej * 80 + c * 16 + 4 * g) * 4);
        const f32x4 bl = *(LDSP f32x4*)(lds + K1_BL + (ej * 80 + c * 16 + 4 * g) * 4);
        float res[4];
#pragma unroll
        for (int e = 0; e < 4; ++e) {
          const float hl = fmaf(a[c][e], st[0], fmaf(st[1], cs[e], bl[e]));
          const float ex = __builtin_amdgcn_exp2f(hl);
          const float rc = __builtin_amdgcn_rcpf(fmaf(ex, ex + 2.f, 2.f));
          res[e] = hl * fmaf(rc, -2.f * LN2, LN2);
        }
        pk[c] = u32x2{pk2(res[0], res[1]), pk2(res[2], res[3])};
      }
      const int T0 = (c0 + sub * ej) >> 4;
      const unsigned base = OFF_ACT + ((rb * 8 + rt) * 54 + (T0 >> 1)) * 1024;
      if (!(T0 & 1)) {
        st16(rs, u32x4{pk[0][0], pk[0][1], pk[1][0], pk[1][1]}, lane * 16, base);
        st16(rs, u32x4{pk[2][0], pk[2][1], pk[3][0], pk[3][1]}, lane * 16, base + 1024);
        if (five) st8(rs, pk[4], lane * 16, base + 2048);
      } else {
        st8(rs, pk[0], lane * 16, base + 8);
        st16(rs, u32x4{pk[1][0], pk[1][1], pk[2][0], pk[2][1]}, lane * 16, base + 1024);
        st16(rs, u32x4{pk[3][0], pk[3][1], pk[4][0], pk[4][1]}, lane * 16, base + 2048);
      }
    };
    if (helper) {
#pragma unroll
      for (int r = 0; r < 2; ++r)
#pragma unroll
        for (int c = 0; c < 5; ++c) acc[r][c] = *(LDSP f32x4*)(lds + K1_HO + (mh * 2 + r) * 5120 + c * 1024 + lane * 16);
    }
#pragma unroll 1
    for (int it = 0; it < (helper ? 1 : 2); ++it) {
      round(acc[0], helper ? 0 : nj, mh * 4 + (helper ? 3 : 2 * it));
      if (it == 0 || nj == 1) round(acc[1], helper ? 2 : nj, mh * 4 + (helper ? 3 : 2 * it + 1));
#pragma unroll
      for (int c = 0; c < 5; ++c) { acc[0][c] = acc[2][c]; acc[1][c] = acc[3][c]; }
    }
    DG(T[4] = stamp();)
    DG_OUT1();
    return;
  }

  auto normalise = [&](const f32x4 (&a)[5], const f32x4 (&cs)[5], int rt, u32x4 (&bq)[3], unsigned one) {
    const f32x2 st = *(LDSP f32x2*)(lds + K1_STAT + (rt * 16 + m) * 8);
#pragma unroll
    for (int c = 0; c < 5; ++c) {
      float v[4];
#pragma unroll
      for (int e = 0; e < 4; ++e) v[e] = fmaf(a[c][e], st[0], st[1] * cs[c][e]);
      bq[c >> 1][(c & 1) * 2] = pk2(v[0], v[1]);
      bq[c >> 1][(c & 1) * 2 + 1] = pk2(v[2], v[3]);
    }
    bq[2][2] = one;
    bq[2][3] = 0u;
  };
  f32x4 csv[5];
  if (!helper && nj != 1) {
#pragma unroll
    for (int c = 0; c < 5; ++c) csv[c] = *(LDSP f32x4*)(lds + K1_CS + (nj * 80 + c * 16 + 4 * g) * 4);
#pragma unroll
    for (int r = 0; r < 4; ++r) {
      u32x4 bq[3];
      normalise(acc[r], csv, mh * 4 + r, bq, nj == 2 ? 0x00003f80u : 0u);
      if (nj == 0) {
        const unsigned so = OFF_QT + (4 * grp * 128 + rb * 8 + mh * 4 + r) * 768;
#pragma unroll
        for (int i = 0; i < 3; ++i) st16(rs, bq[i], g * (128 * 768) + m * 16, so + i * 256);
      } else {
#pragma unroll
        for (int i = 0; i < 3; ++i) *(LDSP u32x4*)(lds + K1_VIMG + (mh * 64 + r * 16 + m) * V_PITCH + g * 48 + i * 16) = bq[i];
      }
    }
  }
  if (helper || nj != 2) {
    const int kr0 = helper ? 3 : (nj == 1 ? 0 : 2);
    if (kr0) {
#pragma unroll
      for (int c = 0; c < 5; ++c) acc[0][c] = *(LDSP f32x4*)(lds + K1_HO + (mh * 2 + kr0 - 2) * 5120 + c * 1024 + lane * 16);
    }
#pragma unroll
    for (int c = 0; c < 5; ++c) csv[c] = *(LDSP f32x4*)(lds + K1_CS + (80 + c * 16 + 4 * g) * 4);
    LDSP char* wl = lds + ((p4 == g) ? K1_WPHI + g * 800 + q4 * 40 : K1_ZERO);
#pragma unroll 1
    for (int kr = kr0; kr < (kr0 ? kr0 + 1 : 2); ++kr) {
      u32x4 bq[3];
      normalise(acc[0], csv, mh * 4 + kr, bq, 0u);
      f32x4 pk[5];
      head_proj(wl, bq, pk);
      unsigned ph[20];
      feature_map(pk, ph);
      LDSP char* dst = lds + (mh * 64 + kr * 16 + m) * PK_PITCH + g * 80;
#pragma unroll
      for (int i = 0; i < 5; ++i) *(LDSP u32x4*)(dst + i * 16) = u32x4{ph[4 * i], ph[4 * i + 1], ph[4 * i + 2], ph[4 * i + 3]};
#pragma unroll
      for (int c = 0; c < 5; ++c) acc[0][c] = acc[1][c];
    }
  }
  DG(T[4] = stamp();)
  WAIT_LGKM0();
  __builtin_amdgcn_s_barrier();
  asm volatile("" ::: "memory");
  {
#pragma unroll 1
    for (int job = wave; job < 12; job += 8) {
      const int hh = job & 3, ft = job >> 2;
      LDSP char* pa = lds + (4 * g + q4) * PK_PITCH + hh * 80 + ft * 32 + p4 * 8;
      LDSP char* pb = lds + K1_VIMG + (4 * g + q4) * V_PITCH + hh * 48 + p4 * 8;
      f32x4 d0 = {0.f, 0.f, 0.f, 0.f}, d1 = {0.f, 0.f, 0.f, 0.f};
#pragma unroll
      for (int kk = 0; kk < 4; ++kk) {
        u32x4 a = trread2(pa + (32 * kk) * PK_PITCH, pa + (32 * kk + 16) * PK_PITCH);
        u32x4 v0 = trread2(pb + (32 * kk) * V_PITCH, pb + (32 * kk + 16) * V_PITCH);
        u32x4 v1 = trread2(pb + (32 * kk) * V_PITCH + 32, pb + (32 * kk + 16) * V_PITCH + 32);
        d0 = mfma(a, v0, d0);
        d1 = mfma(a, v1, d1);
      }
      const unsigned so = OFF_KVP + ((4 * grp + hh) * 16 + rb) * KVP_B + 1344 * ft;
      st16(rs, __builtin_bit_cast(u32x4, d0), m * 64 + g * 16, so);
      if (m < 5) st16(rs, __builtin_bit_cast(u32x4, d1), (16 + m) * 64 + g * 16, so);
    }
  }
  DG_OUT1();
}

constexpr unsigned K2_YD = 0;
constexpr unsigned K2_SD = 4 * 15360;
constexpr unsigned K2_KVT = K2_SD + 4 * 20480;
constexpr unsigned K2_ZERO = K2_KVT + 1920;
constexpr unsigned K2_WPHI = K2_ZERO + 1792;
constexpr unsigned K2_FLAG = K2_WPHI + 3200;
constexpr unsigned K2_OWN = K2_FLAG + 16;
constexpr unsigned K2_LDS = K2_OWN + 8192;

DEVI u32x4 bld(__amdgpu_buffer_rsrc_t rs, unsigned voff, unsigned soff) {
  return __builtin_amdgcn_raw_buffer_load_b128(rs, voff, soff, 16);
}
DEVI u32x4 bld_sc1(__amdgpu_buffer_rsrc_t rs, unsigned voff, unsigned soff) {
  return __builtin_amdgcn_raw_buffer_load_b128(rs, voff, soff, 16);
}
template <int N>
DEVI void y_stream(__amdgpu_buffer_rsrc_t rs, unsigned act, unsigned wt, int rot, bool more, f32x4 (&acc)[4][5]) {
  const unsigned l16 = (threadIdx.x & 63) * 16;
  unsigned ab[4];
#pragma unroll
  for (int l = 0; l < 4; ++l) ab[l] = act + ((l + rot) & 3) * (54 * 1024);
  u32x4 A[3][4], B[3][5];
  auto load = [&](int i) {
#pragma unroll
    for (int l = 0; l < 4; ++l) A[i % 3][l] = bld(rs, l16, ab[l] + i * 1024);
#pragma unroll
    for (int c = 0; c < 5; ++c) B[i % 3][c] = bld(rs, l16, wt + c * (74 * 1024) + i * 1024);
  };
  load(0);
  load(1);
#pragma unroll
  for (int i = 0; i < N; ++i) {
    if (i + 2 < N) load(i + 2);
    else if (i + 2 == N) { if (more) load(N); }
    __builtin_amdgcn_sched_barrier(0);
#pragma unroll
    for (int l = 0; l < 4; ++l)
#pragma unroll
      for (int c = 0; c < 5; ++c) acc[l][c] = mfma(B[i % 3][c], A[i % 3][l], acc[l][c]);
    __builtin_amdgcn_sched_barrier(0);
  }
  if (more) {
#pragma unroll
    for (int l = 0; l < 4; ++l)
#pragma unroll
      for (int c = 0; c < 5; ++c) acc[l][c] = mfma(B[N % 3][c], A[N % 3][l], acc[l][c]);
  }
}

template <int C0, int C1>
DEVI void combine(LDSP char* lds, const u32x2 (&xv)[3], u16* out, int rt, int rb2, int cb, const f32x4 (&own)[5]) {
  const int lane = threadIdx.x & 63;
  const unsigned l16 = lane * 16;
  const size_t orow = (size_t)(64 * rb2 + 16 * rt + (lane & 15)) * 640 + 80 * cb + 4 * (lane >> 4);
  WAIT_LGKM0();
  __builtin_amdgcn_s_barrier();
  asm volatile("" ::: "memory");
#pragma unroll
  for (int c = C0; c < C1; ++c) {
    f32x4 y = C0 == 0 ? own[c] : *(LDSP f32x4*)(lds + K2_OWN + (rt * 2 + c - 3) * 1024 + l16);
#pragma unroll
    for (int l = 1; l < 4; ++l)
      y += *(LDSP f32x4*)(lds + K2_YD + ((rt - l) & 3) * 15360 + ((l - 1) * 5 + c) * 1024 + l16);
    LDSP f32x4* sp = (LDSP f32x4*)(lds + K2_SD + (rt * 5 + c) * 1024 + l16);
    const f32x4 s = (sp[0] + sp[1280]) + (sp[2560] + sp[3840]);
    float o[4];
#pragma unroll
    for (int e = 0; e < 4; ++e) {
      const float gate = y[e] * __builtin_amdgcn_rcpf(1.f + __builtin_amdgcn_exp2f(-y[e] * L2E));
      const unsigned xw = xv[c - C0][e >> 1];
      o[e] = fmaf(s[e], gate, (e & 1) ? bfhi(xw) : bflo(xw));
    }
    *(u32x2*)(out + orow + 16 * c) = u32x2{pk2(o[0], o[1]), pk2(o[2], o[3])};
  }
}

template <int NW>
DEVI bool poll_tags(__amdgpu_buffer_rsrc_t rs, unsigned off, int cnt, u32x2 tag) {
  const int lane = threadIdx.x & 63;
  const unsigned vo = (lane < cnt ? lane : 0) * 64;
  for (int spin = 0; spin < (1 << 20); ++spin) {
    asm volatile("" ::: "memory");
    bool ok = true;
#pragma unroll
    for (int i = 0; i < NW; ++i) {
      const u32x2 w = __builtin_amdgcn_raw_buffer_load_b64(rs, vo, off + i * 4096, 16);
      ok = ok && w[0] == tag[0] && w[1] == tag[1];
    }
    if (__all(ok)) { asm volatile("" ::: "memory"); return true; }
    __builtin_amdgcn_s_sleep(4);
  }
  return false;
}

DEVI void go_set(LDSP char* lds, int i) {
  if ((threadIdx.x & 63) == 0) *(volatile LDSP unsigned*)(lds + F_TAG + 8 + 4 * i) = 1u;
}
DEVI void go_wait(LDSP char* lds, int i) {
  for (int spin = 0; spin < (1 << 24); ++spin) {
    if (__builtin_amdgcn_readfirstlane(*(volatile LDSP unsigned*)(lds + F_TAG + 8 + 4 * i))) break;
    __builtin_amdgcn_s_sleep(2);
  }
  asm volatile("" ::: "memory");
}

DEVI void phase2(LDSP char* lds, u32x2 tag, const u16* __restrict__ x, const u16* __restrict__ wphi, char* __restrict__ ws,
                 u16* __restrict__ out) {
  const int tid = threadIdx.x, lane = tid & 63;
  const int wave = __builtin_amdgcn_readfirstlane(tid >> 6);
  const int g = lane >> 4, m = lane & 15, q4 = m >> 2, p4 = lane & 3;
  const int b = blockIdx.x, xcd = b & 7, jx = b >> 3;
  const int rb2 = 8 * (xcd >> 1) + (jx & 7), cb = 4 * (xcd & 1) + (jx >> 3);
  const __amdgpu_buffer_rsrc_t rs = __builtin_amdgcn_make_buffer_rsrc((void*)ws, 0, WS_END, 0x00020000);
  const unsigned l16 = lane * 16;
  DG(unsigned long long T[8]; T[0] = stamp(); T[7] = rstamp(); T[1] = T[2] = T[3] = T[4] = T[5] = T[6] = 0;)
#define DG_OUT2() DG({ unsigned long long re = rstamp(); if (lane == 0) { unsigned long long* dp = (unsigned long long*)(ws + OFF_DBG + (1u << 20)) + (size_t)(b * 8 + wave) * 16; for (int i = 0; i < 8; ++i) dp[i] = T[i]; dp[8] = re; } })
  asm volatile("" ::"s"(x), "s"(wphi), "s"(out));
  if (wave == 4) {
    *(LDSP u32x4*)(lds + K2_ZERO + l16) = u32x4{0u, 0u, 0u, 0u};
    if (lane < 48) *(LDSP u32x4*)(lds + K2_ZERO + 1024 + l16) = u32x4{0u, 0u, 0u, 0u};
    if (lane == 0) *(LDSP u32x4*)(lds + K2_FLAG) = u32x4{0u, 0u, 0u, 0u};
  }
  WAIT_LGKM0();
  __builtin_amdgcn_s_barrier();
  const unsigned act = OFF_ACT + 4 * rb2 * (54 * 1024), wt = OFF_WT + 5 * cb * (74 * 1024);
  const u16* xt = x + (size_t)(64 * rb2 + 16 * (wave & 3) + m) * 640 + 80 * cb + 4 * g;
  f32x4 acc[4][5];
#pragma unroll
  for (int r = 0; r < 4; ++r)
#pragma unroll
    for (int c = 0; c < 5; ++c) acc[r][c] = f32x4{0.f, 0.f, 0.f, 0.f};

  if (wave < 4) {
    u32x2 xv[3];
#pragma unroll
    for (int c = 0; c < 3; ++c) xv[c] = *(const u32x2*)(xt + 16 * c);
    if (wave == 0) {
      poll_tags<4>(rs, OFF_FLR, 64, tag);
      poll_tags<1>(rs, OFF_FL1 + (128 + (rb2 >> 1) * 8) * 64, 8, tag);
      go_set(lds, 0);
    } else {
      go_wait(lds, 0);
    }
    DG(T[1] = stamp();)
    const unsigned k0 = (wave < 2 ? 14 * wave : 2 + 13 * wave) * 1024;
    y_stream<13>(rs, act + k0, wt + k0, wave, wave < 2, acc);
    DG(T[3] = stamp();)
#pragma unroll
    for (int l = 1; l < 4; ++l)
#pragma unroll
      for (int c = 0; c < 5; ++c)
        *(LDSP f32x4*)(lds + K2_YD + wave * 15360 + ((l - 1) * 5 + c) * 1024 + l16) = acc[l][c];
#pragma unroll
    for (int c = 3; c < 5; ++c) *(LDSP f32x4*)(lds + K2_OWN + (wave * 2 + c - 3) * 1024 + l16) = acc[0][c];
    DG(T[4] = stamp();)
    combine<0, 3>(lds, xv, out, wave, rb2, cb, acc[0]);
    if (b == 0 && wave == 0 && poll_tags<4>(rs, OFF_FLR, 64, tag) && lane == 0) __builtin_amdgcn_raw_buffer_store_b64(tag, rs, OFF_SYNC, 0, 16);
    DG(T[6] = stamp();)
    DG_OUT2();
    return;
  }

  const int aw = wave - 4;
  const int h = 4 * xcd + (jx & 3), sb = jx >> 2;
  const int tok = sb * 256 + aw * 64 + lane;
  __builtin_amdgcn_s_setprio(2);
  u32x4 wv = *(const u32x4*)(wphi + h * 400 + (lane < 50 ? lane : 49) * 8);
  if (aw == 0) {
    poll_tags<1>(rs, OFF_FL1 + (h >> 2) * 16 * 64, 16, tag);
    go_set(lds, 1);
  } else {
    go_wait(lds, 1);
  }
  u32x4 bq[3];
#pragma unroll
  for (int s = 0; s < 3; ++s) bq[s] = bld(rs, (unsigned)(tok >> 4) * 768 + m * 16, OFF_QT + h * (128 * 768) + s * 256);
  int item = aw * 53 + (lane < 53 ? lane : 52);
  item = item < 210 ? item : 209;
  const int d = item / 10, fq = item - 10 * d;
  u32x4 part[16];
#pragma unroll
  for (int r = 0; r < 16; ++r) part[r] = bld(rs, (unsigned)((fq >> 2) * 1344 + d * 64 + (fq & 3) * 16), OFF_KVP + (h * 16 + r) * KVP_B);
  *(LDSP u32x4*)(lds + K2_WPHI + aw * 800 + (lane < 50 ? lane : 49) * 16) = wv;
  unsigned ph[20];
  {
    LDSP char* wl = lds + ((p4 == g) ? K2_WPHI + aw * 800 + q4 * 40 : K2_ZERO);
    f32x4 pq[5];
    head_proj(wl, bq, pq);
    feature_map(pq, ph);
#pragma unroll
    for (int i = 0; i < 20; ++i) asm volatile("" : "+v"(ph[i]));
  }
  DG(T[1] = stamp();)
  {
    f32x4 sum = __builtin_bit_cast(f32x4, part[0]);
#pragma unroll
    for (int r = 1; r < 16; ++r) sum += __builtin_bit_cast(f32x4, part[r]);
    *(LDSP u32x2*)(lds + K2_KVT + d * 80 + fq * 8) = u32x2{pk2(sum[0], sum[1]), pk2(sum[2], sum[3])};
    WAIT_LGKM0();
    if (lane == 0) *(volatile LDSP unsigned*)(lds + K2_FLAG + aw * 4) = 1u;
    for (int spin = 0; spin < (1 << 22); ++spin) {
      const u32x4 f = *(volatile LDSP u32x4*)(lds + K2_FLAG);
      if (__builtin_amdgcn_readfirstlane(f[0] & f[1] & f[2] & f[3])) break;
      __builtin_amdgcn_s_sleep(1);
    }
    asm volatile("" ::: "memory");
  }
  DG(T[2] = stamp();)
  {
    LDSP char* kl = lds + (((m >> 2) == g) ? K2_KVT + (m & 3) * 80 : K2_ZERO);
    f32x4 nd[6];
#pragma unroll
    for (int t = 0; t < 6; ++t) {
      f32x4 c = {0.f, 0.f, 0.f, 0.f};
#pragma unroll
      for (int s = 0; s < 5; ++s)
        c = mfma(*(LDSP u32x4*)(kl + t * 320 + s * 16), u32x4{ph[4 * s], ph[4 * s + 1], ph[4 * s + 2], ph[4 * s + 3]}, c);
      nd[t] = c;
    }
    const float rd = __builtin_amdgcn_rcpf(nd[5][0] + 1e-6f);
    unsigned ow[10];
#pragma unroll
    for (int t = 0; t < 5; ++t) {
      ow[2 * t] = pk2(nd[t][0] * rd, nd[t][1] * rd);
      ow[2 * t + 1] = pk2(nd[t][2] * rd, nd[t][3] * rd);
    }
    const int e0 = (5 * h + 1) >> 1, e8 = (h & 1) ? e0 - 1 : e0 + 2;
    const unsigned vo = (unsigned)(tok >> 4) * 20480 + m * 16;
    const bool odd = h & 1;
    st16(rs, odd ? u32x4{ow[2], ow[3], ow[4], ow[5]} : u32x4{ow[0], ow[1], ow[2], ow[3]}, vo, OFF_OT + e0 * 256);
    st16(rs, odd ? u32x4{ow[6], ow[7], ow[8], ow[9]} : u32x4{ow[4], ow[5], ow[6], ow[7]}, vo, OFF_OT + (e0 + 1) * 256);
    st8(rs, odd ? u32x2{ow[0], ow[1]} : u32x2{ow[8], ow[9]}, vo, OFF_OT + e8 * 256 + (h & 1) * 8);
    WAIT_VM(0);
    if (lane == 0) __builtin_amdgcn_raw_buffer_store_b64(tag, rs, OFF_FO + ((sb * 4 + aw) * 32 + h) * 64, 0, 16);
  }
  DG(T[3] = stamp();)
  const unsigned ring = K2_SD + aw * 20480;
  u32x2 xv[3];
#pragma unroll
  for (int c = 0; c < 2; ++c) xv[c] = *(const u32x2*)(xt + 48 + 16 * c);
  xv[2] = xv[1];
  go_wait(lds, 0);
  {
    u32x4 B[5][5], A[5][4];
    auto loadb = [&](int i) {
#pragma unroll
      for (int c = 0; c < 5; ++c) B[i][c] = bld(rs, l16, wt + c * (74 * 1024) + (54 + 5 * aw + i) * 1024);
    };
    loadb(0);
    loadb(1);
    loadb(2);
    const unsigned ob = OFF_OT + (4 * rb2 * 20 + 5 * aw) * 1024;
    poll_tags<1>(rs, OFF_FO + (rb2 * 32 + 8 * aw) * 64, 8, tag);
    DG(T[4] = stamp();)
#pragma unroll
    for (int i = 0; i < 5; ++i)
#pragma unroll
      for (int l = 0; l < 4; ++l) A[i][l] = bld(rs, l16, ob + l * 20480 + i * 1024);
#pragma unroll
    for (int i = 0; i < 5; ++i) {
      if (i == 1) loadb(3);
      if (i == 2) loadb(4);
      __builtin_amdgcn_sched_barrier(0);
#pragma unroll
      for (int l = 0; l < 4; ++l)
#pragma unroll
        for (int c = 0; c < 5; ++c) acc[l][c] = mfma(B[i][c], A[i][l], acc[l][c]);
      __builtin_amdgcn_sched_barrier(0);
    }
  }
#pragma unroll
  for (int l = 0; l < 4; ++l)
#pragma unroll
    for (int c = 0; c < 5; ++c) *(LDSP f32x4*)(lds + ring + (l * 5 + c) * 1024 + l16) = acc[l][c];
  DG(T[6] = stamp();)
  combine<3, 5>(lds, xv, out, aw, rb2, cb, acc[0]);
  DG_OUT2();
}

static_assert(K1_LDS + 1024 <= F_TAG && K2_LDS <= F_TAG, "LDS map");

__global__ __launch_bounds__(512, 2) void hedgehog_fused(const u16* __restrict__ x, const u16* __restrict__ wqkv,
                                                         const u16* __restrict__ wphi, const u16* __restrict__ wo,
                                                         const u16* __restrict__ w1, const u16* __restrict__ b1,
                                                         const u16* __restrict__ w2, char* __restrict__ ws,
                                                         u16* __restrict__ out) {
  extern __shared__ __attribute__((aligned(16))) char smem[];
  LDSP char* lds = (LDSP char*)smem;
  phase1(lds, __builtin_amdgcn_s_getpc(), x, wqkv, wphi, wo, w1, b1, w2, ws);
  int tid = threadIdx.x;
  asm volatile("" : "+v"(tid));
  const __amdgpu_buffer_rsrc_t rs = __builtin_amdgcn_make_buffer_rsrc((void*)ws, 0, WS_END, 0x00020000);
  WAIT_VM(0);
  WAIT_LGKM0();
  __builtin_amdgcn_s_barrier();
  asm volatile("" ::: "memory");
  const u32x2 tag = *(LDSP u32x2*)(lds + F_TAG);
  if (tid == 0) {
    const int b = blockIdx.x, xcd = b & 7, jx = b >> 3;
    const int rb = 8 * (xcd >> 2) + (jx & 7), grp = 2 * (xcd & 3) + ((jx >> 3) & 1);
    __builtin_amdgcn_raw_buffer_store_b64(tag, rs, OFF_FL1 + (jx < 16 ? grp * 16 + rb : 128 + rb * 8 + grp) * 64, 0, 16);
  }
  phase2(lds, tag, x, wphi, ws, out);
}

extern "C" void kernel_launch(void* const* d_in, const int* in_sizes, int n_in, void* d_out, int out_size, void* d_ws,
                              size_t ws_size, hipStream_t stream) {
  const u16* x = (const u16*)d_in[0];
  const u16* wqkv = (const u16*)d_in[1];
  const u16* wphi = (const u16*)d_in[2];
  const u16* wo = (const u16*)d_in[3];
  const u16* w1 = (const u16*)d_in[5];
  const u16* b1 = (const u16*)d_in[6];
  const u16* w2 = (const u16*)d_in[7];
  (void)hipFuncSetAttribute((const void*)hedgehog_fused, hipFuncAttributeMaxDynamicSharedMemorySize, F_LDS);
  hedgehog_fused<<<256, 512, F_LDS, stream>>>(x, wqkv, wphi, wo, w1, b1, w2, (char*)d_ws, (u16*)d_out);
}
```

```cpp
#define ROT3
#include <hip/hip_runtime.h>
#include <stdint.h>

typedef unsigned short u16;
typedef __attribute__((ext_vector_type(2))) __bf16 bf16x2;
typedef __attribute__((ext_vector_type(8))) __bf16 bf16x8;
typedef __attribute__((ext_vector_type(4))) short s16x4;
typedef __attribute__((ext_vector_type(2))) float f32x2;
typedef __attribute__((ext_vector_type(4))) float f32x4;
typedef __attribute__((ext_vector_type(2))) unsigned u32x2;
typedef __attribute__((ext_vector_type(4))) unsigned u32x4;
#define LDSP __attribute__((address_space(3)))
#define DEVI __device__ __forceinline__

constexpr unsigned OFF_ACT = 0;
constexpr unsigned OFF_WT = OFF_ACT + 128u * 54u * 1024u;
constexpr unsigned OFF_OT = OFF_WT + 40u * 74u * 1024u;
constexpr unsigned OFF_QT = OFF_OT + 128u * 20u * 1024u;
constexpr unsigned OFF_KVP = OFF_QT + 32u * 2048u * 48u;
constexpr unsigned KVP_B = 3 * 1344;
constexpr unsigned OFF_SYNC = OFF_KVP + 32u * 16u * KVP_B;
constexpr unsigned OFF_FL1 = OFF_SYNC + 128;
constexpr unsigned OFF_FLR = OFF_FL1 + 256 * 64;
constexpr unsigned OFF_FO = OFF_FLR + 256 * 64;
constexpr unsigned WS_END = OFF_FO + 1024 * 64;

#define CODE_BYTES (48 * 1024)
#define CODE2_OFF (36 * 1024)
#define K2_NY 11
#define K1_MAP 1
constexpr float L2E = 1.4426950408889634f, LN2 = 0.6931471805599453f;

DEVI unsigned pk2(float a, float b) {
  bf16x2 v = {(__bf16)a, (__bf16)b};
  return __builtin_bit_cast(unsigned, v);
}
DEVI float bflo(unsigned u) { return __uint_as_float(u << 16); }
DEVI float bfhi(unsigned u) { return __uint_as_float(u & 0xffff0000u); }
DEVI f32x4 mfma(u32x4 a, u32x4 b, f32x4 c) {
  return __builtin_amdgcn_mfma_f32_16x16x32_bf16(__builtin_bit_cast(bf16x8, a), __builtin_bit_cast(bf16x8, b), c, 0, 0, 0);
}
DEVI u32x2 trread(LDSP char* p) {
  return __builtin_bit_cast(u32x2, __builtin_amdgcn_ds_read_tr16_b64_v4i16((LDSP s16x4*)p));
}
DEVI u32x4 trread2(LDSP char* p0, LDSP char* p1) {
  u32x2 a = trread(p0), b = trread(p1);
  return u32x4{a[0], a[1], b[0], b[1]};
}
DEVI float dot2(unsigned a, unsigned b, float c) {
  return __builtin_amdgcn_fdot2_f32_bf16(__builtin_bit_cast(bf16x2, a), __builtin_bit_cast(bf16x2, b), c, false);
}
DEVI float dot_ones(u32x4 a, float c) {
  c = dot2(a[0], 0x3f803f80u, c); c = dot2(a[1], 0x3f803f80u, c);
  c = dot2(a[2], 0x3f803f80u, c); c = dot2(a[3], 0x3f803f80u, c);
  return c;
}
DEVI float dot_self(u32x4 a, float c) {
  c = dot2(a[0], a[0], c); c = dot2(a[1], a[1], c); c = dot2(a[2], a[2], c); c = dot2(a[3], a[3], c);
  return c;
}
DEVI float red_g(float v) {
  v += __shfl_xor(v, 16);
  v += __shfl_xor(v, 32);
  return v;
}
DEVI void glds16(unsigned voff, const void* sbase, unsigned lds_dst) {
  unsigned keep;
  asm volatile("s_mov_b32 %0, m0\n\ts_mov_b32 m0, %3\n\ts_nop 0\n\tglobal_load_lds_dwordx4 %1, %2\n\ts_mov_b32 m0, %0"
               : "=&s"(keep) : "v"(voff), "s"(sbase), "s"(lds_dst) : "memory");
}
#define DG(...)
#define WAIT_VM(n) asm volatile("s_waitcnt vmcnt(" #n ")" ::: "memory")
#define WAIT_LGKM0() asm volatile("s_waitcnt lgkmcnt(0)" ::: "memory")

DEVI void feature_map(const f32x4 (&p)[5], unsigned (&o)[20]) {
  float mx = p[0][0], mn = p[0][0];
#pragma unroll
  for (int t = 0; t < 5; ++t)
#pragma unroll
    for (int r = 0; r < 4; ++r) { mx = fmaxf(mx, p[t][r]); mn = fminf(mn, p[t][r]); }
  const float a = -mx * L2E, b = mn * L2E;
  float e1[20], e2[20], s1 = 0.f, s2 = 0.f;
#pragma unroll
  for (int t = 0; t < 5; ++t)
#pragma unroll
    for (int r = 0; r < 4; ++r) {
      e1[4 * t + r] = __builtin_amdgcn_exp2f(fmaf(p[t][r], L2E, a));
      e2[4 * t + r] = __builtin_amdgcn_exp2f(fmaf(p[t][r], -L2E, b));
      s1 += e1[4 * t + r]; s2 += e2[4 * t + r];
    }
  const float r1 = __builtin_amdgcn_rcpf(s1), r2 = __builtin_amdgcn_rcpf(s2);
#pragma unroll
  for (int i = 0; i < 10; ++i) {
    o[i] = pk2(e1[2 * i] * r1, e1[2 * i + 1] * r1);
    o[10 + i] = pk2(e2[2 * i] * r2, e2[2 * i + 1] * r2);
  }
}
DEVI void head_proj(LDSP char* wl, const u32x4 (&b)[3], f32x4 (&p)[5]) {
#pragma unroll
  for (int t = 0; t < 5; ++t) {
    u32x4 a0 = trread2(wl + 0 * 40 + t * 8, wl + 4 * 40 + t * 8);
    u32x4 a1 = trread2(wl + 8 * 40 + t * 8, wl + 12 * 40 + t * 8);
    u32x2 h2 = trread(wl + 16 * 40 + t * 8);
    u32x4 a2 = {h2[0], h2[1], 0u, 0u};
    f32x4 c = {0.f, 0.f, 0.f, 0.f};
    c = mfma(a0, b[0], c);
    c = mfma(a1, b[1], c);
    c = mfma(a2, b[2], c);
    p[t] = c;
  }
}

constexpr unsigned XSLOT = 16384, WSUB = 5120, WSLOT = 3 * WSUB;
constexpr unsigned K1_W = 4 * XSLOT;
constexpr unsigned K1_WPHI = K1_W + 5 * WSLOT;
constexpr unsigned K1_ZERO = K1_WPHI + 3200;
constexpr unsigned K1_STAT = K1_ZERO + 768;
constexpr unsigned K1_CS = K1_STAT + 1024;
constexpr unsigned K1_BL = K1_CS + 960;
constexpr unsigned K1_SCR = K1_BL + 960;
constexpr unsigned K1_MB = K1_SCR + 256;
constexpr unsigned K1_RT = K1_MB + 16;
constexpr unsigned K1_LDS = K1_RT + 10240;
constexpr unsigned F_TAG = 160832;
constexpr unsigned F_LDS = F_TAG + 16;
constexpr unsigned PK_PITCH = 352, V_PITCH = 224, K1_VIMG = 128 * PK_PITCH;
constexpr unsigned K1_HO = K1_VIMG + 128 * V_PITCH;

DEVI void glds16n(unsigned voff, const void* sbase, unsigned lds_dst) {
  asm volatile("s_mov_b32 m0, %2\n\ts_nop 0\n\tglobal_load_lds_dwordx4 %0, %1" ::"v"(voff), "s"(sbase), "s"(lds_dst) : "memory");
}
DEVI void st16(__amdgpu_buffer_rsrc_t rs, u32x4 v, unsigned voff, unsigned soff) {
  __builtin_amdgcn_raw_buffer_store_b128(v, rs, voff + soff, 0, 16);
}
DEVI void st8(__amdgpu_buffer_rsrc_t rs, u32x2 v, unsigned voff, unsigned soff) {
  __builtin_amdgcn_raw_buffer_store_b64(v, rs, voff + soff, 0, 16);
}
#define STEP_SYNC() do { __builtin_amdgcn_s_waitcnt(0xC07F); __builtin_amdgcn_s_barrier(); __builtin_amdgcn_sched_barrier(0); } while (0)

DEVI void mb_arrive(LDSP char* lds, int i) {
  WAIT_LGKM0();
  if ((threadIdx.x & 63) == 0) __hip_atomic_fetch_add((LDSP unsigned*)(lds + K1_MB + 4 * i), 1u, __ATOMIC_RELAXED, __HIP_MEMORY_SCOPE_WORKGROUP);
}
DEVI void mb_wait(LDSP char* lds, int i) {
  for (int spin = 0; spin < (1 << 22); ++spin) {
    if (__builtin_amdgcn_readfirstlane(*(volatile LDSP unsigned*)(lds + K1_MB + 4 * i)) >= 6u) break;
    __builtin_amdgcn_s_sleep(1);
  }
  asm volatile("" ::: "memory");
  __builtin_amdgcn_sched_barrier(0);
}
struct K1C {
  LDSP char* lds;
  unsigned xa, wa[5], wr;
  DG(unsigned long long t1;)
};
template <int NC>
DEVI void c_read(K1C& S, int s, u32x4 (&xf)[4], u32x4 (&wf)[5]) {
  LDSP char* px = S.lds + ((s >> 1) & 3) * XSLOT + (S.xa ^ ((s & 1) << 6));
  LDSP char* pw = S.lds + S.wr;
  xf[0] = *(LDSP u32x4*)(px);
  wf[0] = trread2(pw + S.wa[0], pw + S.wa[0] + 1280);
#pragma unroll
  for (int r = 1; r < 4; ++r) xf[r] = *(LDSP u32x4*)(px + r * 2048);
#pragma unroll
  for (int c = 1; c < NC; ++c) wf[c] = trread2(pw + S.wa[c], pw + S.wa[c] + 1280);
  S.wr = S.wr == 4 * WSLOT ? 0 : S.wr + WSLOT;
}
template <int NC, int ROLE, bool READ, bool MINI = false>
DEVI void c_step(K1C& S, int s, const u32x4 (&xc)[4], const u32x4 (&wc)[5], u32x4 (&xn)[4], u32x4 (&wn)[5], f32x4 (&acc)[4][5],
                 f32x4 (&cs)[3], f32x4 (&s1)[4], f32x4 (&s2)[4], const u32x4& ones) {
  if (READ) {
    if (MINI) { WAIT_VM(0); mb_arrive(S.lds, 1); mb_wait(S.lds, 1); } else STEP_SYNC();
    c_read<NC>(S, s + 1, xn, wn);
  }
#pragma unroll
  for (int c = 0; c < NC; ++c)
#pragma unroll
    for (int r = 0; r < 4; ++r) acc[r][c] = mfma(wc[c], xc[r], acc[r][c]);
  if (ROLE) {
    constexpr int C0 = ROLE == 1 ? 0 : NC - 2, C1 = ROLE == 1 ? NC - 2 : NC;
#pragma unroll
    for (int r = 0; r < 4; ++r) {
      s1[r] = mfma(ones, xc[r], s1[r]);
      s2[r] = mfma(xc[r], xc[r], s2[r]);
    }
#pragma unroll
    for (int c = C0; c < C1; ++c) cs[c - C0] = mfma(wc[c], ones, cs[c - C0]);
  }
  if (READ) {
#pragma unroll
    for (int i = 0; i < 4 + 2 * NC; ++i) {
      __builtin_amdgcn_sched_group_barrier(0x8, 1, 0);
      __builtin_amdgcn_sched_group_barrier(0x100, 1, 0);
    }
  }
  __builtin_amdgcn_sched_barrier(0);
}
template <int NC, int ROLE>
DEVI void c_main(K1C& S, f32x4 (&acc)[4][5], f32x4 (&cs)[3], f32x4 (&s1)[4], f32x4 (&s2)[4]) {
  u32x4 xa[4], wa[5], xb[4], wb[5];
  u32x4 ones = {0x3f803f80u, 0x3f803f80u, 0x3f803f80u, 0x3f803f80u};
  asm volatile("" : "+v"(ones));
  c_read<NC>(S, 0, xa, wa);
  c_step<NC, ROLE, true, true>(S, 0, xa, wa, xb, wb, acc, cs, s1, s2, ones);
#pragma unroll 1
  for (int s = 1; s < 19; s += 2) {
    c_step<NC, ROLE, true>(S, s, xb, wb, xa, wa, acc, cs, s1, s2, ones);
    c_step<NC, ROLE, true>(S, s + 1, xa, wa, xb, wb, acc, cs, s1, s2, ones);
  }
  c_step<NC, ROLE, false>(S, 19, xb, wb, xa, wa, acc, cs, s1, s2, ones);
}

struct K1H {
  LDSP char* lds;
  unsigned ldsbase;
  unsigned vw[8], vx[4];
  const char* pw; const char* px;
  unsigned incw, wrapw, wrapx;
  unsigned dw, dx;
  unsigned wa[5], wr;
  __amdgpu_buffer_rsrc_t rs;
  const char* w2; const char* wo;
  unsigned rv2, rvo, rrd, l16;
  int gi;
  const char* pc2;
  const char* touch;
  DG(unsigned long long t1;)
};
template <int HP, int ODD, bool SKIP = false>
DEVI void h_issue(K1H& S) {
  if (!SKIP) {
#pragma unroll
    for (int i = 0; i < 8 - HP; ++i) glds16n(S.vw[i], S.pw, S.ldsbase + K1_W + HP * 8192 + i * 1024 + S.dw);
#pragma unroll
    for (int i = 0; i < 4; ++i) glds16n(S.vx[i], S.px, S.ldsbase + HP * 4096 + i * 1024 + S.dx);
  }
  S.pw += S.incw;
  S.pw -= (unsigned)(size_t)S.pw == S.wrapw ? 18 * S.incw : 0u;
  S.px += ODD ? 128 - 64 * 1280 : 64 * 1280;
  S.px -= (unsigned)(size_t)S.px == S.wrapx ? 1152u : 0u;
  S.dw = S.dw == 4 * WSLOT ? 0 : S.dw + WSLOT;
  S.dx = (S.dx + 8192) & 0xffff;
}
template <int HP>
DEVI void h_retile(K1H& S, int s) {
  if (s >= 11) {
    const int i = s - 11, gi = S.gi + i, cq = (gi >> 2) / 74, ks = (gi >> 2) - 74 * cq;
    LDSP char* pr = S.lds + S.rrd + (i == 5 ? 0 : i) * 1024;
    st16(S.rs, trread2(pr, pr + 512), S.l16, gi < 2960 ? OFF_WT + ((4 * cq + (gi & 3)) * 74 + ks) * 1024 : WS_END);
  }
  if (s < 12) {
    const int i = s - 6, gi = S.gi + i, fid = gi < 2960 ? gi : 2959, cq = (fid >> 2) / 74, ks = (fid >> 2) - 74 * cq;
    const char* src = (ks < 54 ? S.w2 + ks * 40960 : S.wo + (ks - 54) * 40960) + (4 * cq + (fid & 3)) * 32;
    glds16n(ks < 54 ? S.rv2 : S.rvo, src, S.ldsbase + K1_RT + HP * 5120 + (i == 5 ? 0 : i) * 1024);
  }
  __builtin_amdgcn_sched_barrier(0);
}
template <int HP, int G> DEVI void h_wait() {
  constexpr int N = G * (12 - HP);
  asm volatile("s_waitcnt vmcnt(%0)" ::"i"(N) : "memory");
}
DEVI void h_read(K1H& S, u32x4 (&wf)[5]) {
  LDSP char* pw = S.lds + S.wr;
#pragma unroll
  for (int c = 0; c < 5; ++c) wf[c] = trread2(pw + S.wa[c], pw + S.wa[c] + 1280);
  S.wr = S.wr == 4 * WSLOT ? 0 : S.wr + WSLOT;
}
template <int HP, int G, int ISSUE>
DEVI void h_step(K1H& S, const u32x4 (&wc)[5], u32x4 (&wn)[5], f32x4 (&cs)[5], const u32x4& ones) {
  if (G >= 0 && G < 9) h_wait<HP, (G >= 0 && G < 9 ? G : 0)>();
  if (G >= 0) STEP_SYNC();
#pragma unroll
  for (int c = 0; c < 5; ++c) cs[c] = mfma(wc[c], ones, cs[c]);
  h_read(S, wn);
  __builtin_amdgcn_sched_barrier(0);
  if (ISSUE) h_issue<HP, ISSUE - 1>(S);
  __builtin_amdgcn_sched_barrier(0);
}
template <int HP>
DEVI void h_main(K1H& S, f32x4 (&cs)[5]) {
  u32x4 wa[5], wb[5];
  u32x4 ones = {0x3f803f80u, 0x3f803f80u, 0x3f803f80u, 0x3f803f80u};
  asm volatile("" : "+v"(ones));
  h_issue<HP, 1, true>(S);
  h_issue<HP, 0>(S);
  h_issue<HP, 1>(S);
  if (HP == 0) {
    if ((threadIdx.x & 63) < 16) asm volatile("s_mov_b32 m0, %1\n\ts_nop 0\n\tglobal_load_lds_dword %0, off" ::"v"(S.touch), "s"(S.ldsbase + K1_SCR) : "memory");
  }
  mb_wait(S.lds, 0);
  h_read(S, wa);
  mb_wait(S.lds, 1);
  h_step<HP, -1, 1>(S, wa, wb, cs, ones);
  h_step<HP, 3, 2>(S, wb, wa, cs, ones);
#pragma unroll 1
  for (int s = 2; s < 14; s += 2) {
    h_step<HP, 3, 1>(S, wa, wb, cs, ones);
    if (s >= 6) h_retile<HP>(S, s);
    h_step<HP, 3, 2>(S, wb, wa, cs, ones);
    if (s >= 6) h_retile<HP>(S, s + 1);
  }
  h_step<HP, 3, 1>(S, wa, wb, cs, ones);
  h_retile<HP>(S, 14);
  const int lane = threadIdx.x & 63;
  h_step<HP, 3, 0>(S, wb, wa, cs, ones);
  h_retile<HP>(S, 15);
  if (S.pc2) {
    asm volatile("s_mov_b32 m0, %1\n\ts_nop 0\n\tglobal_load_lds_dword %0, off" ::"v"(S.pc2 + min(CODE2_OFF + HP * 8192 + lane * 128, CODE_BYTES)), "s"(S.ldsbase + K1_SCR) : "memory");
  }
  h_step<HP, 2, 0>(S, wa, wb, cs, ones);
  h_retile<HP>(S, 16);
  h_step<HP, 1, 0>(S, wb, wa, cs, ones);
  h_step<HP, 0, 0>(S, wa, wb, cs, ones);
  if (lane == 0) {
    if (__hip_atomic_fetch_add((LDSP unsigned*)(S.lds + K1_MB + 8), 1u, __ATOMIC_RELAXED, __HIP_MEMORY_SCOPE_WORKGROUP) == 1u)
      __builtin_amdgcn_raw_buffer_store_b64(*(LDSP u32x2*)(S.lds + F_TAG), S.rs, OFF_FLR + blockIdx.x * 64, 0, 16);
  }
#pragma unroll
  for (int c = 0; c < 5; ++c) cs[c] = mfma(wb[c], ones, cs[c]);
  __builtin_amdgcn_s_setprio(0);
}

DEVI void phase1(LDSP char* lds, unsigned long long kpc, const u16* __restrict__ x, const u16* __restrict__ wqkv,
                 const u16* __restrict__ wphi, const u16* __restrict__ wo, const u16* __restrict__ w1,
                 const u16* __restrict__ b1, const u16* __restrict__ w2, char* __restrict__ ws) {
  const int tid = threadIdx.x, lane = tid & 63;
  const int wave = __builtin_amdgcn_readfirstlane(tid >> 6);
  const int b = blockIdx.x, xcd = b & 7, jx = b >> 3;
  const unsigned ldsbase = (unsigned)(size_t)lds;
  const bool helper = wave >= 6;
  DG(unsigned long long T[8]; T[0] = stamp(); T[7] = rstamp(); T[1] = T[6] = 0;)
  if (helper) {
    const char* pc = (const char*)(kpc & ~(unsigned long long)127) + min(((jx & 3) * 2 + (wave & 1)) * 8192 + lane * 128, CODE_BYTES);
    asm volatile("s_mov_b32 m0, %1\n\ts_nop 0\n\tglobal_load_lds_dword %0, off" ::"v"(pc), "s"(ldsbase + K1_SCR) : "memory");
  }
  if (tid == 0) { *(LDSP u32x4*)(lds + K1_MB) = u32x4{0u, 0u, 0u, 0u}; *(LDSP u32x2*)(lds + F_TAG + 8) = u32x2{0u, 0u}; }
  WAIT_LGKM0();
  asm volatile("" ::"s"(x), "s"(wqkv), "s"(wphi), "s"(wo), "s"(w1), "s"(b1), "s"(w2), "s"(ws));
  const int rb = 8 * (xcd >> 2) + (jx & 7);
  const char* xrow = (const char*)x + (size_t)rb * 128 * 1280;
  auto xvoff = [&](int pc) {
    const int row = pc * 8 + (lane >> 3);
    return (unsigned)(row * 1280 + (((lane & 7) ^ ((row >> 1) & 7)) << 4));
  };
  const unsigned x0 = xvoff(wave), x1 = x0 + 48 * 1280;
  if (!helper) {
    glds16n(x0, xrow, ldsbase + wave * 1024);
    glds16n(x0, xrow + 64 * 1280, ldsbase + 8192 + wave * 1024);
    if (wave < 2) {
      glds16n(x1, xrow, ldsbase + wave * 1024 + 6144);
      glds16n(x1, xrow + 64 * 1280, ldsbase + 8192 + wave * 1024 + 6144);
    }
  }
  __builtin_amdgcn_s_barrier();
  const int mh = wave & 1;
  const int nj = wave < 4 ? wave >> 1 : 2;
  const int cj = helper ? 2 * mh : nj;
  const int g = lane >> 4, m = lane & 15, q4 = m >> 2, p4 = lane & 3;
  const int cgi = jx >> 3;
  const bool atype = cgi < 2;
  const int grp = 2 * (xcd & 3) + (cgi & 1);
  const u16* W = atype ? wqkv : w1;
  const int ldw = atype ? 1920 : 1728;
  const int c0 = atype ? 80 * grp : (grp < 6 ? 224 * grp : 1344 + 192 * (grp - 6));
  const int sub = atype ? 640 : (grp < 6 ? 80 : 64);
  const unsigned incw = 64 * ldw;
  const int t1 = 1 + 3 * (((jx & 7) + cgi) % 3);
  auto wvoff = [&](int id) {
    const int sj = id / 5, ss = id - 5 * sj;
    const int c = 64 * ss + lane, rho = c / 10, cc = c - 10 * rho;
    const int k = (rho & 0x13) | ((rho & 4) << 1) | ((rho & 8) >> 1);
    const bool narrow = !atype && (grp >= 6 || sj == 2);
    return (unsigned)(k * ldw + c0 + sub * sj + 8 * ((narrow && cc >= 8) ? cc - 2 : cc)) * 2;
  };
  const unsigned wa0 = K1_W + cj * WSUB + (16 * (g >> 1) + 4 * (g & 1) + q4) * 160 + p4 * (atype ? 40 : 8);
  const unsigned cstr = atype ? 8 : 32;
  const __amdgpu_buffer_rsrc_t rs = __builtin_amdgcn_make_buffer_rsrc((void*)ws, 0, WS_END, 0x00020000);
  f32x4 acc[4][5];

  if (!helper) {
    auto xgrp = [&](int u, int xt) {
      const char* px = xrow + xt * 128 + (u & 1) * (64 * 1280);
      const unsigned d = ldsbase + u * 8192 + wave * 1024;
      glds16n(x0, px, d);
      if (wave < 2) glds16n(x1, px, d + 6144);
    };
    const unsigned dw = ldsbase + K1_W + wave * 1024;
    const unsigned v0 = wvoff(wave);
    glds16n(v0, W, dw);
    const unsigned v1 = wvoff(wave + 6);
    glds16n(v1, W, dw + 6144);
    const unsigned v2 = wvoff(wave < 3 ? wave + 12 : 14);
    if (wave < 3) glds16n(v2, W, dw + 12288);
    glds16n(v0, (const char*)W + incw, dw + WSLOT);
    glds16n(v1, (const char*)W + incw, dw + WSLOT + 6144);
    if (wave < 3) glds16n(v2, (const char*)W + incw, dw + WSLOT + 12288);
    xgrp(2, t1);
    K1C S;
    S.lds = lds;
    S.xa = (mh * 64 + m) * 128 + ((g ^ (m >> 1)) << 4);
#pragma unroll
    for (int c = 0; c < 5; ++c) S.wa[c] = wa0 + c * cstr;
    S.wr = 0;
    DG(S.t1 = 0;)
    f32x4 cs[3], s1[4], s2[4];
#pragma unroll
    for (int i = 0; i < 4; ++i) {
      s1[i] = f32x4{0.f, 0.f, 0.f, 0.f}; s2[i] = s1[i]; cs[i % 3] = s1[i];
#pragma unroll
      for (int c = 0; c < 5; ++c) acc[i][c] = s1[i];
    }
    if (wave == 4 && lane < 48) *(LDSP u32x4*)(lds + K1_ZERO + lane * 16) = u32x4{0u, 0u, 0u, 0u};
    const bool five = atype || (grp < 6 && nj < 2);
    if (wave < 2) WAIT_VM(5); else if (wave == 2) WAIT_VM(4); else WAIT_VM(3);
    mb_arrive(lds, 0);
    {
      const int col = c0 + sub * (lane / 10) + 8 * (lane % 10);
      const unsigned vo = atype ? tid * 16 : (col > 1720 ? 1720 : col) * 2;
      if (tid < (atype ? 200 : 30)) glds16n(vo, atype ? (const char*)wphi + grp * 3200 : (const char*)b1, ldsbase + K1_WPHI + wave * 1024);
    }
    if (tid == 320) {
      const u32x2 e = __builtin_amdgcn_raw_buffer_load_b64(rs, OFF_SYNC, 0, 16);
      const unsigned long long t = (((unsigned long long)e[1] << 32) | e[0]) + 1ull;
      *(LDSP u32x2*)(lds + F_TAG) = u32x2{(unsigned)t, (unsigned)(t >> 32)};
    }
    mb_wait(lds, 0);
    DG(T[1] = stamp();)
    if (nj == 1) {
      if (five) { if (mh == 0) c_main<5, 1>(S, acc, cs, s1, s2); else c_main<5, 2>(S, acc, cs, s1, s2); }
      else { if (mh == 0) c_main<4, 1>(S, acc, cs, s1, s2); else c_main<4, 2>(S, acc, cs, s1, s2); }
      const int cb = mh == 0 ? 0 : (five ? 3 : 2), cn = mh == 0 ? (five ? 3 : 2) : 2;
      if (m == 0) {
#pragma unroll
        for (int c = 0; c < 3; ++c)
          if (c < cn) *(LDSP f32x4*)(lds + K1_CS + (80 + (cb + c) * 16 + 4 * g) * 4) = cs[c];
      }
      if (g == q4) {
#pragma unroll
        for (int r = 0; r < 4; ++r) {
          const float sq = (m & 2) ? ((m & 1) ? s2[r][3] : s2[r][2]) : ((m & 1) ? s2[r][1] : s2[r][0]);
          const float mean = s1[r][0] * (1.0f / 640.0f);
          const float var = fmaxf(sq * (1.0f / 640.0f) - mean * mean, 0.f);
          float rsd = __builtin_amdgcn_rsqf(var + 1e-5f);
          if (!atype) rsd *= L2E;
          *(LDSP f32x2*)(lds + K1_STAT + (mh * 64 + r * 16 + m) * 8) = f32x2{rsd, -mean * rsd};
        }
      }
      if (atype) {
#pragma unroll
        for (int r = 2; r < 4; ++r)
#pragma unroll
          for (int c = 0; c < 5; ++c) *(LDSP f32x4*)(lds + K1_HO + (mh * 2 + r - 2) * 5120 + c * 1024 + lane * 16) = acc[r][c];
      }
    } else {
      if (five) c_main<5, 0>(S, acc, cs, s1, s2); else c_main<4, 0>(S, acc, cs, s1, s2);
      if (!atype) {
#pragma unroll
        for (int c = 0; c < 5; ++c) *(LDSP f32x4*)(lds + K1_HO + (mh * 2 + (nj >> 1)) * 5120 + c * 1024 + lane * 16) = acc[3][c];
        if (nj == 0 && tid < 120) {
          const unsigned w = *(LDSP unsigned*)(lds + K1_WPHI + tid * 4);
          *(LDSP f32x2*)(lds + K1_BL + tid * 8) = f32x2{bflo(w) * L2E, bfhi(w) * L2E};
        }
      }
    }
    DG(T[6] = S.t1;)
  } else {
    __builtin_amdgcn_s_setprio(3);
    K1H S;
    S.lds = lds; S.ldsbase = ldsbase;
    S.incw = incw;
    S.pw = (const char*)W + 2 * t1 * incw;
    S.wrapw = (unsigned)(size_t)W + 20 * incw;
    S.px = xrow + t1 * 128 + 64 * 1280;
    S.wrapx = (unsigned)(size_t)xrow + 1280;
    S.dw = 2 * WSLOT; S.dx = 3 * 8192; S.wr = 0;
    DG(S.t1 = 0;)
#pragma unroll
    for (int i = 0; i < 4; ++i) {
      S.vx[i] = xvoff(4 * mh + i);
      glds16n(S.vx[i], S.px, ldsbase + mh * 4096 + i * 1024 + 3 * 8192);
    }
#pragma unroll
    for (int i = 0; i < 8; ++i) {
      S.vw[i] = wvoff(8 * mh + i < 15 ? 8 * mh + i : 14);
      if (8 * mh + i < 15) glds16n(S.vw[i], S.pw, ldsbase + K1_W + mh * 8192 + i * 1024 + 2 * WSLOT);
    }
    S.touch = lane < 12 ? ws + (size_t)lane * (2u << 20) : lane == 12 ? (const char*)w2 : lane == 13 ? (const char*)w2 + (2u << 20) : lane == 14 ? (const char*)wo : (const char*)wphi;
#pragma unroll
    for (int c = 0; c < 5; ++c) { S.wa[c] = wa0 + c * cstr; acc[0][c] = f32x4{0.f, 0.f, 0.f, 0.f}; }
    S.rs = rs; S.l16 = lane * 16; S.w2 = (const char*)w2; S.wo = (const char*)wo;
    S.rrd = K1_RT + mh * 5120 + (lane >> 2) * 32 + (lane & 3) * 8;
    S.gi = b * 12 + mh * 6;
    S.rv2 = (lane >> 1) * 1280 + (lane & 1) * 16;
    S.rvo = ((((lane >> 1) & 12) << 1) | (((lane >> 1) & 16) >> 2) | ((lane >> 1) & 3)) * 1280 + (lane & 1) * 16;
    S.pc2 = jx < 2 ? (const char*)(kpc & ~(unsigned long long)127) : nullptr;
    if (mh == 0) h_main<0>(S, acc[0]); else h_main<1>(S, acc[0]);
    DG(T[6] = S.t1;)
    if (m == 0) {
#pragma unroll
      for (int c = 0; c < 5; ++c) *(LDSP f32x4*)(lds + K1_CS + (cj * 80 + c * 16 + 4 * g) * 4) = acc[0][c];
    }
  }
  DG(T[2] = stamp();)
  WAIT_LGKM0();
  __builtin_amdgcn_s_barrier();
  asm volatile("" ::: "memory");
  DG(T[3] = stamp();)
#define DG_OUT1() DG({ T[5] = stamp(); unsigned long long re = rstamp(); if (lane == 0) { unsigned long long* dp = (unsigned long long*)(ws + OFF_DBG) + (size_t)(b * 8 + wave) * 16; for (int i = 0; i < 8; ++i) dp[i] = T[i]; dp[8] = re; } if (lane < 20) ((unsigned*)(ws + OFF_DBG + (2u << 20)))[(b * 8 + wave) * 32 + lane] = *(LDSP unsigned*)(lds + K1_LDS + wave * 128 + lane * 4) - (unsigned)T[0]; })

  if (!atype) {
    auto round = [&](const f32x4 (&a)[5], int ej, int rt) {
      const bool five = grp < 6 && ej < 2;
      const f32x2 st = *(LDSP f32x2*)(lds + K1_STAT + (rt * 16 + m) * 8);
      u32x2 pk[5];
#pragma unroll
      for (int c = 0; c < 5; ++c) {
        if (c == 4 && !five) { pk[4] = u32x2{0u, 0u}; break; }
        const f32x4 cs = *(LDSP f32x4*)(lds + K1_CS + (ej * 80 + c * 16 + 4 * g) * 4);
        const f32x4 bl = *(LDSP f32x4*)(lds + K1_BL + (ej * 80 + c * 16 + 4 * g) * 4);
        float res[4];
#pragma unroll
        for (int e = 0; e < 4; ++e) {
          const float hl = fmaf(a[c][e], st[0], fmaf(st[1], cs[e], bl[e]));
          const float ex = __builtin_amdgcn_exp2f(hl);
          const float rc = __builtin_amdgcn_rcpf(fmaf(ex, ex + 2.f, 2.f));
          res[e] = hl * fmaf(rc, -2.f * LN2, LN2);
        }
        pk[c] = u32x2{pk2(res[0], res[1]), pk2(res[2], res[3])};
      }
      const int T0 = (c0 + sub * ej) >> 4;
      const unsigned base = OFF_ACT + ((rb * 8 + rt) * 54 + (T0 >> 1)) * 1024;
      if (!(T0 & 1)) {
        st16(rs, u32x4{pk[0][0], pk[0][1], pk[1][0], pk[1][1]}, lane * 16, base);
        st16(rs, u32x4{pk[2][0], pk[2][1], pk[3][0], pk[3][1]}, lane * 16, base + 1024);
        if (five) st8(rs, pk[4], lane * 16, base + 2048);
      } else {
        st8(rs, pk[0], lane * 16, base + 8);
        st16(rs, u32x4{pk[1][0], pk[1][1], pk[2][0], pk[2][1]}, lane * 16, base + 1024);
        st16(rs, u32x4{pk[3][0], pk[3][1], pk[4][0], pk[4][1]}, lane * 16, base + 2048);
      }
    };
    if (helper) {
#pragma unroll
      for (int r = 0; r < 2; ++r)
#pragma unroll
        for (int c = 0; c < 5; ++c) acc[r][c] = *(LDSP f32x4*)(lds + K1_HO + (mh * 2 + r) * 5120 + c * 1024 + lane * 16);
    }
#pragma unroll 1
    for (int it = 0; it < (helper ? 1 : 2); ++it) {
      round(acc[0], helper ? 0 : nj, mh * 4 + (helper ? 3 : 2 * it));
      if (it == 0 || nj == 1) round(acc[1], helper ? 2 : nj, mh * 4 + (helper ? 3 : 2 * it + 1));
#pragma unroll
      for (int c = 0; c < 5; ++c) { acc[0][c] = acc[2][c]; acc[1][c] = acc[3][c]; }
    }
    DG(T[4] = stamp();)
    DG_OUT1();
    return;
  }

  auto normalise = [&](const f32x4 (&a)[5], const f32x4 (&cs)[5], int rt, u32x4 (&bq)[3], unsigned one) {
    const f32x2 st = *(LDSP f32x2*)(lds + K1_STAT + (rt * 16 + m) * 8);
#pragma unroll
    for (int c = 0; c < 5; ++c) {
      float v[4];
#pragma unroll
      for (int e = 0; e < 4; ++e) v[e] = fmaf(a[c][e], st[0], st[1] * cs[c][e]);
      bq[c >> 1][(c & 1) * 2] = pk2(v[0], v[1]);
      bq[c >> 1][(c & 1) * 2 + 1] = pk2(v[2], v[3]);
    }
    bq[2][2] = one;
    bq[2][3] = 0u;
  };
  f32x4 csv[5];
  if (!helper && nj != 1) {
#pragma unroll
    for (int c = 0; c < 5; ++c) csv[c] = *(LDSP f32x4*)(lds + K1_CS + (nj * 80 + c * 16 + 4 * g) * 4);
#pragma unroll
    for (int r = 0; r < 4; ++r) {
      u32x4 bq[3];
      normalise(acc[r], csv, mh * 4 + r, bq, nj == 2 ? 0x00003f80u : 0u);
      if (nj == 0) {
        const unsigned so = OFF_QT + (4 * grp * 128 + rb * 8 + mh * 4 + r) * 768;
#pragma unroll
        for (int i = 0; i < 3; ++i) st16(rs, bq[i], g * (128 * 768) + m * 16, so + i * 256);
      } else {
#pragma unroll
        for (int i = 0; i < 3; ++i) *(LDSP u32x4*)(lds + K1_VIMG + (mh * 64 + r * 16 + m) * V_PITCH + g * 48 + i * 16) = bq[i];
      }
    }
  }
  if (helper || nj != 2) {
    const int kr0 = helper ? 3 : (nj == 1 ? 0 : 2);
    if (kr0) {
#pragma unroll
      for (int c = 0; c < 5; ++c) acc[0][c] = *(LDSP f32x4*)(lds + K1_HO + (mh * 2 + kr0 - 2) * 5120 + c * 1024 + lane * 16);
    }
#pragma unroll
    for (int c = 0; c < 5; ++c) csv[c] = *(LDSP f32x4*)(lds + K1_CS + (80 + c * 16 + 4 * g) * 4);
    LDSP char* wl = lds + ((p4 == g) ? K1_WPHI + g * 800 + q4 * 40 : K1_ZERO);
#pragma unroll 1
    for (int kr = kr0; kr < (kr0 ? kr0 + 1 : 2); ++kr) {
      u32x4 bq[3];
      normalise(acc[0], csv, mh * 4 + kr, bq, 0u);
      f32x4 pk[5];
      head_proj(wl, bq, pk);
      unsigned ph[20];
      feature_map(pk, ph);
      LDSP char* dst = lds + (mh * 64 + kr * 16 + m) * PK_PITCH + g * 80;
#pragma unroll
      for (int i = 0; i < 5; ++i) *(LDSP u32x4*)(dst + i * 16) = u32x4{ph[4 * i], ph[4 * i + 1], ph[4 * i + 2], ph[4 * i + 3]};
#pragma unroll
      for (int c = 0; c < 5; ++c) acc[0][c] = acc[1][c];
    }
  }
  DG(T[4] = stamp();)
  WAIT_LGKM0();
  __builtin_amdgcn_s_barrier();
  asm volatile("" ::: "memory");
  {
#pragma unroll 1
    for (int job = wave; job < 12; job += 8) {
      const int hh = job & 3, ft = job >> 2;
      LDSP char* pa = lds + (4 * g + q4) * PK_PITCH + hh * 80 + ft * 32 + p4 * 8;
      LDSP char* pb = lds + K1_VIMG + (4 * g + q4) * V_PITCH + hh * 48 + p4 * 8;
      f32x4 d0 = {0.f, 0.f, 0.f, 0.f}, d1 = {0.f, 0.f, 0.f, 0.f};
#pragma unroll
      for (int kk = 0; kk < 4; ++kk) {
        u32x4 a = trread2(pa + (32 * kk) * PK_PITCH, pa + (32 * kk + 16) * PK_PITCH);
        u32x4 v0 = trread2(pb + (32 * kk) * V_PITCH, pb + (32 * kk + 16) * V_PITCH);
        u32x4 v1 = trread2(pb + (32 * kk) * V_PITCH + 32, pb + (32 * kk + 16) * V_PITCH + 32);
        d0 = mfma(a, v0, d0);
        d1 = mfma(a, v1, d1);
      }
      const unsigned so = OFF_KVP + ((4 * grp + hh) * 16 + rb) * KVP_B + 1344 * ft;
      st16(rs, __builtin_bit_cast(u32x4, d0), m * 64 + g * 16, so);
      if (m < 5) st16(rs, __builtin_bit_cast(u32x4, d1), (16 + m) * 64 + g * 16, so);
    }
  }
  DG_OUT1();
}

constexpr unsigned K2_YD = 0;
constexpr unsigned K2_SD = 4 * 15360;
constexpr unsigned K2_KVT = K2_SD + 4 * 20480;
constexpr unsigned K2_ZERO = K2_KVT + 1920;
constexpr unsigned K2_WPHI = K2_ZERO + 1792;
constexpr unsigned K2_FLAG = K2_WPHI + 3200;
constexpr unsigned K2_OWN = K2_FLAG + 16;
constexpr unsigned K2_LDS = K2_OWN + 8192;

DEVI u32x4 bld(__amdgpu_buffer_rsrc_t rs, unsigned voff, unsigned soff) {
  return __builtin_amdgcn_raw_buffer_load_b128(rs, voff, soff, 16);
}
DEVI u32x4 bld_sc1(__amdgpu_buffer_rsrc_t rs, unsigned voff, unsigned soff) {
  return __builtin_amdgcn_raw_buffer_load_b128(rs, voff, soff, 16);
}
template <int N>
DEVI void y_stream(__amdgpu_buffer_rsrc_t rs, unsigned act, unsigned wt, int rot, bool more, f32x4 (&acc)[4][5]) {
  const unsigned l16 = (threadIdx.x & 63) * 16;
  unsigned ab[4];
#pragma unroll
  for (int l = 0; l < 4; ++l) ab[l] = act + ((l + rot) & 3) * (54 * 1024);
  u32x4 A[3][4], B[3][5];
  auto load = [&](int i) {
#pragma unroll
    for (int l = 0; l < 4; ++l) A[i % 3][l] = bld(rs, l16, ab[l] + i * 1024);
#pragma unroll
    for (int c = 0; c < 5; ++c) B[i % 3][c] = bld(rs, l16, wt + c * (74 * 1024) + i * 1024);
  };
  load(0);
  load(1);
#pragma unroll
  for (int i = 0; i < N; ++i) {
    if (i + 2 < N) load(i + 2);
    else if (i + 2 == N) { if (more) load(N); }
    __builtin_amdgcn_sched_barrier(0);
#pragma unroll
    for (int l = 0; l < 4; ++l)
#pragma unroll
      for (int c = 0; c < 5; ++c) acc[l][c] = mfma(B[i % 3][c], A[i % 3][l], acc[l][c]);
    __builtin_amdgcn_sched_barrier(0);
  }
  if (more) {
#pragma unroll
    for (int l = 0; l < 4; ++l)
#pragma unroll
      for (int c = 0; c < 5; ++c) acc[l][c] = mfma(B[N % 3][c], A[N % 3][l], acc[l][c]);
  }
}

template <int C0, int C1>
DEVI void combine(LDSP char* lds, const u32x2 (&xv)[3], u16* out, int rt, int rb2, int cb, const f32x4 (&own)[5]) {
  const int lane = threadIdx.x & 63;
  const unsigned l16 = lane * 16;
  const size_t orow = (size_t)(64 * rb2 + 16 * rt + (lane & 15)) * 640 + 80 * cb + 4 * (lane >> 4);
  WAIT_LGKM0();
  __builtin_amdgcn_s_barrier();
  asm volatile("" ::: "memory");
#pragma unroll
  for (int c = C0; c < C1; ++c) {
    f32x4 y = C0 == 0 ? own[c] : *(LDSP f32x4*)(lds + K2_OWN + (rt * 2 + c - 3) * 1024 + l16);
#pragma unroll
    for (int l = 1; l < 4; ++l)
      y += *(LDSP f32x4*)(lds + K2_YD + ((rt - l) & 3) * 15360 + ((l - 1) * 5 + c) * 1024 + l16);
    LDSP f32x4* sp = (LDSP f32x4*)(lds + K2_SD + (rt * 5 + c) * 1024 + l16);
    const f32x4 s = (sp[0] + sp[1280]) + (sp[2560] + sp[3840]);
    float o[4];
#pragma unroll
    for (int e = 0; e < 4; ++e) {
      const float gate = y[e] * __builtin_amdgcn_rcpf(1.f + __builtin_amdgcn_exp2f(-y[e] * L2E));
      const unsigned xw = xv[c - C0][e >> 1];
      o[e] = fmaf(s[e], gate, (e & 1) ? bfhi(xw) : bflo(xw));
    }
    *(u32x2*)(out + orow + 16 * c) = u32x2{pk2(o[0], o[1]), pk2(o[2], o[3])};
  }
}

template <int NW>
DEVI bool poll_tags(__amdgpu_buffer_rsrc_t rs, unsigned off, int cnt, u32x2 tag) {
  const int lane = threadIdx.x & 63;
  const unsigned vo = (lane < cnt ? lane : 0) * 64;
  for (int spin = 0; spin < (1 << 20); ++spin) {
    asm volatile("" ::: "memory");
    bool ok = true;
#pragma unroll
    for (int i = 0; i < NW; ++i) {
      const u32x2 w = __builtin_amdgcn_raw_buffer_load_b64(rs, vo, off + i * 4096, 16);
      ok = ok && w[0] == tag[0] && w[1] == tag[1];
    }
    if (__all(ok)) { asm volatile("" ::: "memory"); return true; }
    __builtin_amdgcn_s_sleep(4);
  }
  return false;
}

DEVI void go_set(LDSP char* lds, int i) {
  if ((threadIdx.x & 63) == 0) *(volatile LDSP unsigned*)(lds + F_TAG + 8 + 4 * i) = 1u;
}
DEVI void go_wait(LDSP char* lds, int i) {
  for (int spin = 0; spin < (1 << 24); ++spin) {
    if (__builtin_amdgcn_readfirstlane(*(volatile LDSP unsigned*)(lds + F_TAG + 8 + 4 * i))) break;
    __builtin_amdgcn_s_sleep(2);
  }
  asm volatile("" ::: "memory");
}

DEVI void phase2(LDSP char* lds, u32x2 tag, const u16* __restrict__ x, const u16* __restrict__ wphi, char* __restrict__ ws,
                 u16* __restrict__ out) {
  const int tid = threadIdx.x, lane = tid & 63;
  const int wave = __builtin_amdgcn_readfirstlane(tid >> 6);
  const int g = lane >> 4, m = lane & 15, q4 = m >> 2, p4 = lane & 3;
  const int b = blockIdx.x, xcd = b & 7, jx = b >> 3;
  const int rb2 = 8 * (xcd >> 1) + (jx & 7), cb = 4 * (xcd & 1) + (jx >> 3);
  const __amdgpu_buffer_rsrc_t rs = __builtin_amdgcn_make_buffer_rsrc((void*)ws, 0, WS_END, 0x00020000);
  const unsigned l16 = lane * 16;
  DG(unsigned long long T[8]; T[0] = stamp(); T[7] = rstamp(); T[1] = T[2] = T[3] = T[4] = T[5] = T[6] = 0;)
#define DG_OUT2() DG({ unsigned long long re = rstamp(); if (lane == 0) { unsigned long long* dp = (unsigned long long*)(ws + OFF_DBG + (1u << 20)) + (size_t)(b * 8 + wave) * 16; for (int i = 0; i < 8; ++i) dp[i] = T[i]; dp[8] = re; } })
  asm volatile("" ::"s"(x), "s"(wphi), "s"(out));
  if (wave == 4) {
    *(LDSP u32x4*)(lds + K2_ZERO + l16) = u32x4{0u, 0u, 0u, 0u};
    if (lane < 48) *(LDSP u32x4*)(lds + K2_ZERO + 1024 + l16) = u32x4{0u, 0u, 0u, 0u};
    if (lane == 0) *(LDSP u32x4*)(lds + K2_FLAG) = u32x4{0u, 0u, 0u, 0u};
  }
  WAIT_LGKM0();
  __builtin_amdgcn_s_barrier();
  const unsigned act = OFF_ACT + 4 * rb2 * (54 * 1024), wt = OFF_WT + 5 * cb * (74 * 1024);
  const u16* xt = x + (size_t)(64 * rb2 + 16 * (wave & 3) + m) * 640 + 80 * cb + 4 * g;
  f32x4 acc[4][5];
#pragma unroll
  for (int r = 0; r < 4; ++r)
#pragma unroll
    for (int c = 0; c < 5; ++c) acc[r][c] = f32x4{0.f, 0.f, 0.f, 0.f};

  if (wave < 4) {
    u32x2 xv[3];
#pragma unroll
    for (int c = 0; c < 3; ++c) xv[c] = *(const u32x2*)(xt + 16 * c);
    if (wave == 0) {
      poll_tags<4>(rs, OFF_FLR, 64, tag);
      poll_tags<1>(rs, OFF_FL1 + (128 + (rb2 >> 1) * 8) * 64, 8, tag);
      go_set(lds, 0);
    } else {
      go_wait(lds, 0);
    }
    DG(T[1] = stamp();)
    const unsigned k0 = (wave < 2 ? 14 * wave : 2 + 13 * wave) * 1024;
    y_stream<13>(rs, act + k0, wt + k0, wave, wave < 2, acc);
    DG(T[3] = stamp();)
#pragma unroll
    for (int l = 1; l < 4; ++l)
#pragma unroll
      for (int c = 0; c < 5; ++c)
        *(LDSP f32x4*)(lds + K2_YD + wave * 15360 + ((l - 1) * 5 + c) * 1024 + l16) = acc[l][c];
#pragma unroll
    for (int c = 3; c < 5; ++c) *(LDSP f32x4*)(lds + K2_OWN + (wave * 2 + c - 3) * 1024 + l16) = acc[0][c];
    DG(T[4] = stamp();)
    combine<0, 3>(lds, xv, out, wave, rb2, cb, acc[0]);
    if (b == 0 && wave == 0 && poll_tags<4>(rs, OFF_FLR, 64, tag) && lane == 0) __builtin_amdgcn_raw_buffer_store_b64(tag, rs, OFF_SYNC, 0, 16);
    DG(T[6] = stamp();)
    DG_OUT2();
    return;
  }

  const int aw = wave - 4;
  const int h = 4 * xcd + (jx & 3), sb = jx >> 2;
  const int tok = sb * 256 + aw * 64 + lane;
  __builtin_amdgcn_s_setprio(2);
  u32x4 wv = *(const u32x4*)(wphi + h * 400 + (lane < 50 ? lane : 49) * 8);
  if (aw == 0) {
    poll_tags<1>(rs, OFF_FL1 + (h >> 2) * 16 * 64, 16, tag);
    go_set(lds, 1);
  } else {
    go_wait(lds, 1);
  }
  u32x4 bq[3];
#pragma unroll
  for (int s = 0; s < 3; ++s) bq[s] = bld(rs, (unsigned)(tok >> 4) * 768 + m * 16, OFF_QT + h * (128 * 768) + s * 256);
  int item = aw * 53 + (lane < 53 ? lane : 52);
  item = item < 210 ? item : 209;
  const int d = item / 10, fq = item - 10 * d;
  u32x4 part[16];
#pragma unroll
  for (int r = 0; r < 16; ++r) part[r] = bld(rs, (unsigned)((fq >> 2) * 1344 + d * 64 + (fq & 3) * 16), OFF_KVP + (h * 16 + r) * KVP_B);
  *(LDSP u32x4*)(lds + K2_WPHI + aw * 800 + (lane < 50 ? lane : 49) * 16) = wv;
  unsigned ph[20];
  {
    LDSP char* wl = lds + ((p4 == g) ? K2_WPHI + aw * 800 + q4 * 40 : K2_ZERO);
    f32x4 pq[5];
    head_proj(wl, bq, pq);
    feature_map(pq, ph);
#pragma unroll
    for (int i = 0; i < 20; ++i) asm volatile("" : "+v"(ph[i]));
  }
  DG(T[1] = stamp();)
  {
    f32x4 sum = __builtin_bit_cast(f32x4, part[0]);
#pragma unroll
    for (int r = 1; r < 16; ++r) sum += __builtin_bit_cast(f32x4, part[r]);
    *(LDSP u32x2*)(lds + K2_KVT + d * 80 + fq * 8) = u32x2{pk2(sum[0], sum[1]), pk2(sum[2], sum[3])};
    WAIT_LGKM0();
    if (lane == 0) *(volatile LDSP unsigned*)(lds + K2_FLAG + aw * 4) = 1u;
    for (int spin = 0; spin < (1 << 22); ++spin) {
      const u32x4 f = *(volatile LDSP u32x4*)(lds + K2_FLAG);
      if (__builtin_amdgcn_readfirstlane(f[0] & f[1] & f[2] & f[3])) break;
      __builtin_amdgcn_s_sleep(1);
    }
    asm volatile("" ::: "memory");
  }
  DG(T[2] = stamp();)
  {
    LDSP char* kl = lds + (((m >> 2) == g) ? K2_KVT + (m & 3) * 80 : K2_ZERO);
    f32x4 nd[6];
#pragma unroll
    for (int t = 0; t < 6; ++t) {
      f32x4 c = {0.f, 0.f, 0.f, 0.f};
#pragma unroll
      for (int s = 0; s < 5; ++s)
        c = mfma(*(LDSP u32x4*)(kl + t * 320 + s * 16), u32x4{ph[4 * s], ph[4 * s + 1], ph[4 * s + 2], ph[4 * s + 3]}, c);
      nd[t] = c;
    }
    const float rd = __builtin_amdgcn_rcpf(nd[5][0] + 1e-6f);
    unsigned ow[10];
#pragma unroll
    for (int t = 0; t < 5; ++t) {
      ow[2 * t] = pk2(nd[t][0] * rd, nd[t][1] * rd);
      ow[2 * t + 1] = pk2(nd[t][2] * rd, nd[t][3] * rd);
    }
    const int e0 = (5 * h + 1) >> 1, e8 = (h & 1) ? e0 - 1 : e0 + 2;
    const unsigned vo = (unsigned)(tok >> 4) * 20480 + m * 16;
    const bool odd = h & 1;
    st16(rs, odd ? u32x4{ow[2], ow[3], ow[4], ow[5]} : u32x4{ow[0], ow[1], ow[2], ow[3]}, vo, OFF_OT + e0 * 256);
    st16(rs, odd ? u32x4{ow[6], ow[7], ow[8], ow[9]} : u32x4{ow[4], ow[5], ow[6], ow[7]}, vo, OFF_OT + (e0 + 1) * 256);
    st8(rs, odd ? u32x2{ow[0], ow[1]} : u32x2{ow[8], ow[9]}, vo, OFF_OT + e8 * 256 + (h & 1) * 8);
    WAIT_VM(0);
    if (lane == 0) __builtin_amdgcn_raw_buffer_store_b64(tag, rs, OFF_FO + ((sb * 4 + aw) * 32 + h) * 64, 0, 16);
  }
  DG(T[3] = stamp();)
  const unsigned ring = K2_SD + aw * 20480;
  u32x2 xv[3];
#pragma unroll
  for (int c = 0; c < 2; ++c) xv[c] = *(const u32x2*)(xt + 48 + 16 * c);
  xv[2] = xv[1];
  go_wait(lds, 0);
  {
    u32x4 B[5][5], A[5][4];
    auto loadb = [&](int i) {
#pragma unroll
      for (int c = 0; c < 5; ++c) B[i][c] = bld(rs, l16, wt + c * (74 * 1024) + (54 + 5 * aw + i) * 1024);
    };
    loadb(0);
    loadb(1);
    loadb(2);
    const unsigned ob = OFF_OT + (4 * rb2 * 20 + 5 * aw) * 1024;
    poll_tags<1>(rs, OFF_FO + (rb2 * 32 + 8 * aw) * 64, 8, tag);
    DG(T[4] = stamp();)
#pragma unroll
    for (int i = 0; i < 5; ++i)
#pragma unroll
      for (int l = 0; l < 4; ++l) A[i][l] = bld(rs, l16, ob + l * 20480 + i * 1024);
#pragma unroll
    for (int i = 0; i < 5; ++i) {
      if (i == 1) loadb(3);
      if (i == 2) loadb(4);
      __builtin_amdgcn_sched_barrier(0);
#pragma unroll
      for (int l = 0; l < 4; ++l)
#pragma unroll
        for (int c = 0; c < 5; ++c) acc[l][c] = mfma(B[i][c], A[i][l], acc[l][c]);
      __builtin_amdgcn_sched_barrier(0);
    }
  }
#pragma unroll
  for (int l = 0; l < 4; ++l)
#pragma unroll
    for (int c = 0; c < 5; ++c) *(LDSP f32x4*)(lds + ring + (l * 5 + c) * 1024 + l16) = acc[l][c];
  DG(T[6] = stamp();)
  combine<3, 5>(lds, xv, out, aw, rb2, cb, acc[0]);
  DG_OUT2();
}

static_assert(K1_LDS + 1024 <= F_TAG && K2_LDS <= F_TAG, "LDS map");

__global__ __launch_bounds__(512, 2) void hedgehog_fused(const u16* __restrict__ x, const u16* __restrict__ wqkv,
                                                         const u16* __restrict__ wphi, const u16* __restrict__ wo,
                                                         const u16* __restrict__ w1, const u16* __restrict__ b1,
                                                         const u16* __restrict__ w2, char* __restrict__ ws,
                                                         u16* __restrict__ out) {
  extern __shared__ __attribute__((aligned(16))) char smem[];
  LDSP char* lds = (LDSP char*)smem;
  phase1(lds, __builtin_amdgcn_s_getpc(), x, wqkv, wphi, wo, w1, b1, w2, ws);
  int tid = threadIdx.x;
  asm volatile("" : "+v"(tid));
  const __amdgpu_buffer_rsrc_t rs = __builtin_amdgcn_make_buffer_rsrc((void*)ws, 0, WS_END, 0x00020000);
  WAIT_VM(0);
  WAIT_LGKM0();
  __builtin_amdgcn_s_barrier();
  asm volatile("" ::: "memory");
  const u32x2 tag = *(LDSP u32x2*)(lds + F_TAG);
  if (tid == 0) {
    const int b = blockIdx.x, xcd = b & 7, jx = b >> 3;
    const int rb = 8 * (xcd >> 2) + (jx & 7), grp = 2 * (xcd & 3) + ((jx >> 3) & 1);
    __builtin_amdgcn_raw_buffer_store_b64(tag, rs, OFF_FL1 + (jx < 16 ? grp * 16 + rb : 128 + rb * 8 + grp) * 64, 0, 16);
  }
  phase2(lds, tag, x, wphi, ws, out);
}

extern "C" void kernel_launch(void* const* d_in, const int* in_sizes, int n_in, void* d_out, int out_size, void* d_ws,
                              size_t ws_size, hipStream_t stream) {
  const u16* x = (const u16*)d_in[0];
  const u16* wqkv = (const u16*)d_in[1];
  const u16* wphi = (const u16*)d_in[2];
  const u16* wo = (const u16*)d_in[3];
  const u16* w1 = (const u16*)d_in[5];
  const u16* b1 = (const u16*)d_in[6];
  const u16* w2 = (const u16*)d_in[7];
  (void)hipFuncSetAttribute((const void*)hedgehog_fused, hipFuncAttributeMaxDynamicSharedMemorySize, F_LDS);
  hedgehog_fused<<<256, 512, F_LDS, stream>>>(x, wqkv, wphi, wo, w1, b1, w2, (char*)d_ws, (u16*)d_out);
}
```

```cpp
#define ROT3
#include <hip/hip_runtime.h>
#include <stdint.h>

typedef unsigned short u16;
typedef __attribute__((ext_vector_type(2))) __bf16 bf16x2;
typedef __attribute__((ext_vector_type(8))) __bf16 bf16x8;
typedef __attribute__((ext_vector_type(4))) short s16x4;
typedef __attribute__((ext_vector_type(2))) float f32x2;
typedef __attribute__((ext_vector_type(4))) float f32x4;
typedef __attribute__((ext_vector_type(2))) unsigned u32x2;
typedef __attribute__((ext_vector_type(4))) unsigned u32x4;
#define LDSP __attribute__((address_space(3)))
#define DEVI __device__ __forceinline__

constexpr unsigned OFF_ACT = 0;
constexpr unsigned OFF_WT = OFF_ACT + 128u * 54u * 1024u;
constexpr unsigned OFF_OT = OFF_WT + 40u * 74u * 1024u;
constexpr unsigned OFF_QT = OFF_OT + 128u * 20u * 1024u;
constexpr unsigned OFF_KVP = OFF_QT + 32u * 2048u * 48u;
constexpr unsigned KVP_B = 3 * 1344;
constexpr unsigned OFF_SYNC = OFF_KVP + 32u * 16u * KVP_B;
constexpr unsigned OFF_FL1 = OFF_SYNC + 128;
constexpr unsigned OFF_FLR = OFF_FL1 + 256 * 64;
constexpr unsigned OFF_FO = OFF_FLR + 256 * 64;
constexpr unsigned WS_END = OFF_FO + 1024 * 64;

#define CODE_BYTES (48 * 1024)
#define CODE2_OFF (36 * 1024)
#define K2_NY 11
#define K1_MAP 1
constexpr float L2E = 1.4426950408889634f, LN2 = 0.6931471805599453f;

DEVI unsigned pk2(float a, float b) {
  bf16x2 v = {(__bf16)a, (__bf16)b};
  return __builtin_bit_cast(unsigned, v);
}
DEVI float bflo(unsigned u) { return __uint_as_float(u << 16); }
DEVI float bfhi(unsigned u) { return __uint_as_float(u & 0xffff0000u); }
DEVI f32x4 mfma(u32x4 a, u32x4 b, f32x4 c) {
  return __builtin_amdgcn_mfma_f32_16x16x32_bf16(__builtin_bit_cast(bf16x8, a), __builtin_bit_cast(bf16x8, b), c, 0, 0, 0);
}
DEVI u32x2 trread(LDSP char* p) {
  return __builtin_bit_cast(u32x2, __builtin_amdgcn_ds_read_tr16_b64_v4i16((LDSP s16x4*)p));
}
DEVI u32x4 trread2(LDSP char* p0, LDSP char* p1) {
  u32x2 a = trread(p0), b = trread(p1);
  return u32x4{a[0], a[1], b[0], b[1]};
}
DEVI float dot2(unsigned a, unsigned b, float c) {
  return __builtin_amdgcn_fdot2_f32_bf16(__builtin_bit_cast(bf16x2, a), __builtin_bit_cast(bf16x2, b), c, false);
}
DEVI float dot_ones(u32x4 a, float c) {
  c = dot2(a[0], 0x3f803f80u, c); c = dot2(a[1], 0x3f803f80u, c);
  c = dot2(a[2], 0x3f803f80u, c); c = dot2(a[3], 0x3f803f80u, c);
  return c;
}
DEVI float dot_self(u32x4 a, float c) {
  c = dot2(a[0], a[0], c); c = dot2(a[1], a[1], c); c = dot2(a[2], a[2], c); c = dot2(a[3], a[3], c);
  return c;
}
DEVI float red_g(float v) {
  v += __shfl_xor(v, 16);
  v += __shfl_xor(v, 32);
  return v;
}
DEVI void glds16(unsigned voff, const void* sbase, unsigned lds_dst) {
  unsigned keep;
  asm volatile("s_mov_b32 %0, m0\n\ts_mov_b32 m0, %3\n\ts_nop 0\n\tglobal_load_lds_dwordx4 %1, %2\n\ts_mov_b32 m0, %0"
               : "=&s"(keep) : "v"(voff), "s"(sbase), "s"(lds_dst) : "memory");
}
#define DG(...)
#define WAIT_VM(n) asm volatile("s_waitcnt vmcnt(" #n ")" ::: "memory")
#define WAIT_LGKM0() asm volatile("s_waitcnt lgkmcnt(0)" ::: "memory")

DEVI void feature_map(const f32x4 (&p)[5], unsigned (&o)[20]) {
  float mx = p[0][0], mn = p[0][0];
#pragma unroll
  for (int t = 0; t < 5; ++t)
#pragma unroll
    for (int r = 0; r < 4; ++r) { mx = fmaxf(mx, p[t][r]); mn = fminf(mn, p[t][r]); }
  const float a = -mx * L2E, b = mn * L2E;
  float e1[20], e2[20], s1 = 0.f, s2 = 0.f;
#pragma unroll
  for (int t = 0; t < 5; ++t)
#pragma unroll
    for (int r = 0; r < 4; ++r) {
      e1[4 * t + r] = __builtin_amdgcn_exp2f(fmaf(p[t][r], L2E, a));
      e2[4 * t + r] = __builtin_amdgcn_exp2f(fmaf(p[t][r], -L2E, b));
      s1 += e1[4 * t + r]; s2 += e2[4 * t + r];
    }
  const float r1 = __builtin_amdgcn_rcpf(s1), r2 = __builtin_amdgcn_rcpf(s2);
#pragma unroll
  for (int i = 0; i < 10; ++i) {
    o[i] = pk2(e1[2 * i] * r1, e1[2 * i + 1] * r1);
    o[10 + i] = pk2(e2[2 * i] * r2, e2[2 * i + 1] * r2);
  }
}
DEVI void head_proj(LDSP char* wl, const u32x4 (&b)[3], f32x4 (&p)[5]) {
#pragma unroll
  for (int t = 0; t < 5; ++t) {
    u32x4 a0 = trread2(wl + 0 * 40 + t * 8, wl + 4 * 40 + t * 8);
    u32x4 a1 = trread2(wl + 8 * 40 + t * 8, wl + 12 * 40 + t * 8);
    u32x2 h2 = trread(wl + 16 * 40 + t * 8);
    u32x4 a2 = {h2[0], h2[1], 0u, 0u};
    f32x4 c = {0.f, 0.f, 0.f, 0.f};
    c = mfma(a0, b[0], c);
    c = mfma(a1, b[1], c);
    c = mfma(a2, b[2], c);
    p[t] = c;
  }
}

constexpr unsigned XSLOT = 16384, WSUB = 5120, WSLOT = 3 * WSUB;
constexpr unsigned K1_W = 4 * XSLOT;
constexpr unsigned K1_WPHI = K1_W + 5 * WSLOT;
constexpr unsigned K1_ZERO = K1_WPHI + 3200;
constexpr unsigned K1_STAT = K1_ZERO + 768;
constexpr unsigned K1_CS = K1_STAT + 1024;
constexpr unsigned K1_BL = K1_CS + 960;
constexpr unsigned K1_SCR = K1_BL + 960;
constexpr unsigned K1_MB = K1_SCR + 256;
constexpr unsigned K1_RT = K1_MB + 16;
constexpr unsigned K1_LDS = K1_RT + 10240;
constexpr unsigned F_TAG = 160832;
constexpr unsigned F_LDS = F_TAG + 16;
constexpr unsigned PK_PITCH = 352, V_PITCH = 224, K1_VIMG = 128 * PK_PITCH;
constexpr unsigned K1_HO = K1_VIMG + 128 * V_PITCH;

DEVI void glds16n(unsigned voff, const void* sbase, unsigned lds_dst) {
  asm volatile("s_mov_b32 m0, %2\n\ts_nop 0\n\tglobal_load_lds_dwordx4 %0, %1" ::"v"(voff), "s"(sbase), "s"(lds_dst) : "memory");
}
DEVI void st16(__amdgpu_buffer_rsrc_t rs, u32x4 v, unsigned voff, unsigned soff) {
  __builtin_amdgcn_raw_buffer_store_b128(v, rs, voff + soff, 0, 16);
}
DEVI void st8(__amdgpu_buffer_rsrc_t rs, u32x2 v, unsigned voff, unsigned soff) {
  __builtin_amdgcn_raw_buffer_store_b64(v, rs, voff + soff, 0, 16);
}
#define STEP_SYNC() do { __builtin_amdgcn_s_waitcnt(0xC07F); __builtin_amdgcn_s_barrier(); __builtin_amdgcn_sched_barrier(0); } while (0)

DEVI void mb_arrive(LDSP char* lds, int i) {
  WAIT_LGKM0();
  if ((threadIdx.x & 63) == 0) __hip_atomic_fetch_add((LDSP unsigned*)(lds + K1_MB + 4 * i), 1u, __ATOMIC_RELAXED, __HIP_MEMORY_SCOPE_WORKGROUP);
}
DEVI void mb_wait(LDSP char* lds, int i) {
  for (int spin = 0; spin < (1 << 22); ++spin) {
    if (__builtin_amdgcn_readfirstlane(*(volatile LDSP unsigned*)(lds + K1_MB + 4 * i)) >= 6u) break;
    __builtin_amdgcn_s_sleep(1);
  }
  asm volatile("" ::: "memory");
  __builtin_amdgcn_sched_barrier(0);
}
struct K1C {
  LDSP char* lds;
  unsigned xa, wa[5], wr;
  DG(unsigned long long t1;)
};
template <int NC>
DEVI void c_read(K1C& S, int s, u32x4 (&xf)[4], u32x4 (&wf)[5]) {
  LDSP char* px = S.lds + ((s >> 1) & 3) * XSLOT + (S.xa ^ ((s & 1) << 6));
  LDSP char* pw = S.lds + S.wr;
  xf[0] = *(LDSP u32x4*)(px);
  wf[0] = trread2(pw + S.wa[0], pw + S.wa[0] + 1280);
#pragma unroll
  for (int r = 1; r < 4; ++r) xf[r] = *(LDSP u32x4*)(px + r * 2048);
#pragma unroll
  for (int c = 1; c < NC; ++c) wf[c] = trread2(pw + S.wa[c], pw + S.wa[c] + 1280);
  S.wr = S.wr == 4 * WSLOT ? 0 : S.wr + WSLOT;
}
template <int NC, int ROLE, bool READ, bool MINI = false>
DEVI void c_step(K1C& S, int s, const u32x4 (&xc)[4], const u32x4 (&wc)[5], u32x4 (&xn)[4], u32x4 (&wn)[5], f32x4 (&acc)[4][5],
                 f32x4 (&cs)[3], f32x4 (&s1)[4], f32x4 (&s2)[4], const u32x4& ones) {
  if (READ) {
    if (MINI) { WAIT_VM(0); mb_arrive(S.lds, 1); mb_wait(S.lds, 1); } else STEP_SYNC();
    c_read<NC>(S, s + 1, xn, wn);
  }
#pragma unroll
  for (int c = 0; c < NC; ++c)
#pragma unroll
    for (int r = 0; r < 4; ++r) acc[r][c] = mfma(wc[c], xc[r], acc[r][c]);
  if (ROLE) {
    constexpr int C0 = ROLE == 1 ? 0 : NC - 2, C1 = ROLE == 1 ? NC - 2 : NC;
#pragma unroll
    for (int r = 0; r < 4; ++r) {
      s1[r] = mfma(ones, xc[r], s1[r]);
      s2[r] = mfma(xc[r], xc[r], s2[r]);
    }
#pragma unroll
    for (int c = C0; c < C1; ++c) cs[c - C0] = mfma(wc[c], ones, cs[c - C0]);
  }
  if (READ) {
#pragma unroll
    for (int i = 0; i < 4 + 2 * NC; ++i) {
      __builtin_amdgcn_sched_group_barrier(0x8, 1, 0);
      __builtin_amdgcn_sched_group_barrier(0x100, 1, 0);
    }
  }
  __builtin_amdgcn_sched_barrier(0);
}
template <int NC, int ROLE>
DEVI void c_main(K1C& S, f32x4 (&acc)[4][5], f32x4 (&cs)[3], f32x4 (&s1)[4], f32x4 (&s2)[4]) {
  u32x4 xa[4], wa[5], xb[4], wb[5];
  u32x4 ones = {0x3f803f80u, 0x3f803f80u, 0x3f803f80u, 0x3f803f80u};
  asm volatile("" : "+v"(ones));
  c_read<NC>(S, 0, xa, wa);
  c_step<NC, ROLE, true, true>(S, 0, xa, wa, xb, wb, acc, cs, s1, s2, ones);
#pragma unroll 1
  for (int s = 1; s < 19; s += 2) {
    c_step<NC, ROLE, true>(S, s, xb, wb, xa, wa, acc, cs, s1, s2, ones);
    c_step<NC, ROLE, true>(S, s + 1, xa, wa, xb, wb, acc, cs, s1, s2, ones);
  }
  c_step<NC, ROLE, false>(S, 19, xb, wb, xa, wa, acc, cs, s1, s2, ones);
}

struct K1H {
  LDSP char* lds;
  unsigned ldsbase;
  unsigned vw[8], vx[4];
  const char* pw; const char* px;
  unsigned incw, wrapw, wrapx;
  unsigned dw, dx;
  unsigned wa[5], wr;
  __amdgpu_buffer_rsrc_t rs;
  const char* w2; const char* wo;
  unsigned rv2, rvo, rrd, l16;
  int gi;
  const char* pc2;
  const char* touch;
  DG(unsigned long long t1;)
};
template <int HP, int ODD, bool SKIP = false>
DEVI void h_issue(K1H& S) {
  if (!SKIP) {
#pragma unroll
    for (int i = 0; i < 8 - HP; ++i) glds16n(S.vw[i], S.pw, S.ldsbase + K1_W + HP * 8192 + i * 1024 + S.dw);
#pragma unroll
    for (int i = 0; i < 4; ++i) glds16n(S.vx[i], S.px, S.ldsbase + HP * 4096 + i * 1024 + S.dx);
  }
  S.pw += S.incw;
  S.pw -= (unsigned)(size_t)S.pw == S.wrapw ? 18 * S.incw : 0u;
  S.px += ODD ? 128 - 64 * 1280 : 64 * 1280;
  S.px -= (unsigned)(size_t)S.px == S.wrapx ? 1152u : 0u;
  S.dw = S.dw == 4 * WSLOT ? 0 : S.dw + WSLOT;
  S.dx = (S.dx + 8192) & 0xffff;
}
template <int HP>
DEVI void h_retile(K1H& S, int s) {
  if (s >= 11) {
    const int i = s - 11, gi = S.gi + i, cq = (gi >> 2) / 74, ks = (gi >> 2) - 74 * cq;
    LDSP char* pr = S.lds + S.rrd + (i == 5 ? 0 : i) * 1024;
    st16(S.rs, trread2(pr, pr + 512), S.l16, gi < 2960 ? OFF_WT + ((4 * cq + (gi & 3)) * 74 + ks) * 1024 : WS_END);
  }
  if (s < 12) {
    const int i = s - 6, gi = S.gi + i, fid = gi < 2960 ? gi : 2959, cq = (fid >> 2) / 74, ks = (fid >> 2) - 74 * cq;
    const char* src = (ks < 54 ? S.w2 + ks * 40960 : S.wo + (ks - 54) * 40960) + (4 * cq + (fid & 3)) * 32;
    glds16n(ks < 54 ? S.rv2 : S.rvo, src, S.ldsbase + K1_RT + HP * 5120 + (i == 5 ? 0 : i) * 1024);
  }
  __builtin_amdgcn_sched_barrier(0);
}
template <int HP, int G> DEVI void h_wait() {
  constexpr int N = G * (12 - HP);
  asm volatile("s_waitcnt vmcnt(%0)" ::"i"(N) : "memory");
}
DEVI void h_read(K1H& S, u32x4 (&wf)[5]) {
  LDSP char* pw = S.lds + S.wr;
#pragma unroll
  for (int c = 0; c < 5; ++c) wf[c] = trread2(pw + S.wa[c], pw + S.wa[c] + 1280);
  S.wr = S.wr == 4 * WSLOT ? 0 : S.wr + WSLOT;
}
template <int HP, int G, int ISSUE>
DEVI void h_step(K1H& S, const u32x4 (&wc)[5], u32x4 (&wn)[5], f32x4 (&cs)[5], const u32x4& ones) {
  if (G >= 0 && G < 9) h_wait<HP, (G >= 0 && G < 9 ? G : 0)>();
  if (G >= 0) STEP_SYNC();
#pragma unroll
  for (int c = 0; c < 5; ++c) cs[c] = mfma(wc[c], ones, cs[c]);
  h_read(S, wn);
  __builtin_amdgcn_sched_barrier(0);
  if (ISSUE) h_issue<HP, ISSUE - 1>(S);
  __builtin_amdgcn_sched_barrier(0);
}
template <int HP>
DEVI void h_main(K1H& S, f32x4 (&cs)[5]) {
  u32x4 wa[5], wb[5];
  u32x4 ones = {0x3f803f80u, 0x3f803f80u, 0x3f803f80u, 0x3f803f80u};
  asm volatile("" : "+v"(ones));
  h_issue<HP, 1, true>(S);
  h_issue<HP, 0>(S);
  h_issue<HP, 1>(S);
  if (HP == 0) {
    if ((threadIdx.x & 63) < 16) asm volatile("s_mov_b32 m0, %1\n\ts_nop 0\n\tglobal_load_lds_dword %0, off" ::"v"(S.touch), "s"(S.ldsbase + K1_SCR) : "memory");
  }
  mb_wait(S.lds, 0);
  h_read(S, wa);
  mb_wait(S.lds, 1);
  h_step<HP, -1, 1>(S, wa, wb, cs, ones);
  h_step<HP, 3, 2>(S, wb, wa, cs, ones);
#pragma unroll 1
  for (int s = 2; s < 14; s += 2) {
    h_step<HP, 3, 1>(S, wa, wb, cs, ones);
    if (s >= 6) h_retile<HP>(S, s);
    h_step<HP, 3, 2>(S, wb, wa, cs, ones);
    if (s >= 6) h_retile<HP>(S, s + 1);
  }
  h_step<HP, 3, 1>(S, wa, wb, cs, ones);
  h_retile<HP>(S, 14);
  const int lane = threadIdx.x & 63;
  h_step<HP, 3, 0>(S, wb, wa, cs, ones);
  h_retile<HP>(S, 15);
  if (S.pc2) {
    asm volatile("s_mov_b32 m0, %1\n\ts_nop 0\n\tglobal_load_lds_dword %0, off" ::"v"(S.pc2 + min(CODE2_OFF + HP * 8192 + lane * 128, CODE_BYTES)), "s"(S.ldsbase + K1_SCR) : "memory");
  }
  h_step<HP, 2, 0>(S, wa, wb, cs, ones);
  h_retile<HP>(S, 16);
  h_step<HP, 1, 0>(S, wb, wa, cs, ones);
  h_step<HP, 0, 0>(S, wa, wb, cs, ones);
  if (lane == 0) {
    if (__hip_atomic_fetch_add((LDSP unsigned*)(S.lds + K1_MB + 8), 1u, __ATOMIC_RELAXED, __HIP_MEMORY_SCOPE_WORKGROUP) == 1u)
      __builtin_amdgcn_raw_buffer_store_b64(*(LDSP u32x2*)(S.lds + F_TAG), S.rs, OFF_FLR + blockIdx.x * 64, 0, 16);
  }
#pragma unroll
  for (int c = 0; c < 5; ++c) cs[c] = mfma(wb[c], ones, cs[c]);
  __builtin_amdgcn_s_setprio(0);
}

DEVI void phase1(LDSP char* lds, unsigned long long kpc, const u16* __restrict__ x, const u16* __restrict__ wqkv,
                 const u16* __restrict__ wphi, const u16* __restrict__ wo, const u16* __restrict__ w1,
                 const u16* __restrict__ b1, const u16* __restrict__ w2, char* __restrict__ ws) {
  const int tid = threadIdx.x, lane = tid & 63;
  const int wave = __builtin_amdgcn_readfirstlane(tid >> 6);
  const int b = blockIdx.x, xcd = b & 7, jx = b >> 3;
  const unsigned ldsbase = (unsigned)(size_t)lds;
  const bool helper = wave >= 6;
  DG(unsigned long long T[8]; T[0] = stamp(); T[7] = rstamp(); T[1] = T[6] = 0;)
  if (helper) {
    const char* pc = (const char*)(kpc & ~(unsigned long long)127) + min(((jx & 3) * 2 + (wave & 1)) * 8192 + lane * 128, CODE_BYTES);
    asm volatile("s_mov_b32 m0, %1\n\ts_nop 0\n\tglobal_load_lds_dword %0, off" ::"v"(pc), "s"(ldsbase + K1_SCR) : "memory");
  }
  if (tid == 0) { *(LDSP u32x4*)(lds + K1_MB) = u32x4{0u, 0u, 0u, 0u}; *(LDSP u32x2*)(lds + F_TAG + 8) = u32x2{0u, 0u}; }
  WAIT_LGKM0();
  asm volatile("" ::"s"(x), "s"(wqkv), "s"(wphi), "s"(wo), "s"(w1), "s"(b1), "s"(w2), "s"(ws));
  const int rb = 8 * (xcd >> 2) + (jx & 7);
  const char* xrow = (const char*)x + (size_t)rb * 128 * 1280;
  auto xvoff = [&](int pc) {
    const int row = pc * 8 + (lane >> 3);
    return (unsigned)(row * 1280 + (((lane & 7) ^ ((row >> 1) & 7)) << 4));
  };
  const unsigned x0 = xvoff(wave), x1 = x0 + 48 * 1280;
  if (!helper) {
    glds16n(x0, xrow, ldsbase + wave * 1024);
    glds16n(x0, xrow + 64 * 1280, ldsbase + 8192 + wave * 1024);
    if (wave < 2) {
      glds16n(x1, xrow, ldsbase + wave * 1024 + 6144);
      glds16n(x1, xrow + 64 * 1280, ldsbase + 8192 + wave * 1024 + 6144);
    }
  }
  __builtin_amdgcn_s_barrier();
  const int mh = wave & 1;
  const int nj = wave < 4 ? wave >> 1 : 2;
  const int cj = helper ? 2 * mh : nj;
  const int g = lane >> 4, m = lane & 15, q4 = m >> 2, p4 = lane & 3;
  const int cgi = jx >> 3;
  const bool atype = cgi < 2;
  const int grp = 2 * (xcd & 3) + (cgi & 1);
  const u16* W = atype ? wqkv : w1;
  const int ldw = atype ? 1920 : 1728;
  const int c0 = atype ? 80 * grp : (grp < 6 ? 224 * grp : 1344 + 192 * (grp - 6));
  const int sub = atype ? 640 : (grp < 6 ? 80 : 64);
  const unsigned incw = 64 * ldw;
  const int t1 = 1 + 3 * (((jx & 7) + cgi) % 3);
  auto wvoff = [&](int id) {
    const int sj = id / 5, ss = id - 5 * sj;
    const int c = 64 * ss + lane, rho = c / 10, cc = c - 10 * rho;
    const int k = (rho & 0x13) | ((rho & 4) << 1) | ((rho & 8) >> 1);
    const bool narrow = !atype && (grp >= 6 || sj == 2);
    return (unsigned)(k * ldw + c0 + sub * sj + 8 * ((narrow && cc >= 8) ? cc - 2 : cc)) * 2;
  };
  const unsigned wa0 = K1_W + cj * WSUB + (16 * (g >> 1) + 4 * (g & 1) + q4) * 160 + p4 * (atype ? 40 : 8);
  const unsigned cstr = atype ? 8 : 32;
  const __amdgpu_buffer_rsrc_t rs = __builtin_amdgcn_make_buffer_rsrc((void*)ws, 0, WS_END, 0x00020000);
  f32x4 acc[4][5];

  if (!helper) {
    auto xgrp = [&](int u, int xt) {
      const char* px = xrow + xt * 128 + (u & 1) * (64 * 1280);
      const unsigned d = ldsbase + u * 8192 + wave * 1024;
      glds16n(x0, px, d);
      if (wave < 2) glds16n(x1, px, d + 6144);
    };
    const unsigned dw = ldsbase + K1_W + wave * 1024;
    const unsigned v0 = wvoff(wave);
    glds16n(v0, W, dw);
    const unsigned v1 = wvoff(wave + 6);
    glds16n(v1, W, dw + 6144);
    const unsigned v2 = wvoff(wave < 3 ? wave + 12 : 14);
    if (wave < 3) glds16n(v2, W, dw + 12288);
    glds16n(v0, (const char*)W + incw, dw + WSLOT);
    glds16n(v1, (const char*)W + incw, dw + WSLOT + 6144);
    if (wave < 3) glds16n(v2, (const char*)W + incw, dw + WSLOT + 12288);
    xgrp(2, t1);
    K1C S;
    S.lds = lds;
    S.xa = (mh * 64 + m) * 128 + ((g ^ (m >> 1)) << 4);
#pragma unroll
    for (int c = 0; c < 5; ++c) S.wa[c] = wa0 + c * cstr;
    S.wr = 0;
    DG(S.t1 = 0;)
    f32x4 cs[3], s1[4], s2[4];
#pragma unroll
    for (int i = 0; i < 4; ++i) {
      s1[i] = f32x4{0.f, 0.f, 0.f, 0.f}; s2[i] = s1[i]; cs[i % 3] = s1[i];
#pragma unroll
      for (int c = 0; c < 5; ++c) acc[i][c] = s1[i];
    }
    if (wave == 4 && lane < 48) *(LDSP u32x4*)(lds + K1_ZERO + lane * 16) = u32x4{0u, 0u, 0u, 0u};
    const bool five = atype || (grp < 6 && nj < 2);
    if (wave < 2) WAIT_VM(5); else if (wave == 2) WAIT_VM(4); else WAIT_VM(3);
    mb_arrive(lds, 0);
    {
      const int col = c0 + sub * (lane / 10) + 8 * (lane % 10);
      const unsigned vo = atype ? tid * 16 : (col > 1720 ? 1720 : col) * 2;
      if (tid < (atype ? 200 : 30)) glds16n(vo, atype ? (const char*)wphi + grp * 3200 : (const char*)b1, ldsbase + K1_WPHI + wave * 1024);
    }
    if (tid == 320) {
      const u32x2 e = __builtin_amdgcn_raw_buffer_load_b64(rs, OFF_SYNC, 0, 16);
      const unsigned long long t = (((unsigned long long)e[1] << 32) | e[0]) + 1ull;
      *(LDSP u32x2*)(lds + F_TAG) = u32x2{(unsigned)t, (unsigned)(t >> 32)};
    }
    mb_wait(lds, 0);
    DG(T[1] = stamp();)
    if (nj == 1) {
      if (five) { if (mh == 0) c_main<5, 1>(S, acc, cs, s1, s2); else c_main<5, 2>(S, acc, cs, s1, s2); }
      else { if (mh == 0) c_main<4, 1>(S, acc, cs, s1, s2); else c_main<4, 2>(S, acc, cs, s1, s2); }
      const int cb = mh == 0 ? 0 : (five ? 3 : 2), cn = mh == 0 ? (five ? 3 : 2) : 2;
      if (m == 0) {
#pragma unroll
        for (int c = 0; c < 3; ++c)
          if (c < cn) *(LDSP f32x4*)(lds + K1_CS + (80 + (cb + c) * 16 + 4 * g) * 4) = cs[c];
      }
      if (g == q4) {
#pragma unroll
        for (int r = 0; r < 4; ++r) {
          const float sq = (m & 2) ? ((m & 1) ? s2[r][3] : s2[r][2]) : ((m & 1) ? s2[r][1] : s2[r][0]);
          const float mean = s1[r][0] * (1.0f / 640.0f);
          const float var = fmaxf(sq * (1.0f / 640.0f) - mean * mean, 0.f);
          float rsd = __builtin_amdgcn_rsqf(var + 1e-5f);
          if (!atype) rsd *= L2E;
          *(LDSP f32x2*)(lds + K1_STAT + (mh * 64 + r * 16 + m) * 8) = f32x2{rsd, -mean * rsd};
        }
      }
      if (atype) {
#pragma unroll
        for (int r = 2; r < 4; ++r)
#pragma unroll
          for (int c = 0; c < 5; ++c) *(LDSP f32x4*)(lds + K1_HO + (mh * 2 + r - 2) * 5120 + c * 1024 + lane * 16) = acc[r][c];
      }
    } else {
      if (five) c_main<5, 0>(S, acc, cs, s1, s2); else c_main<4, 0>(S, acc, cs, s1, s2);
      if (!atype) {
#pragma unroll
        for (int c = 0; c < 5; ++c) *(LDSP f32x4*)(lds + K1_HO + (mh * 2 + (nj >> 1)) * 5120 + c * 1024 + lane * 16) = acc[3][c];
        if (nj == 0 && tid < 120) {
          const unsigned w = *(LDSP unsigned*)(lds + K1_WPHI + tid * 4);
          *(LDSP f32x2*)(lds + K1_BL + tid * 8) = f32x2{bflo(w) * L2E, bfhi(w) * L2E};
        }
      }
    }
    DG(T[6] = S.t1;)
  } else {
    __builtin_amdgcn_s_setprio(3);
    K1H S;
    S.lds = lds; S.ldsbase = ldsbase;
    S.incw = incw;
    S.pw = (const char*)W + 2 * t1 * incw;
    S.wrapw = (unsigned)(size_t)W + 20 * incw;
    S.px = xrow + t1 * 128 + 64 * 1280;
    S.wrapx = (unsigned)(size_t)xrow + 1280;
    S.dw = 2 * WSLOT; S.dx = 3 * 8192; S.wr = 0;
    DG(S.t1 = 0;)
#pragma unroll
    for (int i = 0; i < 4; ++i) {
      S.vx[i] = xvoff(4 * mh + i);
      glds16n(S.vx[i], S.px, ldsbase + mh * 4096 + i * 1024 + 3 * 8192);
    }
#pragma unroll
    for (int i = 0; i < 8; ++i) {
      S.vw[i] = wvoff(8 * mh + i < 15 ? 8 * mh + i : 14);
      if (8 * mh + i < 15) glds16n(S.vw[i], S.pw, ldsbase + K1_W + mh * 8192 + i * 1024 + 2 * WSLOT);
    }
    S.touch = lane < 12 ? ws + (size_t)lane * (2u << 20) : lane == 12 ? (const char*)w2 : lane == 13 ? (const char*)w2 + (2u << 20) : lane == 14 ? (const char*)wo : (const char*)wphi;
#pragma unroll
    for (int c = 0; c < 5; ++c) { S.wa[c] = wa0 + c * cstr; acc[0][c] = f32x4{0.f, 0.f, 0.f, 0.f}; }
    S.rs = rs; S.l16 = lane * 16; S.w2 = (const char*)w2; S.wo = (const char*)wo;
    S.rrd = K1_RT + mh * 5120 + (lane >> 2) * 32 + (lane & 3) * 8;
    S.gi = b * 12 + mh * 6;
    S.rv2 = (lane >> 1) * 1280 + (lane & 1) * 16;
    S.rvo = ((((lane >> 1) & 12) << 1) | (((lane >> 1) & 16) >> 2) | ((lane >> 1) & 3)) * 1280 + (lane & 1) * 16;
    S.pc2 = jx < 2 ? (const char*)(kpc & ~(unsigned long long)127) : nullptr;
    if (mh == 0) h_main<0>(S, acc[0]); else h_main<1>(S, acc[0]);
    DG(T[6] = S.t1;)
    if (m == 0) {
#pragma unroll
      for (int c = 0; c < 5; ++c) *(LDSP f32x4*)(lds + K1_CS + (cj * 80 + c * 16 + 4 * g) * 4) = acc[0][c];
    }
  }
  DG(T[2] = stamp();)
  WAIT_LGKM0();
  __builtin_amdgcn_s_barrier();
  asm volatile("" ::: "memory");
  DG(T[3] = stamp();)
#define DG_OUT1() DG({ T[5] = stamp(); unsigned long long re = rstamp(); if (lane == 0) { unsigned long long* dp = (unsigned long long*)(ws + OFF_DBG) + (size_t)(b * 8 + wave) * 16; for (int i = 0; i < 8; ++i) dp[i] = T[i]; dp[8] = re; } if (lane < 20) ((unsigned*)(ws + OFF_DBG + (2u << 20)))[(b * 8 + wave) * 32 + lane] = *(LDSP unsigned*)(lds + K1_LDS + wave * 128 + lane * 4) - (unsigned)T[0]; })

  if (!atype) {
    auto round = [&](const f32x4 (&a)[5], int ej, int rt) {
      const bool five = grp < 6 && ej < 2;
      const f32x2 st = *(LDSP f32x2*)(lds + K1_STAT + (rt * 16 + m) * 8);
      u32x2 pk[5];
#pragma unroll
      for (int c = 0; c < 5; ++c) {
        if (c == 4 && !five) { pk[4] = u32x2{0u, 0u}; break; }
        const f32x4 cs = *(LDSP f32x4*)(lds + K1_CS + (ej * 80 + c * 16 + 4 * g) * 4);
        const f32x4 bl = *(LDSP f32x4*)(lds + K1_BL + (ej * 80 + c * 16 + 4 * g) * 4);
        float res[4];
#pragma unroll
        for (int e = 0; e < 4; ++e) {
          const float hl = fmaf(a[c][e], st[0], fmaf(st[1], cs[e], bl[e]));
          const float ex = __builtin_amdgcn_exp2f(hl);
          const float rc = __builtin_amdgcn_rcpf(fmaf(ex, ex + 2.f, 2.f));
          res[e] = hl * fmaf(rc, -2.f * LN2, LN2);
        }
        pk[c] = u32x2{pk2(res[0], res[1]), pk2(res[2], res[3])};
      }
      const int T0 = (c0 + sub * ej) >> 4;
      const unsigned base = OFF_ACT + ((rb * 8 + rt) * 54 + (T0 >> 1)) * 1024;
      if (!(T0 & 1)) {
        st16(rs, u32x4{pk[0][0], pk[0][1], pk[1][0], pk[1][1]}, lane * 16, base);
        st16(rs, u32x4{pk[2][0], pk[2][1], pk[3][0], pk[3][1]}, lane * 16, base + 1024);
        if (five) st8(rs, pk[4], lane * 16, base + 2048);
      } else {
        st8(rs, pk[0], lane * 16, base + 8);
        st16(rs, u32x4{pk[1][0], pk[1][1], pk[2][0], pk[2][1]}, lane * 16, base + 1024);
        st16(rs, u32x4{pk[3][0], pk[3][1], pk[4][0], pk[4][1]}, lane * 16, base + 2048);
      }
    };
    if (helper) {
#pragma unroll
      for (int r = 0; r < 2; ++r)
#pragma unroll
        for (int c = 0; c < 5; ++c) acc[r][c] = *(LDSP f32x4*)(lds + K1_HO + (mh * 2 + r) * 5120 + c * 1024 + lane * 16);
    }
#pragma unroll 1
    for (int it = 0; it < (helper ? 1 : 2); ++it) {
      round(acc[0], helper ? 0 : nj, mh * 4 + (helper ? 3 : 2 * it));
      if (it == 0 || nj == 1) round(acc[1], helper ? 2 : nj, mh * 4 + (helper ? 3 : 2 * it + 1));
#pragma unroll
      for (int c = 0; c < 5; ++c) { acc[0][c] = acc[2][c]; acc[1][c] = acc[3][c]; }
    }
    DG(T[4] = stamp();)
    DG_OUT1();
    return;
  }

  auto normalise = [&](const f32x4 (&a)[5], const f32x4 (&cs)[5], int rt, u32x4 (&bq)[3], unsigned one) {
    const f32x2 st = *(LDSP f32x2*)(lds + K1_STAT + (rt * 16 + m) * 8);
#pragma unroll
    for (int c = 0; c < 5; ++c) {
      float v[4];
#pragma unroll
      for (int e = 0; e < 4; ++e) v[e] = fmaf(a[c][e], st[0], st[1] * cs[c][e]);
      bq[c >> 1][(c & 1) * 2] = pk2(v[0], v[1]);
      bq[c >> 1][(c & 1) * 2 + 1] = pk2(v[2], v[3]);
    }
    bq[2][2] = one;
    bq[2][3] = 0u;
  };
  f32x4 csv[5];
  if (!helper && nj != 1) {
#pragma unroll
    for (int c = 0; c < 5; ++c) csv[c] = *(LDSP f32x4*)(lds + K1_CS + (nj * 80 + c * 16 + 4 * g) * 4);
#pragma unroll
    for (int r = 0; r < 4; ++r) {
      u32x4 bq[3];
      normalise(acc[r], csv, mh * 4 + r, bq, nj == 2 ? 0x00003f80u : 0u);
      if (nj == 0) {
        const unsigned so = OFF_QT + (4 * grp * 128 + rb * 8 + mh * 4 + r) * 768;
#pragma unroll
        for (int i = 0; i < 3; ++i) st16(rs, bq[i], g * (128 * 768) + m * 16, so + i * 256);
      } else {
#pragma unroll
        for (int i = 0; i < 3; ++i) *(LDSP u32x4*)(lds + K1_VIMG + (mh * 64 + r * 16 + m) * V_PITCH + g * 48 + i * 16) = bq[i];
      }
    }
  }
  if (helper || nj != 2) {
    const int kr0 = helper ? 3 : (nj == 1 ? 0 : 2);
    if (kr0) {
#pragma unroll
      for (int c = 0; c < 5; ++c) acc[0][c] = *(LDSP f32x4*)(lds + K1_HO + (mh * 2 + kr0 - 2) * 5120 + c * 1024 + lane * 16);
    }
#pragma unroll
    for (int c = 0; c < 5; ++c) csv[c] = *(LDSP f32x4*)(lds + K1_CS + (80 + c * 16 + 4 * g) * 4);
    LDSP char* wl = lds + ((p4 == g) ? K1_WPHI + g * 800 + q4 * 40 : K1_ZERO);
#pragma unroll 1
    for (int kr = kr0; kr < (kr0 ? kr0 + 1 : 2); ++kr) {
      u32x4 bq[3];
      normalise(acc[0], csv, mh * 4 + kr, bq, 0u);
      f32x4 pk[5];
      head_proj(wl, bq, pk);
      unsigned ph[20];
      feature_map(pk, ph);
      LDSP char* dst = lds + (mh * 64 + kr * 16 + m) * PK_PITCH + g * 80;
#pragma unroll
      for (int i = 0; i < 5; ++i) *(LDSP u32x4*)(dst + i * 16) = u32x4{ph[4 * i], ph[4 * i + 1], ph[4 * i + 2], ph[4 * i + 3]};
#pragma unroll
      for (int c = 0; c < 5; ++c) acc[0][c] = acc[1][c];
    }
  }
  DG(T[4] = stamp();)
  WAIT_LGKM0();
  __builtin_amdgcn_s_barrier();
  asm volatile("" ::: "memory");
  {
#pragma unroll 1
    for (int job = wave; job < 12; job += 8) {
      const int hh = job & 3, ft = job >> 2;
      LDSP char* pa = lds + (4 * g + q4) * PK_PITCH + hh * 80 + ft * 32 + p4 * 8;
      LDSP char* pb = lds + K1_VIMG + (4 * g + q4) * V_PITCH + hh * 48 + p4 * 8;
      f32x4 d0 = {0.f, 0.f, 0.f, 0.f}, d1 = {0.f, 0.f, 0.f, 0.f};
#pragma unroll
      for (int kk = 0; kk < 4; ++kk) {
        u32x4 a = trread2(pa + (32 * kk) * PK_PITCH, pa + (32 * kk + 16) * PK_PITCH);
        u32x4 v0 = trread2(pb + (32 * kk) * V_PITCH, pb + (32 * kk + 16) * V_PITCH);
        u32x4 v1 = trread2(pb + (32 * kk) * V_PITCH + 32, pb + (32 * kk + 16) * V_PITCH + 32);
        d0 = mfma(a, v0, d0);
        d1 = mfma(a, v1, d1);
      }
      const unsigned so = OFF_KVP + ((4 * grp + hh) * 16 + rb) * KVP_B + 1344 * ft;
      st16(rs, __builtin_bit_cast(u32x4, d0), m * 64 + g * 16, so);
      if (m < 5) st16(rs, __builtin_bit_cast(u32x4, d1), (16 + m) * 64 + g * 16, so);
    }
  }
  DG_OUT1();
}

constexpr unsigned K2_YD = 0;
constexpr unsigned K2_SD = 4 * 15360;
constexpr unsigned K2_KVT = K2_SD + 4 * 20480;
constexpr unsigned K2_ZERO = K2_KVT + 1920;
constexpr unsigned K2_WPHI = K2_ZERO + 1792;
constexpr unsigned K2_FLAG = K2_WPHI + 3200;
constexpr unsigned K2_OWN = K2_FLAG + 16;
constexpr unsigned K2_LDS = K2_OWN + 8192;

DEVI u32x4 bld(__amdgpu_buffer_rsrc_t rs, unsigned voff, unsigned soff) {
  return __builtin_amdgcn_raw_buffer_load_b128(rs, voff, soff, 16);
}
DEVI u32x4 bld_sc1(__amdgpu_buffer_rsrc_t rs, unsigned voff, unsigned soff) {
  return __builtin_amdgcn_raw_buffer_load_b128(rs, voff, soff, 16);
}
template <int N>
DEVI void y_stream(__amdgpu_buffer_rsrc_t rs, unsigned act, unsigned wt, int rot, bool more, f32x4 (&acc)[4][5]) {
  const unsigned l16 = (threadIdx.x & 63) * 16;
  unsigned ab[4];
#pragma unroll
  for (int l = 0; l < 4; ++l) ab[l] = act + ((l + rot) & 3) * (54 * 1024);
  u32x4 A[3][4], B[3][5];
  auto load = [&](int i) {
#pragma unroll
    for (int l = 0; l < 4; ++l) A[i % 3][l] = bld(rs, l16, ab[l] + i * 1024);
#pragma unroll
    for (int c = 0; c < 5; ++c) B[i % 3][c] = bld(rs, l16, wt + c * (74 * 1024) + i * 1024);
  };
  load(0);
  load(1);
#pragma unroll
  for (int i = 0; i < N; ++i) {
    if (i + 2 < N) load(i + 2);
    else if (i + 2 == N) { if (more) load(N); }
    __builtin_amdgcn_sched_barrier(0);
#pragma unroll
    for (int l = 0; l < 4; ++l)
#pragma unroll
      for (int c = 0; c < 5; ++c) acc[l][c] = mfma(B[i % 3][c], A[i % 3][l], acc[l][c]);
    __builtin_amdgcn_sched_barrier(0);
  }
  if (more) {
#pragma unroll
    for (int l = 0; l < 4; ++l)
#pragma unroll
      for (int c = 0; c < 5; ++c) acc[l][c] = mfma(B[N % 3][c], A[N % 3][l], acc[l][c]);
  }
}

template <int C0, int C1>
DEVI void combine(LDSP char* lds, const u32x2 (&xv)[3], u16* out, int rt, int rb2, int cb, const f32x4 (&own)[5]) {
  const int lane = threadIdx.x & 63;
  const unsigned l16 = lane * 16;
  const size_t orow = (size_t)(64 * rb2 + 16 * rt + (lane & 15)) * 640 + 80 * cb + 4 * (lane >> 4);
  WAIT_LGKM0();
  __builtin_amdgcn_s_barrier();
  asm volatile("" ::: "memory");
#pragma unroll
  for (int c = C0; c < C1; ++c) {
    f32x4 y = C0 == 0 ? own[c] : *(LDSP f32x4*)(lds + K2_OWN + (rt * 2 + c - 3) * 1024 + l16);
#pragma unroll
    for (int l = 1; l < 4; ++l)
      y += *(LDSP f32x4*)(lds + K2_YD + ((rt - l) & 3) * 15360 + ((l - 1) * 5 + c) * 1024 + l16);
    LDSP f32x4* sp = (LDSP f32x4*)(lds + K2_SD + (rt * 5 + c) * 1024 + l16);
    const f32x4 s = (sp[0] + sp[1280]) + (sp[2560] + sp[3840]);
    float o[4];
#pragma unroll
    for (int e = 0; e < 4; ++e) {
      const float gate = y[e] * __builtin_amdgcn_rcpf(1.f + __builtin_amdgcn_exp2f(-y[e] * L2E));
      const unsigned xw = xv[c - C0][e >> 1];
      o[e] = fmaf(s[e], gate, (e & 1) ? bfhi(xw) : bflo(xw));
    }
    *(u32x2*)(out + orow + 16 * c) = u32x2{pk2(o[0], o[1]), pk2(o[2], o[3])};
  }
}

template <int NW>
DEVI bool poll_tags(__amdgpu_buffer_rsrc_t rs, unsigned off, int cnt, u32x2 tag) {
  const int lane = threadIdx.x & 63;
  const unsigned vo = (lane < cnt ? lane : 0) * 64;
  for (int spin = 0; spin < (1 << 20); ++spin) {
    asm volatile("" ::: "memory");
    bool ok = true;
#pragma unroll
    for (int i = 0; i < NW; ++i) {
      const u32x2 w = __builtin_amdgcn_raw_buffer_load_b64(rs, vo, off + i * 4096, 16);
      ok = ok && w[0] == tag[0] && w[1] == tag[1];
    }
    if (__all(ok)) { asm volatile("" ::: "memory"); return true; }
    __builtin_amdgcn_s_sleep(4);
  }
  return false;
}

DEVI bool poll_y(__amdgpu_buffer_rsrc_t rs, int rb, u32x2 tag, int tries) {
  const int lane = threadIdx.x & 63;
  for (int spin = 0; spin < tries; ++spin) {
    asm volatile("" ::: "memory");
    const u32x2 a = __builtin_amdgcn_raw_buffer_load_b64(rs, (lane & 7) * 64, OFF_FL1 + (128 + rb * 8) * 64, 16);
    bool ok = a[0] == tag[0] && a[1] == tag[1];
#pragma unroll
    for (int i = 0; i < 4; ++i) {
      const u32x2 w = __builtin_amdgcn_raw_buffer_load_b64(rs, lane * 64, OFF_FLR + i * 4096, 16);
      ok = ok && w[0] == tag[0] && w[1] == tag[1];
    }
    if (__all(ok)) { asm volatile("" ::: "memory"); return true; }
    __builtin_amdgcn_s_sleep(4);
  }
  return false;
}
DEVI void go_set(LDSP char* lds, int i) {
  if ((threadIdx.x & 63) == 0) *(volatile LDSP unsigned*)(lds + F_TAG + 8 + 4 * i) = 1u;
}
DEVI void go_wait(LDSP char* lds, int i) {
  for (int spin = 0; spin < (1 << 24); ++spin) {
    if (__builtin_amdgcn_readfirstlane(*(volatile LDSP unsigned*)(lds + F_TAG + 8 + 4 * i))) break;
    __builtin_amdgcn_s_sleep(2);
  }
  asm volatile("" ::: "memory");
}

DEVI void phase2(LDSP char* lds, u32x2 tag, bool seen, const u16* __restrict__ x, const u16* __restrict__ wphi, char* __restrict__ ws,
                 u16* __restrict__ out) {
  const int tid = threadIdx.x, lane = tid & 63;
  const int wave = __builtin_amdgcn_readfirstlane(tid >> 6);
  const int g = lane >> 4, m = lane & 15, q4 = m >> 2, p4 = lane & 3;
  const int b = blockIdx.x, xcd = b & 7, jx = b >> 3;
  const int rb2 = 8 * (xcd >> 1) + (jx & 7), cb = 4 * (xcd & 1) + (jx >> 3);
  const __amdgpu_buffer_rsrc_t rs = __builtin_amdgcn_make_buffer_rsrc((void*)ws, 0, WS_END, 0x00020000);
  const unsigned l16 = lane * 16;
  DG(unsigned long long T[8]; T[0] = stamp(); T[7] = rstamp(); T[1] = T[2] = T[3] = T[4] = T[5] = T[6] = 0;)
#define DG_OUT2() DG({ unsigned long long re = rstamp(); if (lane == 0) { unsigned long long* dp = (unsigned long long*)(ws + OFF_DBG + (1u << 20)) + (size_t)(b * 8 + wave) * 16; for (int i = 0; i < 8; ++i) dp[i] = T[i]; dp[8] = re; } })
  asm volatile("" ::"s"(x), "s"(wphi), "s"(out));
  if (wave == 4) {
    *(LDSP u32x4*)(lds + K2_ZERO + l16) = u32x4{0u, 0u, 0u, 0u};
    if (lane < 48) *(LDSP u32x4*)(lds + K2_ZERO + 1024 + l16) = u32x4{0u, 0u, 0u, 0u};
    if (lane == 0) *(LDSP u32x4*)(lds + K2_FLAG) = u32x4{0u, 0u, 0u, 0u};
  }
  const unsigned act = OFF_ACT + 4 * rb2 * (54 * 1024), wt = OFF_WT + 5 * cb * (74 * 1024);
  const u16* xt = x + (size_t)(64 * rb2 + 16 * (wave & 3) + m) * 640 + 80 * cb + 4 * g;
  f32x4 acc[4][5];
#pragma unroll
  for (int r = 0; r < 4; ++r)
#pragma unroll
    for (int c = 0; c < 5; ++c) acc[r][c] = f32x4{0.f, 0.f, 0.f, 0.f};

  if (wave < 4) {
    u32x2 xv[3];
#pragma unroll
    for (int c = 0; c < 3; ++c) xv[c] = *(const u32x2*)(xt + 16 * c);
    if (wave == 0 && !seen) {
      poll_y(rs, rb2 >> 1, tag, 1 << 18);
      go_set(lds, 0);
    } else {
      go_wait(lds, 0);
    }
    DG(T[1] = stamp();)
    const unsigned k0 = (wave < 2 ? 14 * wave : 2 + 13 * wave) * 1024;
    y_stream<13>(rs, act + k0, wt + k0, wave, wave < 2, acc);
    DG(T[3] = stamp();)
#pragma unroll
    for (int l = 1; l < 4; ++l)
#pragma unroll
      for (int c = 0; c < 5; ++c)
        *(LDSP f32x4*)(lds + K2_YD + wave * 15360 + ((l - 1) * 5 + c) * 1024 + l16) = acc[l][c];
#pragma unroll
    for (int c = 3; c < 5; ++c) *(LDSP f32x4*)(lds + K2_OWN + (wave * 2 + c - 3) * 1024 + l16) = acc[0][c];
    DG(T[4] = stamp();)
    combine<0, 3>(lds, xv, out, wave, rb2, cb, acc[0]);
    if (b == 0 && wave == 0 && poll_tags<4>(rs, OFF_FLR, 64, tag) && lane == 0) __builtin_amdgcn_raw_buffer_store_b64(tag, rs, OFF_SYNC, 0, 16);
    DG(T[6] = stamp();)
    DG_OUT2();
    return;
  }

  const int aw = wave - 4;
  const int h = 4 * xcd + (jx & 3), sb = jx >> 2;
  const int tok = sb * 256 + aw * 64 + lane;
  __builtin_amdgcn_s_setprio(2);
  u32x4 wv = *(const u32x4*)(wphi + h * 400 + (lane < 50 ? lane : 49) * 8);
  if (aw == 0) {
    poll_tags<1>(rs, OFF_FL1 + (h >> 2) * 16 * 64, 16, tag);
    WAIT_LGKM0();
    go_set(lds, 1);
  } else {
    go_wait(lds, 1);
  }
  u32x4 bq[3];
#pragma unroll
  for (int s = 0; s < 3; ++s) bq[s] = bld(rs, (unsigned)(tok >> 4) * 768 + m * 16, OFF_QT + h * (128 * 768) + s * 256);
  int item = aw * 53 + (lane < 53 ? lane : 52);
  item = item < 210 ? item : 209;
  const int d = item / 10, fq = item - 10 * d;
  u32x4 part[16];
#pragma unroll
  for (int r = 0; r < 16; ++r) part[r] = bld(rs, (unsigned)((fq >> 2) * 1344 + d * 64 + (fq & 3) * 16), OFF_KVP + (h * 16 + r) * KVP_B);
  *(LDSP u32x4*)(lds + K2_WPHI + aw * 800 + (lane < 50 ? lane : 49) * 16) = wv;
  unsigned ph[20];
  {
    LDSP char* wl = lds + ((p4 == g) ? K2_WPHI + aw * 800 + q4 * 40 : K2_ZERO);
    f32x4 pq[5];
    head_proj(wl, bq, pq);
    feature_map(pq, ph);
#pragma unroll
    for (int i = 0; i < 20; ++i) asm volatile("" : "+v"(ph[i]));
  }
  DG(T[1] = stamp();)
  {
    f32x4 sum = __builtin_bit_cast(f32x4, part[0]);
#pragma unroll
    for (int r = 1; r < 16; ++r) sum += __builtin_bit_cast(f32x4, part[r]);
    *(LDSP u32x2*)(lds + K2_KVT + d * 80 + fq * 8) = u32x2{pk2(sum[0], sum[1]), pk2(sum[2], sum[3])};
    WAIT_LGKM0();
    if (lane == 0) *(volatile LDSP unsigned*)(lds + K2_FLAG + aw * 4) = 1u;
    for (int spin = 0; spin < (1 << 22); ++spin) {
      const u32x4 f = *(volatile LDSP u32x4*)(lds + K2_FLAG);
      if (__builtin_amdgcn_readfirstlane(f[0] & f[1] & f[2] & f[3])) break;
      __builtin_amdgcn_s_sleep(1);
    }
    asm volatile("" ::: "memory");
  }
  DG(T[2] = stamp();)
  {
    LDSP char* kl = lds + (((m >> 2) == g) ? K2_KVT + (m & 3) * 80 : K2_ZERO);
    f32x4 nd[6];
#pragma unroll
    for (int t = 0; t < 6; ++t) {
      f32x4 c = {0.f, 0.f, 0.f, 0.f};
#pragma unroll
      for (int s = 0; s < 5; ++s)
        c = mfma(*(LDSP u32x4*)(kl + t * 320 + s * 16), u32x4{ph[4 * s], ph[4 * s + 1], ph[4 * s + 2], ph[4 * s + 3]}, c);
      nd[t] = c;
    }
    const float rd = __builtin_amdgcn_rcpf(nd[5][0] + 1e-6f);
    unsigned ow[10];
#pragma unroll
    for (int t = 0; t < 5; ++t) {
      ow[2 * t] = pk2(nd[t][0] * rd, nd[t][1] * rd);
      ow[2 * t + 1] = pk2(nd[t][2] * rd, nd[t][3] * rd);
    }
    const int e0 = (5 * h + 1) >> 1, e8 = (h & 1) ? e0 - 1 : e0 + 2;
    const unsigned vo = (unsigned)(tok >> 4) * 20480 + m * 16;
    const bool odd = h & 1;
    st16(rs, odd ? u32x4{ow[2], ow[3], ow[4], ow[5]} : u32x4{ow[0], ow[1], ow[2], ow[3]}, vo, OFF_OT + e0 * 256);
    st16(rs, odd ? u32x4{ow[6], ow[7], ow[8], ow[9]} : u32x4{ow[4], ow[5], ow[6], ow[7]}, vo, OFF_OT + (e0 + 1) * 256);
    st8(rs, odd ? u32x2{ow[0], ow[1]} : u32x2{ow[8], ow[9]}, vo, OFF_OT + e8 * 256 + (h & 1) * 8);
    WAIT_VM(0);
    if (lane == 0) __builtin_amdgcn_raw_buffer_store_b64(tag, rs, OFF_FO + ((sb * 4 + aw) * 32 + h) * 64, 0, 16);
  }
  DG(T[3] = stamp();)
  const unsigned ring = K2_SD + aw * 20480;
  u32x2 xv[3];
#pragma unroll
  for (int c = 0; c < 2; ++c) xv[c] = *(const u32x2*)(xt + 48 + 16 * c);
  xv[2] = xv[1];
  go_wait(lds, 0);
  {
    u32x4 B[5][5], A[5][4];
    auto loadb = [&](int i) {
#pragma unroll
      for (int c = 0; c < 5; ++c) B[i][c] = bld(rs, l16, wt + c * (74 * 1024) + (54 + 5 * aw + i) * 1024);
    };
    loadb(0);
    loadb(1);
    loadb(2);
    const unsigned ob = OFF_OT + (4 * rb2 * 20 + 5 * aw) * 1024;
    poll_tags<1>(rs, OFF_FO + (rb2 * 32 + 8 * aw) * 64, 8, tag);
    DG(T[4] = stamp();)
#pragma unroll
    for (int i = 0; i < 5; ++i)
#pragma unroll
      for (int l = 0; l < 4; ++l) A[i][l] = bld(rs, l16, ob + l * 20480 + i * 1024);
#pragma unroll
    for (int i = 0; i < 5; ++i) {
      if (i == 1) loadb(3);
      if (i == 2) loadb(4);
      __builtin_amdgcn_sched_barrier(0);
#pragma unroll
      for (int l = 0; l < 4; ++l)
#pragma unroll
        for (int c = 0; c < 5; ++c) acc[l][c] = mfma(B[i][c], A[i][l], acc[l][c]);
      __builtin_amdgcn_sched_barrier(0);
    }
  }
#pragma unroll
  for (int l = 0; l < 4; ++l)
#pragma unroll
    for (int c = 0; c < 5; ++c) *(LDSP f32x4*)(lds + ring + (l * 5 + c) * 1024 + l16) = acc[l][c];
  DG(T[6] = stamp();)
  combine<3, 5>(lds, xv, out, aw, rb2, cb, acc[0]);
  DG_OUT2();
}

static_assert(K1_LDS + 1024 <= F_TAG && K2_LDS <= F_TAG, "LDS map");

__global__ __launch_bounds__(512, 2) void hedgehog_fused(const u16* __restrict__ x, const u16* __restrict__ wqkv,
                                                         const u16* __restrict__ wphi, const u16* __restrict__ wo,
                                                         const u16* __restrict__ w1, const u16* __restrict__ b1,
                                                         const u16* __restrict__ w2, char* __restrict__ ws,
                                                         u16* __restrict__ out) {
  extern __shared__ __attribute__((aligned(16))) char smem[];
  LDSP char* lds = (LDSP char*)smem;
  phase1(lds, __builtin_amdgcn_s_getpc(), x, wqkv, wphi, wo, w1, b1, w2, ws);
  int tid = threadIdx.x;
  asm volatile("" : "+v"(tid));
  const __amdgpu_buffer_rsrc_t rs = __builtin_amdgcn_make_buffer_rsrc((void*)ws, 0, WS_END, 0x00020000);
  const u32x2 tag = *(LDSP u32x2*)(lds + F_TAG);
  bool seen = false;
  if (tid < 64) {
    seen = poll_y(rs, 4 * ((blockIdx.x & 7) >> 1) + (((blockIdx.x >> 3) & 7) >> 1), tag, 1);
    if (seen) go_set(lds, 0);
  }
  WAIT_VM(0);
  WAIT_LGKM0();
  __builtin_amdgcn_s_barrier();
  asm volatile("" ::: "memory");
  if (tid == 0) {
    const int b = blockIdx.x, xcd = b & 7, jx = b >> 3;
    const int rb = 8 * (xcd >> 2) + (jx & 7), grp = 2 * (xcd & 3) + ((jx >> 3) & 1);
    __builtin_amdgcn_raw_buffer_store_b64(tag, rs, OFF_FL1 + (jx < 16 ? grp * 16 + rb : 128 + rb * 8 + grp) * 64, 0, 16);
  }
  phase2(lds, tag, seen, x, wphi, ws, out);
}

extern "C" void kernel_launch(void* const* d_in, const int* in_sizes, int n_in, void* d_out, int out_size, void* d_ws,
                              size_t ws_size, hipStream_t stream) {
  const u16* x = (const u16*)d_in[0];
  const u16* wqkv = (const u16*)d_in[1];
  const u16* wphi = (const u16*)d_in[2];
  const u16* wo = (const u16*)d_in[3];
  const u16* w1 = (const u16*)d_in[5];
  const u16* b1 = (const u16*)d_in[6];
  const u16* w2 = (const u16*)d_in[7];
  (void)hipFuncSetAttribute((const void*)hedgehog_fused, hipFuncAttributeMaxDynamicSharedMemorySize, F_LDS);
  hedgehog_fused<<<256, 512, F_LDS, stream>>>(x, wqkv, wphi, wo, w1, b1, w2, (char*)d_ws, (u16*)d_out);
}
```

```cpp
#define ROT3
#include <hip/hip_runtime.h>
#include <stdint.h>

typedef unsigned short u16;
typedef __attribute__((ext_vector_type(2))) __bf16 bf16x2;
typedef __attribute__((ext_vector_type(8))) __bf16 bf16x8;
typedef __attribute__((ext_vector_type(4))) short s16x4;
typedef __attribute__((ext_vector_type(2))) float f32x2;
typedef __attribute__((ext_vector_type(4))) float f32x4;
typedef __attribute__((ext_vector_type(2))) unsigned u32x2;
typedef __attribute__((ext_vector_type(4))) unsigned u32x4;
#define LDSP __attribute__((address_space(3)))
#define DEVI __device__ __forceinline__

constexpr unsigned OFF_ACT = 0;
constexpr unsigned OFF_WT = OFF_ACT + 128u * 54u * 1024u;
constexpr unsigned OFF_OT = OFF_WT + 40u * 74u * 1024u;
constexpr unsigned OFF_QT = OFF_OT + 128u * 20u * 1024u;
constexpr unsigned OFF_KVP = OFF_QT + 32u * 2048u * 48u;
constexpr unsigned KVP_B = 3 * 1344;
constexpr unsigned OFF_SYNC = OFF_KVP + 32u * 16u * KVP_B;
constexpr unsigned OFF_FL1 = OFF_SYNC + 128;
constexpr unsigned OFF_FLR = OFF_FL1 + 256 * 64;
constexpr unsigned OFF_FO = OFF_FLR + 256 * 64;
constexpr unsigned WS_END = OFF_FO + 1024 * 64;

#define CODE_BYTES (48 * 1024)
#define K2_BPRE 4
#define CODE2_OFF (36 * 1024)
#define K2_NY 11
#define K1_MAP 1
constexpr float L2E = 1.4426950408889634f, LN2 = 0.6931471805599453f;

DEVI unsigned pk2(float a, float b) {
  bf16x2 v = {(__bf16)a, (__bf16)b};
  return __builtin_bit_cast(unsigned, v);
}
DEVI float bflo(unsigned u) { return __uint_as_float(u << 16); }
DEVI float bfhi(unsigned u) { return __uint_as_float(u & 0xffff0000u); }
DEVI f32x4 mfma(u32x4 a, u32x4 b, f32x4 c) {
  return __builtin_amdgcn_mfma_f32_16x16x32_bf16(__builtin_bit_cast(bf16x8, a), __builtin_bit_cast(bf16x8, b), c, 0, 0, 0);
}
DEVI u32x2 trread(LDSP char* p) {
  return __builtin_bit_cast(u32x2, __builtin_amdgcn_ds_read_tr16_b64_v4i16((LDSP s16x4*)p));
}
DEVI u32x4 trread2(LDSP char* p0, LDSP char* p1) {
  u32x2 a = trread(p0), b = trread(p1);
  return u32x4{a[0], a[1], b[0], b[1]};
}
DEVI float dot2(unsigned a, unsigned b, float c) {
  return __builtin_amdgcn_fdot2_f32_bf16(__builtin_bit_cast(bf16x2, a), __builtin_bit_cast(bf16x2, b), c, false);
}
DEVI float dot_ones(u32x4 a, float c) {
  c = dot2(a[0], 0x3f803f80u, c); c = dot2(a[1], 0x3f803f80u, c);
  c = dot2(a[2], 0x3f803f80u, c); c = dot2(a[3], 0x3f803f80u, c);
  return c;
}
DEVI float dot_self(u32x4 a, float c) {
  c = dot2(a[0], a[0], c); c = dot2(a[1], a[1], c); c = dot2(a[2], a[2], c); c = dot2(a[3], a[3], c);
  return c;
}
DEVI float red_g(float v) {
  v += __shfl_xor(v, 16);
  v += __shfl_xor(v, 32);
  return v;
}
DEVI void glds16(unsigned voff, const void* sbase, unsigned lds_dst) {
  unsigned keep;
  asm volatile("s_mov_b32 %0, m0\n\ts_mov_b32 m0, %3\n\ts_nop 0\n\tglobal_load_lds_dwordx4 %1, %2\n\ts_mov_b32 m0, %0"
               : "=&s"(keep) : "v"(voff), "s"(sbase), "s"(lds_dst) : "memory");
}
#define DG(...)
#define WAIT_VM(n) asm volatile("s_waitcnt vmcnt(" #n ")" ::: "memory")
#define WAIT_LGKM0() asm volatile("s_waitcnt lgkmcnt(0)" ::: "memory")

DEVI void feature_map(const f32x4 (&p)[5], unsigned (&o)[20]) {
  float mx = p[0][0], mn = p[0][0];
#pragma unroll
  for (int t = 0; t < 5; ++t)
#pragma unroll
    for (int r = 0; r < 4; ++r) { mx = fmaxf(mx, p[t][r]); mn = fminf(mn, p[t][r]); }
  const float a = -mx * L2E, b = mn * L2E;
  float e1[20], e2[20], s1 = 0.f, s2 = 0.f;
#pragma unroll
  for (int t = 0; t < 5; ++t)
#pragma unroll
    for (int r = 0; r < 4; ++r) {
      e1[4 * t + r] = __builtin_amdgcn_exp2f(fmaf(p[t][r], L2E, a));
      e2[4 * t + r] = __builtin_amdgcn_exp2f(fmaf(p[t][r], -L2E, b));
      s1 += e1[4 * t + r]; s2 += e2[4 * t + r];
    }
  const float r1 = __builtin_amdgcn_rcpf(s1), r2 = __builtin_amdgcn_rcpf(s2);
#pragma unroll
  for (int i = 0; i < 10; ++i) {
    o[i] = pk2(e1[2 * i] * r1, e1[2 * i + 1] * r1);
    o[10 + i] = pk2(e2[2 * i] * r2, e2[2 * i + 1] * r2);
  }
}
DEVI void head_proj(LDSP char* wl, const u32x4 (&b)[3], f32x4 (&p)[5]) {
#pragma unroll
  for (int t = 0; t < 5; ++t) {
    u32x4 a0 = trread2(wl + 0 * 40 + t * 8, wl + 4 * 40 + t * 8);
    u32x4 a1 = trread2(wl + 8 * 40 + t * 8, wl + 12 * 40 + t * 8);
    u32x2 h2 = trread(wl + 16 * 40 + t * 8);
    u32x4 a2 = {h2[0], h2[1], 0u, 0u};
    f32x4 c = {0.f, 0.f, 0.f, 0.f};
    c = mfma(a0, b[0], c);
    c = mfma(a1, b[1], c);
    c = mfma(a2, b[2], c);
    p[t] = c;
  }
}

constexpr unsigned XSLOT = 16384, WSUB = 5120, WSLOT = 3 * WSUB;
constexpr unsigned K1_W = 4 * XSLOT;
constexpr unsigned K1_WPHI = K1_W + 5 * WSLOT;
constexpr unsigned K1_ZERO = K1_WPHI + 3200;
constexpr unsigned K1_STAT = K1_ZERO + 768;
constexpr unsigned K1_CS = K1_STAT + 1024;
constexpr unsigned K1_BL = K1_CS + 960;
constexpr unsigned K1_SCR = K1_BL + 960;
constexpr unsigned K1_MB = K1_SCR + 256;
constexpr unsigned K1_RT = K1_MB + 16;
constexpr unsigned K1_LDS = K1_RT + 10240;
constexpr unsigned F_TAG = 160832;
constexpr unsigned F_LDS = F_TAG + 16;
constexpr unsigned PK_PITCH = 352, V_PITCH = 224, K1_VIMG = 128 * PK_PITCH;
constexpr unsigned K1_HO = K1_VIMG + 128 * V_PITCH;

DEVI void glds16n(unsigned voff, const void* sbase, unsigned lds_dst) {
  asm volatile("s_mov_b32 m0, %2\n\ts_nop 0\n\tglobal_load_lds_dwordx4 %0, %1" ::"v"(voff), "s"(sbase), "s"(lds_dst) : "memory");
}
DEVI void st16(__amdgpu_buffer_rsrc_t rs, u32x4 v, unsigned voff, unsigned soff) {
  __builtin_amdgcn_raw_buffer_store_b128(v, rs, voff + soff, 0, 16);
}
DEVI void st8(__amdgpu_buffer_rsrc_t rs, u32x2 v, unsigned voff, unsigned soff) {
  __builtin_amdgcn_raw_buffer_store_b64(v, rs, voff + soff, 0, 16);
}
#define STEP_SYNC() do { __builtin_amdgcn_s_waitcnt(0xC07F); __builtin_amdgcn_s_barrier(); __builtin_amdgcn_sched_barrier(0); } while (0)

DEVI void mb_arrive(LDSP char* lds, int i) {
  WAIT_LGKM0();
  if ((threadIdx.x & 63) == 0) __hip_atomic_fetch_add((LDSP unsigned*)(lds + K1_MB + 4 * i), 1u, __ATOMIC_RELAXED, __HIP_MEMORY_SCOPE_WORKGROUP);
}
DEVI void mb_wait(LDSP char* lds, int i) {
  for (int spin = 0; spin < (1 << 22); ++spin) {
    if (__builtin_amdgcn_readfirstlane(*(volatile LDSP unsigned*)(lds + K1_MB + 4 * i)) >= 6u) break;
    __builtin_amdgcn_s_sleep(1);
  }
  asm volatile("" ::: "memory");
  __builtin_amdgcn_sched_barrier(0);
}
struct K1C {
  LDSP char* lds;
  unsigned xa, wa[5], wr;
  DG(unsigned long long t1;)
};
template <int NC>
DEVI void c_read(K1C& S, int s, u32x4 (&xf)[4], u32x4 (&wf)[5]) {
  LDSP char* px = S.lds + ((s >> 1) & 3) * XSLOT + (S.xa ^ ((s & 1) << 6));
  LDSP char* pw = S.lds + S.wr;
  xf[0] = *(LDSP u32x4*)(px);
  wf[0] = trread2(pw + S.wa[0], pw + S.wa[0] + 1280);
#pragma unroll
  for (int r = 1; r < 4; ++r) xf[r] = *(LDSP u32x4*)(px + r * 2048);
#pragma unroll
  for (int c = 1; c < NC; ++c) wf[c] = trread2(pw + S.wa[c], pw + S.wa[c] + 1280);
  S.wr = S.wr == 4 * WSLOT ? 0 : S.wr + WSLOT;
}
template <int NC, int ROLE, bool READ, bool MINI = false>
DEVI void c_step(K1C& S, int s, const u32x4 (&xc)[4], const u32x4 (&wc)[5], u32x4 (&xn)[4], u32x4 (&wn)[5], f32x4 (&acc)[4][5],
                 f32x4 (&cs)[3], f32x4 (&s1)[4], f32x4 (&s2)[4], const u32x4& ones) {
  if (READ) {
    if (MINI) { WAIT_VM(0); mb_arrive(S.lds, 1); mb_wait(S.lds, 1); } else STEP_SYNC();
    c_read<NC>(S, s + 1, xn, wn);
  }
#pragma unroll
  for (int c = 0; c < NC; ++c)
#pragma unroll
    for (int r = 0; r < 4; ++r) acc[r][c] = mfma(wc[c], xc[r], acc[r][c]);
  if (ROLE) {
    constexpr int C0 = ROLE == 1 ? 0 : NC - 2, C1 = ROLE == 1 ? NC - 2 : NC;
#pragma unroll
    for (int r = 0; r < 4; ++r) {
      s1[r] = mfma(ones, xc[r], s1[r]);
      s2[r] = mfma(xc[r], xc[r], s2[r]);
    }
#pragma unroll
    for (int c = C0; c < C1; ++c) cs[c - C0] = mfma(wc[c], ones, cs[c - C0]);
  }
  if (READ) {
#pragma unroll
    for (int i = 0; i < 4 + 2 * NC; ++i) {
      __builtin_amdgcn_sched_group_barrier(0x8, 1, 0);
      __builtin_amdgcn_sched_group_barrier(0x100, 1, 0);
    }
  }
  __builtin_amdgcn_sched_barrier(0);
}
template <int NC, int ROLE>
DEVI void c_main(K1C& S, f32x4 (&acc)[4][5], f32x4 (&cs)[3], f32x4 (&s1)[4], f32x4 (&s2)[4]) {
  u32x4 xa[4], wa[5], xb[4], wb[5];
  u32x4 ones = {0x3f803f80u, 0x3f803f80u, 0x3f803f80u, 0x3f803f80u};
  asm volatile("" : "+v"(ones));
  c_read<NC>(S, 0, xa, wa);
  c_step<NC, ROLE, true, true>(S, 0, xa, wa, xb, wb, acc, cs, s1, s2, ones);
#pragma unroll 1
  for (int s = 1; s < 19; s += 2) {
    c_step<NC, ROLE, true>(S, s, xb, wb, xa, wa, acc, cs, s1, s2, ones);
    c_step<NC, ROLE, true>(S, s + 1, xa, wa, xb, wb, acc, cs, s1, s2, ones);
  }
  c_step<NC, ROLE, false>(S, 19, xb, wb, xa, wa, acc, cs, s1, s2, ones);
}

struct K1H {
  LDSP char* lds;
  unsigned ldsbase;
  unsigned vw[8], vx[4];
  const char* pw; const char* px;
  unsigned incw, wrapw, wrapx;
  unsigned dw, dx;
  unsigned wa[5], wr;
  __amdgpu_buffer_rsrc_t rs;
  const char* w2; const char* wo;
  unsigned rv2, rvo, rrd, l16;
  int gi;
  const char* pc2;
  const char* touch;
  DG(unsigned long long t1;)
};
template <int HP, int ODD, bool SKIP = false>
DEVI void h_issue(K1H& S) {
  if (!SKIP) {
#pragma unroll
    for (int i = 0; i < 8 - HP; ++i) glds16n(S.vw[i], S.pw, S.ldsbase + K1_W + HP * 8192 + i * 1024 + S.dw);
#pragma unroll
    for (int i = 0; i < 4; ++i) glds16n(S.vx[i], S.px, S.ldsbase + HP * 4096 + i * 1024 + S.dx);
  }
  S.pw += S.incw;
  S.pw -= (unsigned)(size_t)S.pw == S.wrapw ? 18 * S.incw : 0u;
  S.px += ODD ? 128 - 64 * 1280 : 64 * 1280;
  S.px -= (unsigned)(size_t)S.px == S.wrapx ? 1152u : 0u;
  S.dw = S.dw == 4 * WSLOT ? 0 : S.dw + WSLOT;
  S.dx = (S.dx + 8192) & 0xffff;
}
template <int HP>
DEVI void h_retile(K1H& S, int s) {
  if (s >= 11) {
    const int i = s - 11, gi = S.gi + i, cq = (gi >> 2) / 74, ks = (gi >> 2) - 74 * cq;
    LDSP char* pr = S.lds + S.rrd + (i == 5 ? 0 : i) * 1024;
    st16(S.rs, trread2(pr, pr + 512), S.l16, gi < 2960 ? OFF_WT + ((4 * cq + (gi & 3)) * 74 + ks) * 1024 : WS_END);
  }
  if (s < 12) {
    const int i = s - 6, gi = S.gi + i, fid = gi < 2960 ? gi : 2959, cq = (fid >> 2) / 74, ks = (fid >> 2) - 74 * cq;
    const char* src = (ks < 54 ? S.w2 + ks * 40960 : S.wo + (ks - 54) * 40960) + (4 * cq + (fid & 3)) * 32;
    glds16n(ks < 54 ? S.rv2 : S.rvo, src, S.ldsbase + K1_RT + HP * 5120 + (i == 5 ? 0 : i) * 1024);
  }
  __builtin_amdgcn_sched_barrier(0);
}
template <int HP, int G> DEVI void h_wait() {
  constexpr int N = G * (12 - HP);
  asm volatile("s_waitcnt vmcnt(%0)" ::"i"(N) : "memory");
}
DEVI void h_read(K1H& S, u32x4 (&wf)[5]) {
  LDSP char* pw = S.lds + S.wr;
#pragma unroll
  for (int c = 0; c < 5; ++c) wf[c] = trread2(pw + S.wa[c], pw + S.wa[c] + 1280);
  S.wr = S.wr == 4 * WSLOT ? 0 : S.wr + WSLOT;
}
template <int HP, int G, int ISSUE>
DEVI void h_step(K1H& S, const u32x4 (&wc)[5], u32x4 (&wn)[5], f32x4 (&cs)[5], const u32x4& ones) {
  if (G >= 0 && G < 9) h_wait<HP, (G >= 0 && G < 9 ? G : 0)>();
  if (G >= 0) STEP_SYNC();
#pragma unroll
  for (int c = 0; c < 5; ++c) cs[c] = mfma(wc[c], ones, cs[c]);
  h_read(S, wn);
  __builtin_amdgcn_sched_barrier(0);
  if (ISSUE) h_issue<HP, ISSUE - 1>(S);
  __builtin_amdgcn_sched_barrier(0);
}
template <int HP>
DEVI void h_main(K1H& S, f32x4 (&cs)[5]) {
  u32x4 wa[5], wb[5];
  u32x4 ones = {0x3f803f80u, 0x3f803f80u, 0x3f803f80u, 0x3f803f80u};
  asm volatile("" : "+v"(ones));
  h_issue<HP, 1, true>(S);
  h_issue<HP, 0>(S);
  h_issue<HP, 1>(S);
  if (HP == 0) {
    if ((threadIdx.x & 63) < 16) asm volatile("s_mov_b32 m0, %1\n\ts_nop 0\n\tglobal_load_lds_dword %0, off" ::"v"(S.touch), "s"(S.ldsbase + K1_SCR) : "memory");
  }
  mb_wait(S.lds, 0);
  h_read(S, wa);
  mb_wait(S.lds, 1);
  h_step<HP, -1, 1>(S, wa, wb, cs, ones);
  h_step<HP, 3, 2>(S, wb, wa, cs, ones);
#pragma unroll 1
  for (int s = 2; s < 14; s += 2) {
    h_step<HP, 3, 1>(S, wa, wb, cs, ones);
    if (s >= 6) h_retile<HP>(S, s);
    h_step<HP, 3, 2>(S, wb, wa, cs, ones);
    if (s >= 6) h_retile<HP>(S, s + 1);
  }
  h_step<HP, 3, 1>(S, wa, wb, cs, ones);
  h_retile<HP>(S, 14);
  const int lane = threadIdx.x & 63;
  h_step<HP, 3, 0>(S, wb, wa, cs, ones);
  h_retile<HP>(S, 15);
  if (S.pc2) {
    asm volatile("s_mov_b32 m0, %1\n\ts_nop 0\n\tglobal_load_lds_dword %0, off" ::"v"(S.pc2 + min(CODE2_OFF + HP * 8192 + lane * 128, CODE_BYTES)), "s"(S.ldsbase + K1_SCR) : "memory");
  }
  h_step<HP, 2, 0>(S, wa, wb, cs, ones);
  h_retile<HP>(S, 16);
  h_step<HP, 1, 0>(S, wb, wa, cs, ones);
  h_step<HP, 0, 0>(S, wa, wb, cs, ones);
  if (lane == 0) {
    if (__hip_atomic_fetch_add((LDSP unsigned*)(S.lds + K1_MB + 8), 1u, __ATOMIC_RELAXED, __HIP_MEMORY_SCOPE_WORKGROUP) == 1u)
      __builtin_amdgcn_raw_buffer_store_b64(*(LDSP u32x2*)(S.lds + F_TAG), S.rs, OFF_FLR + blockIdx.x * 64, 0, 16);
  }
#pragma unroll
  for (int c = 0; c < 5; ++c) cs[c] = mfma(wb[c], ones, cs[c]);
  __builtin_amdgcn_s_setprio(0);
}

DEVI void phase1(LDSP char* lds, unsigned long long kpc, const u16* __restrict__ x, const u16* __restrict__ wqkv,
                 const u16* __restrict__ wphi, const u16* __restrict__ wo, const u16* __restrict__ w1,
                 const u16* __restrict__ b1, const u16* __restrict__ w2, char* __restrict__ ws) {
  const int tid = threadIdx.x, lane = tid & 63;
  const int wave = __builtin_amdgcn_readfirstlane(tid >> 6);
  const int b = blockIdx.x, xcd = b & 7, jx = b >> 3;
  const unsigned ldsbase = (unsigned)(size_t)lds;
  const bool helper = wave >= 6;
  DG(unsigned long long T[8]; T[0] = stamp(); T[7] = rstamp(); T[1] = T[6] = 0;)
  if (helper) {
    const char* pc = (const char*)(kpc & ~(unsigned long long)127) + min(((jx & 3) * 2 + (wave & 1)) * 8192 + lane * 128, CODE_BYTES);
    asm volatile("s_mov_b32 m0, %1\n\ts_nop 0\n\tglobal_load_lds_dword %0, off" ::"v"(pc), "s"(ldsbase + K1_SCR) : "memory");
  }
  if (tid == 0) { *(LDSP u32x4*)(lds + K1_MB) = u32x4{0u, 0u, 0u, 0u}; *(LDSP u32x2*)(lds + F_TAG + 8) = u32x2{0u, 0u}; }
  WAIT_LGKM0();
  asm volatile("" ::"s"(x), "s"(wqkv), "s"(wphi), "s"(wo), "s"(w1), "s"(b1), "s"(w2), "s"(ws));
  const int rb = 8 * (xcd >> 2) + (jx & 7);
  const char* xrow = (const char*)x + (size_t)rb * 128 * 1280;
  auto xvoff = [&](int pc) {
    const int row = pc * 8 + (lane >> 3);
    return (unsigned)(row * 1280 + (((lane & 7) ^ ((row >> 1) & 7)) << 4));
  };
  const unsigned x0 = xvoff(wave), x1 = x0 + 48 * 1280;
  if (!helper) {
    glds16n(x0, xrow, ldsbase + wave * 1024);
    glds16n(x0, xrow + 64 * 1280, ldsbase + 8192 + wave * 1024);
    if (wave < 2) {
      glds16n(x1, xrow, ldsbase + wave * 1024 + 6144);
      glds16n(x1, xrow + 64 * 1280, ldsbase + 8192 + wave * 1024 + 6144);
    }
  }
  __builtin_amdgcn_s_barrier();
  const int mh = wave & 1;
  const int nj = wave < 4 ? wave >> 1 : 2;
  const int cj = helper ? 2 * mh : nj;
  const int g = lane >> 4, m = lane & 15, q4 = m >> 2, p4 = lane & 3;
  const int cgi = jx >> 3;
  const bool atype = cgi < 2;
  const int grp = 2 * (xcd & 3) + (cgi & 1);
  const u16* W = atype ? wqkv : w1;
  const int ldw = atype ? 1920 : 1728;
  const int c0 = atype ? 80 * grp : (grp < 6 ? 224 * grp : 1344 + 192 * (grp - 6));
  const int sub = atype ? 640 : (grp < 6 ? 80 : 64);
  const unsigned incw = 64 * ldw;
  const int t1 = 1 + 3 * (((jx & 7) + cgi) % 3);
  auto wvoff = [&](int id) {
    const int sj = id / 5, ss = id - 5 * sj;
    const int c = 64 * ss + lane, rho = c / 10, cc = c - 10 * rho;
    const int k = (rho & 0x13) | ((rho & 4) << 1) | ((rho & 8) >> 1);
    const bool narrow = !atype && (grp >= 6 || sj == 2);
    return (unsigned)(k * ldw + c0 + sub * sj + 8 * ((narrow && cc >= 8) ? cc - 2 : cc)) * 2;
  };
  const unsigned wa0 = K1_W + cj * WSUB + (16 * (g >> 1) + 4 * (g & 1) + q4) * 160 + p4 * (atype ? 40 : 8);
  const unsigned cstr = atype ? 8 : 32;
  const __amdgpu_buffer_rsrc_t rs = __builtin_amdgcn_make_buffer_rsrc((void*)ws, 0, WS_END, 0x00020000);
  f32x4 acc[4][5];

  if (!helper) {
    auto xgrp = [&](int u, int xt) {
      const char* px = xrow + xt * 128 + (u & 1) * (64 * 1280);
      const unsigned d = ldsbase + u * 8192 + wave * 1024;
      glds16n(x0, px, d);
      if (wave < 2) glds16n(x1, px, d + 6144);
    };
    const unsigned dw = ldsbase + K1_W + wave * 1024;
    const unsigned v0 = wvoff(wave);
    glds16n(v0, W, dw);
    const unsigned v1 = wvoff(wave + 6);
    glds16n(v1, W, dw + 6144);
    const unsigned v2 = wvoff(wave < 3 ? wave + 12 : 14);
    if (wave < 3) glds16n(v2, W, dw + 12288);
    glds16n(v0, (const char*)W + incw, dw + WSLOT);
    glds16n(v1, (const char*)W + incw, dw + WSLOT + 6144);
    if (wave < 3) glds16n(v2, (const char*)W + incw, dw + WSLOT + 12288);
    xgrp(2, t1);
    K1C S;
    S.lds = lds;
    S.xa = (mh * 64 + m) * 128 + ((g ^ (m >> 1)) << 4);
#pragma unroll
    for (int c = 0; c < 5; ++c) S.wa[c] = wa0 + c * cstr;
    S.wr = 0;
    DG(S.t1 = 0;)
    f32x4 cs[3], s1[4], s2[4];
#pragma unroll
    for (int i = 0; i < 4; ++i) {
      s1[i] = f32x4{0.f, 0.f, 0.f, 0.f}; s2[i] = s1[i]; cs[i % 3] = s1[i];
#pragma unroll
      for (int c = 0; c < 5; ++c) acc[i][c] = s1[i];
    }
    if (wave == 4 && lane < 48) *(LDSP u32x4*)(lds + K1_ZERO + lane * 16) = u32x4{0u, 0u, 0u, 0u};
    const bool five = atype || (grp < 6 && nj < 2);
    if (wave < 2) WAIT_VM(5); else if (wave == 2) WAIT_VM(4); else WAIT_VM(3);
    mb_arrive(lds, 0);
    {
      const int col = c0 + sub * (lane / 10) + 8 * (lane % 10);
      const unsigned vo = atype ? tid * 16 : (col > 1720 ? 1720 : col) * 2;
      if (tid < (atype ? 200 : 30)) glds16n(vo, atype ? (const char*)wphi + grp * 3200 : (const char*)b1, ldsbase + K1_WPHI + wave * 1024);
    }
    if (tid == 320) {
      const u32x2 e = __builtin_amdgcn_raw_buffer_load_b64(rs, OFF_SYNC, 0, 16);
      const unsigned long long t = (((unsigned long long)e[1] << 32) | e[0]) + 1ull;
      *(LDSP u32x2*)(lds + F_TAG) = u32x2{(unsigned)t, (unsigned)(t >> 32)};
    }
    mb_wait(lds, 0);
    DG(T[1] = stamp();)
    if (nj == 1) {
      if (five) { if (mh == 0) c_main<5, 1>(S, acc, cs, s1, s2); else c_main<5, 2>(S, acc, cs, s1, s2); }
      else { if (mh == 0) c_main<4, 1>(S, acc, cs, s1, s2); else c_main<4, 2>(S, acc, cs, s1, s2); }
      const int cb = mh == 0 ? 0 : (five ? 3 : 2), cn = mh == 0 ? (five ? 3 : 2) : 2;
      if (m == 0) {
#pragma unroll
        for (int c = 0; c < 3; ++c)
          if (c < cn) *(LDSP f32x4*)(lds + K1_CS + (80 + (cb + c) * 16 + 4 * g) * 4) = cs[c];
      }
      if (g == q4) {
#pragma unroll
        for (int r = 0; r < 4; ++r) {
          const float sq = (m & 2) ? ((m & 1) ? s2[r][3] : s2[r][2]) : ((m & 1) ? s2[r][1] : s2[r][0]);
          const float mean = s1[r][0] * (1.0f / 640.0f);
          const float var = fmaxf(sq * (1.0f / 640.0f) - mean * mean, 0.f);
          float rsd = __builtin_amdgcn_rsqf(var + 1e-5f);
          if (!atype) rsd *= L2E;
          *(LDSP f32x2*)(lds + K1_STAT + (mh * 64 + r * 16 + m) * 8) = f32x2{rsd, -mean * rsd};
        }
      }
      if (atype) {
#pragma unroll
        for (int r = 2; r < 4; ++r)
#pragma unroll
          for (int c = 0; c < 5; ++c) *(LDSP f32x4*)(lds + K1_HO + (mh * 2 + r - 2) * 5120 + c * 1024 + lane * 16) = acc[r][c];
      }
    } else {
      if (five) c_main<5, 0>(S, acc, cs, s1, s2); else c_main<4, 0>(S, acc, cs, s1, s2);
      if (!atype) {
#pragma unroll
        for (int c = 0; c < 5; ++c) *(LDSP f32x4*)(lds + K1_HO + (mh * 2 + (nj >> 1)) * 5120 + c * 1024 + lane * 16) = acc[3][c];
        if (nj == 0 && tid < 120) {
          const unsigned w = *(LDSP unsigned*)(lds + K1_WPHI + tid * 4);
          *(LDSP f32x2*)(lds + K1_BL + tid * 8) = f32x2{bflo(w) * L2E, bfhi(w) * L2E};
        }
      }
    }
    DG(T[6] = S.t1;)
  } else {
    __builtin_amdgcn_s_setprio(3);
    K1H S;
    S.lds = lds; S.ldsbase = ldsbase;
    S.incw = incw;
    S.pw = (const char*)W + 2 * t1 * incw;
    S.wrapw = (unsigned)(size_t)W + 20 * incw;
    S.px = xrow + t1 * 128 + 64 * 1280;
    S.wrapx = (unsigned)(size_t)xrow + 1280;
    S.dw = 2 * WSLOT; S.dx = 3 * 8192; S.wr = 0;
    DG(S.t1 = 0;)
#pragma unroll
    for (int i = 0; i < 4; ++i) {
      S.vx[i] = xvoff(4 * mh + i);
      glds16n(S.vx[i], S.px, ldsbase + mh * 4096 + i * 1024 + 3 * 8192);
    }
#pragma unroll
    for (int i = 0; i < 8; ++i) {
      S.vw[i] = wvoff(8 * mh + i < 15 ? 8 * mh + i : 14);
      if (8 * mh + i < 15) glds16n(S.vw[i], S.pw, ldsbase + K1_W + mh * 8192 + i * 1024 + 2 * WSLOT);
    }
    S.touch = lane < 12 ? ws + (size_t)lane * (2u << 20) : lane == 12 ? (const char*)w2 : lane == 13 ? (const char*)w2 + (2u << 20) : lane == 14 ? (const char*)wo : (const char*)wphi;
#pragma unroll
    for (int c = 0; c < 5; ++c) { S.wa[c] = wa0 + c * cstr; acc[0][c] = f32x4{0.f, 0.f, 0.f, 0.f}; }
    S.rs = rs; S.l16 = lane * 16; S.w2 = (const char*)w2; S.wo = (const char*)wo;
    S.rrd = K1_RT + mh * 5120 + (lane >> 2) * 32 + (lane & 3) * 8;
    S.gi = b * 12 + mh * 6;
    S.rv2 = (lane >> 1) * 1280 + (lane & 1) * 16;
    S.rvo = ((((lane >> 1) & 12) << 1) | (((lane >> 1) & 16) >> 2) | ((lane >> 1) & 3)) * 1280 + (lane & 1) * 16;
    S.pc2 = jx < 2 ? (const char*)(kpc & ~(unsigned long long)127) : nullptr;
    if (mh == 0) h_main<0>(S, acc[0]); else h_main<1>(S, acc[0]);
    DG(T[6] = S.t1;)
    if (m == 0) {
#pragma unroll
      for (int c = 0; c < 5; ++c) *(LDSP f32x4*)(lds + K1_CS + (cj * 80 + c * 16 + 4 * g) * 4) = acc[0][c];
    }
  }
  DG(T[2] = stamp();)
  WAIT_LGKM0();
  __builtin_amdgcn_s_barrier();
  asm volatile("" ::: "memory");
  DG(T[3] = stamp();)
#define DG_OUT1() DG({ T[5] = stamp(); unsigned long long re = rstamp(); if (lane == 0) { unsigned long long* dp = (unsigned long long*)(ws + OFF_DBG) + (size_t)(b * 8 + wave) * 16; for (int i = 0; i < 8; ++i) dp[i] = T[i]; dp[8] = re; } if (lane < 20) ((unsigned*)(ws + OFF_DBG + (2u << 20)))[(b * 8 + wave) * 32 + lane] = *(LDSP unsigned*)(lds + K1_LDS + wave * 128 + lane * 4) - (unsigned)T[0]; })

  if (!atype) {
    auto round = [&](const f32x4 (&a)[5], int ej, int rt) {
      const bool five = grp < 6 && ej < 2;
      const f32x2 st = *(LDSP f32x2*)(lds + K1_STAT + (rt * 16 + m) * 8);
      u32x2 pk[5];
#pragma unroll
      for (int c = 0; c < 5; ++c) {
        if (c == 4 && !five) { pk[4] = u32x2{0u, 0u}; break; }
        const f32x4 cs = *(LDSP f32x4*)(lds + K1_CS + (ej * 80 + c * 16 + 4 * g) * 4);
        const f32x4 bl = *(LDSP f32x4*)(lds + K1_BL + (ej * 80 + c * 16 + 4 * g) * 4);
        float res[4];
#pragma unroll
        for (int e = 0; e < 4; ++e) {
          const float hl = fmaf(a[c][e], st[0], fmaf(st[1], cs[e], bl[e]));
          const float ex = __builtin_amdgcn_exp2f(hl);
          const float rc = __builtin_amdgcn_rcpf(fmaf(ex, ex + 2.f, 2.f));
          res[e] = hl * fmaf(rc, -2.f * LN2, LN2);
        }
        pk[c] = u32x2{pk2(res[0], res[1]), pk2(res[2], res[3])};
      }
      const int T0 = (c0 + sub * ej) >> 4;
      const unsigned base = OFF_ACT + ((rb * 8 + rt) * 54 + (T0 >> 1)) * 1024;
      if (!(T0 & 1)) {
        st16(rs, u32x4{pk[0][0], pk[0][1], pk[1][0], pk[1][1]}, lane * 16, base);
        st16(rs, u32x4{pk[2][0], pk[2][1], pk[3][0], pk[3][1]}, lane * 16, base + 1024);
        if (five) st8(rs, pk[4], lane * 16, base + 2048);
      } else {
        st8(rs, pk[0], lane * 16, base + 8);
        st16(rs, u32x4{pk[1][0], pk[1][1], pk[2][0], pk[2][1]}, lane * 16, base + 1024);
        st16(rs, u32x4{pk[3][0], pk[3][1], pk[4][0], pk[4][1]}, lane * 16, base + 2048);
      }
    };
    if (helper) {
#pragma unroll
      for (int r = 0; r < 2; ++r)
#pragma unroll
        for (int c = 0; c < 5; ++c) acc[r][c] = *(LDSP f32x4*)(lds + K1_HO + (mh * 2 + r) * 5120 + c * 1024 + lane * 16);
    }
#pragma unroll 1
    for (int it = 0; it < (helper ? 1 : 2); ++it) {
      round(acc[0], helper ? 0 : nj, mh * 4 + (helper ? 3 : 2 * it));
      if (it == 0 || nj == 1) round(acc[1], helper ? 2 : nj, mh * 4 + (helper ? 3 : 2 * it + 1));
#pragma unroll
      for (int c = 0; c < 5; ++c) { acc[0][c] = acc[2][c]; acc[1][c] = acc[3][c]; }
    }
    DG(T[4] = stamp();)
    DG_OUT1();
    return;
  }

  auto normalise = [&](const f32x4 (&a)[5], const f32x4 (&cs)[5], int rt, u32x4 (&bq)[3], unsigned one) {
    const f32x2 st = *(LDSP f32x2*)(lds + K1_STAT + (rt * 16 + m) * 8);
#pragma unroll
    for (int c = 0; c < 5; ++c) {
      float v[4];
#pragma unroll
      for (int e = 0; e < 4; ++e) v[e] = fmaf(a[c][e], st[0], st[1] * cs[c][e]);
      bq[c >> 1][(c & 1) * 2] = pk2(v[0], v[1]);
      bq[c >> 1][(c & 1) * 2 + 1] = pk2(v[2], v[3]);
    }
    bq[2][2] = one;
    bq[2][3] = 0u;
  };
  f32x4 csv[5];
  if (!helper && nj != 1) {
#pragma unroll
    for (int c = 0; c < 5; ++c) csv[c] = *(LDSP f32x4*)(lds + K1_CS + (nj * 80 + c * 16 + 4 * g) * 4);
#pragma unroll
    for (int r = 0; r < 4; ++r) {
      u32x4 bq[3];
      normalise(acc[r], csv, mh * 4 + r, bq, nj == 2 ? 0x00003f80u : 0u);
      if (nj == 0) {
        const unsigned so = OFF_QT + (4 * grp * 128 + rb * 8 + mh * 4 + r) * 768;
#pragma unroll
        for (int i = 0; i < 3; ++i) st16(rs, bq[i], g * (128 * 768) + m * 16, so + i * 256);
      } else {
#pragma unroll
        for (int i = 0; i < 3; ++i) *(LDSP u32x4*)(lds + K1_VIMG + (mh * 64 + r * 16 + m) * V_PITCH + g * 48 + i * 16) = bq[i];
      }
    }
  }
  if (helper || nj != 2) {
    const int kr0 = helper ? 3 : (nj == 1 ? 0 : 2);
    if (kr0) {
#pragma unroll
      for (int c = 0; c < 5; ++c) acc[0][c] = *(LDSP f32x4*)(lds + K1_HO + (mh * 2 + kr0 - 2) * 5120 + c * 1024 + lane * 16);
    }
#pragma unroll
    for (int c = 0; c < 5; ++c) csv[c] = *(LDSP f32x4*)(lds + K1_CS + (80 + c * 16 + 4 * g) * 4);
    LDSP char* wl = lds + ((p4 == g) ? K1_WPHI + g * 800 + q4 * 40 : K1_ZERO);
#pragma unroll 1
    for (int kr = kr0; kr < (kr0 ? kr0 + 1 : 2); ++kr) {
      u32x4 bq[3];
      normalise(acc[0], csv, mh * 4 + kr, bq, 0u);
      f32x4 pk[5];
      head_proj(wl, bq, pk);
      unsigned ph[20];
      feature_map(pk, ph);
      LDSP char* dst = lds + (mh * 64 + kr * 16 + m) * PK_PITCH + g * 80;
#pragma unroll
      for (int i = 0; i < 5; ++i) *(LDSP u32x4*)(dst + i * 16) = u32x4{ph[4 * i], ph[4 * i + 1], ph[4 * i + 2], ph[4 * i + 3]};
#pragma unroll
      for (int c = 0; c < 5; ++c) acc[0][c] = acc[1][c];
    }
  }
  DG(T[4] = stamp();)
  WAIT_LGKM0();
  __builtin_amdgcn_s_barrier();
  asm volatile("" ::: "memory");
  {
#pragma unroll 1
    for (int job = wave; job < 12; job += 8) {
      const int hh = job & 3, ft = job >> 2;
      LDSP char* pa = lds + (4 * g + q4) * PK_PITCH + hh * 80 + ft * 32 + p4 * 8;
      LDSP char* pb = lds + K1_VIMG + (4 * g + q4) * V_PITCH + hh * 48 + p4 * 8;
      f32x4 d0 = {0.f, 0.f, 0.f, 0.f}, d1 = {0.f, 0.f, 0.f, 0.f};
#pragma unroll
      for (int kk = 0; kk < 4; ++kk) {
        u32x4 a = trread2(pa + (32 * kk) * PK_PITCH, pa + (32 * kk + 16) * PK_PITCH);
        u32x4 v0 = trread2(pb + (32 * kk) * V_PITCH, pb + (32 * kk + 16) * V_PITCH);
        u32x4 v1 = trread2(pb + (32 * kk) * V_PITCH + 32, pb + (32 * kk + 16) * V_PITCH + 32);
        d0 = mfma(a, v0, d0);
        d1 = mfma(a, v1, d1);
      }
      const unsigned so = OFF_KVP + ((4 * grp + hh) * 16 + rb) * KVP_B + 1344 * ft;
      st16(rs, __builtin_bit_cast(u32x4, d0), m * 64 + g * 16, so);
      if (m < 5) st16(rs, __builtin_bit_cast(u32x4, d1), (16 + m) * 64 + g * 16, so);
    }
  }
  DG_OUT1();
}

constexpr unsigned K2_YD = 0;
constexpr unsigned K2_SD = 4 * 15360;
constexpr unsigned K2_KVT = K2_SD + 4 * 20480;
constexpr unsigned K2_ZERO = K2_KVT + 1920;
constexpr unsigned K2_WPHI = K2_ZERO + 1792;
constexpr unsigned K2_FLAG = K2_WPHI + 3200;
constexpr unsigned K2_OWN = K2_FLAG + 16;
constexpr unsigned K2_LDS = K2_OWN + 8192;

DEVI u32x4 bld(__amdgpu_buffer_rsrc_t rs, unsigned voff, unsigned soff) {
  return __builtin_amdgcn_raw_buffer_load_b128(rs, voff, soff, 16);
}
DEVI u32x4 bld_sc1(__amdgpu_buffer_rsrc_t rs, unsigned voff, unsigned soff) {
  return __builtin_amdgcn_raw_buffer_load_b128(rs, voff, soff, 16);
}
template <int N, class F>
DEVI void y_stream(__amdgpu_buffer_rsrc_t rs, unsigned act, unsigned wt, int rot, bool more, f32x4 (&acc)[4][5], F ready) {
  const unsigned l16 = (threadIdx.x & 63) * 16;
  unsigned ab[4];
#pragma unroll
  for (int l = 0; l < 4; ++l) ab[l] = act + ((l + rot) & 3) * (54 * 1024);
  u32x4 A[3][4], B[3][5];
  auto loada = [&](int i) {
#pragma unroll
    for (int l = 0; l < 4; ++l) A[i % 3][l] = bld(rs, l16, ab[l] + i * 1024);
  };
  auto loadb = [&](int i) {
#pragma unroll
    for (int c = 0; c < 5; ++c) B[i % 3][c] = bld(rs, l16, wt + c * (74 * 1024) + i * 1024);
  };
  auto comp = [&](int i) {
    __builtin_amdgcn_sched_barrier(0);
#pragma unroll
    for (int l = 0; l < 4; ++l)
#pragma unroll
      for (int c = 0; c < 5; ++c) acc[l][c] = mfma(B[i % 3][c], A[i % 3][l], acc[l][c]);
    __builtin_amdgcn_sched_barrier(0);
  };
  loadb(0);
  loadb(1);
  loadb(2);
  ready();
  loada(0);
  loada(1);
#pragma unroll
  for (int i = 0; i < N; ++i) {
    if (i + 2 < N) loada(i + 2);
    else if (i + 2 == N) { if (more) loada(N); }
    comp(i);
    if (i + 3 < N) loadb(i + 3);
    else if (i + 3 == N) { if (more) loadb(N); }
  }
  if (more) comp(N);
}

template <int C0, int C1>
DEVI void combine(LDSP char* lds, const u32x2 (&xv)[3], u16* out, int rt, int rb2, int cb, const f32x4 (&own)[5]) {
  const int lane = threadIdx.x & 63;
  const unsigned l16 = lane * 16;
  const size_t orow = (size_t)(64 * rb2 + 16 * rt + (lane & 15)) * 640 + 80 * cb + 4 * (lane >> 4);
  WAIT_LGKM0();
  __builtin_amdgcn_s_barrier();
  asm volatile("" ::: "memory");
#pragma unroll
  for (int c = C0; c < C1; ++c) {
    f32x4 y = C0 == 0 ? own[c] : *(LDSP f32x4*)(lds + K2_OWN + (rt * 2 + c - 3) * 1024 + l16);
#pragma unroll
    for (int l = 1; l < 4; ++l)
      y += *(LDSP f32x4*)(lds + K2_YD + ((rt - l) & 3) * 15360 + ((l - 1) * 5 + c) * 1024 + l16);
    LDSP f32x4* sp = (LDSP f32x4*)(lds + K2_SD + (rt * 5 + c) * 1024 + l16);
    const f32x4 s = (sp[0] + sp[1280]) + (sp[2560] + sp[3840]);
    float o[4];
#pragma unroll
    for (int e = 0; e < 4; ++e) {
      const float gate = y[e] * __builtin_amdgcn_rcpf(1.f + __builtin_amdgcn_exp2f(-y[e] * L2E));
      const unsigned xw = xv[c - C0][e >> 1];
      o[e] = fmaf(s[e], gate, (e & 1) ? bfhi(xw) : bflo(xw));
    }
    *(u32x2*)(out + orow + 16 * c) = u32x2{pk2(o[0], o[1]), pk2(o[2], o[3])};
  }
}

template <int NW>
DEVI bool poll_tags(__amdgpu_buffer_rsrc_t rs, unsigned off, int cnt, u32x2 tag) {
  const int lane = threadIdx.x & 63;
  const unsigned vo = (lane < cnt ? lane : 0) * 64;
  for (int spin = 0; spin < (1 << 20); ++spin) {
    asm volatile("" ::: "memory");
    bool ok = true;
#pragma unroll
    for (int i = 0; i < NW; ++i) {
      const u32x2 w = __builtin_amdgcn_raw_buffer_load_b64(rs, vo, off + i * 4096, 16);
      ok = ok && w[0] == tag[0] && w[1] == tag[1];
    }
    if (__all(ok)) { asm volatile("" ::: "memory"); return true; }
    __builtin_amdgcn_s_sleep(4);
  }
  return false;
}

DEVI void go_set(LDSP char* lds, int i) {
  if ((threadIdx.x & 63) == 0) *(volatile LDSP unsigned*)(lds + F_TAG + 8 + 4 * i) = 1u;
}
DEVI void go_wait(LDSP char* lds, int i) {
  for (int spin = 0; spin < (1 << 24); ++spin) {
    if (__builtin_amdgcn_readfirstlane(*(volatile LDSP unsigned*)(lds + F_TAG + 8 + 4 * i))) break;
    __builtin_amdgcn_s_sleep(2);
  }
  asm volatile("" ::: "memory");
}

DEVI void phase2(LDSP char* lds, u32x2 tag, bool seen, const u16* __restrict__ x, const u16* __restrict__ wphi, char* __restrict__ ws,
                 u16* __restrict__ out) {
  const int tid = threadIdx.x, lane = tid & 63;
  const int wave = __builtin_amdgcn_readfirstlane(tid >> 6);
  const int g = lane >> 4, m = lane & 15, q4 = m >> 2, p4 = lane & 3;
  const int b = blockIdx.x, xcd = b & 7, jx = b >> 3;
  const int rb2 = 8 * (xcd >> 1) + (jx & 7), cb = 4 * (xcd & 1) + (jx >> 3);
  const __amdgpu_buffer_rsrc_t rs = __builtin_amdgcn_make_buffer_rsrc((void*)ws, 0, WS_END, 0x00020000);
  const unsigned l16 = lane * 16;
  DG(unsigned long long T[8]; T[0] = stamp(); T[7] = rstamp(); T[1] = T[2] = T[3] = T[4] = T[5] = T[6] = 0;)
#define DG_OUT2() DG({ unsigned long long re = rstamp(); if (lane == 0) { unsigned long long* dp = (unsigned long long*)(ws + OFF_DBG + (1u << 20)) + (size_t)(b * 8 + wave) * 16; for (int i = 0; i < 8; ++i) dp[i] = T[i]; dp[8] = re; } })
  asm volatile("" ::"s"(x), "s"(wphi), "s"(out));
  if (wave == 4) {
    *(LDSP u32x4*)(lds + K2_ZERO + l16) = u32x4{0u, 0u, 0u, 0u};
    if (lane < 48) *(LDSP u32x4*)(lds + K2_ZERO + 1024 + l16) = u32x4{0u, 0u, 0u, 0u};
    if (lane == 0) *(LDSP u32x4*)(lds + K2_FLAG) = u32x4{0u, 0u, 0u, 0u};
  }
  const unsigned act = OFF_ACT + 4 * rb2 * (54 * 1024), wt = OFF_WT + 5 * cb * (74 * 1024);
  const u16* xt = x + (size_t)(64 * rb2 + 16 * (wave & 3) + m) * 640 + 80 * cb + 4 * g;
  f32x4 acc[4][5];
#pragma unroll
  for (int r = 0; r < 4; ++r)
#pragma unroll
    for (int c = 0; c < 5; ++c) acc[r][c] = f32x4{0.f, 0.f, 0.f, 0.f};

  if (wave < 4) {
    u32x2 xv[3];
#pragma unroll
    for (int c = 0; c < 3; ++c) xv[c] = *(const u32x2*)(xt + 16 * c);
    const unsigned k0 = (wave < 2 ? 14 * wave : 2 + 13 * wave) * 1024;
    y_stream<13>(rs, act + k0, wt + k0, wave, wave < 2, acc, [&]() {
      if (wave == 0 && !seen) {
        poll_tags<1>(rs, OFF_FL1 + (128 + (rb2 >> 1) * 8) * 64, 8, tag);
        go_set(lds, 0);
      } else {
        go_wait(lds, 0);
      }
      DG(T[1] = stamp();)
    });
    DG(T[3] = stamp();)
#pragma unroll
    for (int l = 1; l < 4; ++l)
#pragma unroll
      for (int c = 0; c < 5; ++c)
        *(LDSP f32x4*)(lds + K2_YD + wave * 15360 + ((l - 1) * 5 + c) * 1024 + l16) = acc[l][c];
#pragma unroll
    for (int c = 3; c < 5; ++c) *(LDSP f32x4*)(lds + K2_OWN + (wave * 2 + c - 3) * 1024 + l16) = acc[0][c];
    DG(T[4] = stamp();)
    combine<0, 3>(lds, xv, out, wave, rb2, cb, acc[0]);
    if (b == 0 && wave == 0 && poll_tags<4>(rs, OFF_FLR, 64, tag) && lane == 0) __builtin_amdgcn_raw_buffer_store_b64(tag, rs, OFF_SYNC, 0, 16);
    DG(T[6] = stamp();)
    DG_OUT2();
    return;
  }

  const int aw = wave - 4;
  const int h = 4 * xcd + (jx & 3), sb = jx >> 2;
  const int tok = sb * 256 + aw * 64 + lane;
  __builtin_amdgcn_s_setprio(2);
  u32x4 wv = *(const u32x4*)(wphi + h * 400 + (lane < 50 ? lane : 49) * 8);
  u32x4 B[5][5];
#pragma unroll
  for (int i = 0; i < K2_BPRE; ++i)
#pragma unroll
    for (int c = 0; c < 5; ++c) B[i][c] = bld(rs, l16, wt + c * (74 * 1024) + (54 + 5 * aw + i) * 1024);
  if (aw == 0) {
    poll_tags<1>(rs, OFF_FL1 + (h >> 2) * 16 * 64, 16, tag);
    WAIT_LGKM0();
    go_set(lds, 1);
  } else {
    go_wait(lds, 1);
  }
  u32x4 bq[3];
#pragma unroll
  for (int s = 0; s < 3; ++s) bq[s] = bld(rs, (unsigned)(tok >> 4) * 768 + m * 16, OFF_QT + h * (128 * 768) + s * 256);
  int item = aw * 53 + (lane < 53 ? lane : 52);
  item = item < 210 ? item : 209;
  const int d = item / 10, fq = item - 10 * d;
  u32x4 part[16];
#pragma unroll
  for (int r = 0; r < 16; ++r) part[r] = bld(rs, (unsigned)((fq >> 2) * 1344 + d * 64 + (fq & 3) * 16), OFF_KVP + (h * 16 + r) * KVP_B);
  *(LDSP u32x4*)(lds + K2_WPHI + aw * 800 + (lane < 50 ? lane : 49) * 16) = wv;
  unsigned ph[20];
  {
    LDSP char* wl = lds + ((p4 == g) ? K2_WPHI + aw * 800 + q4 * 40 : K2_ZERO);
    f32x4 pq[5];
    head_proj(wl, bq, pq);
    feature_map(pq, ph);
#pragma unroll
    for (int i = 0; i < 20; ++i) asm volatile("" : "+v"(ph[i]));
  }
  DG(T[1] = stamp();)
  {
    f32x4 sum = __builtin_bit_cast(f32x4, part[0]);
#pragma unroll
    for (int r = 1; r < 16; ++r) sum += __builtin_bit_cast(f32x4, part[r]);
    *(LDSP u32x2*)(lds + K2_KVT + d * 80 + fq * 8) = u32x2{pk2(sum[0], sum[1]), pk2(sum[2], sum[3])};
    WAIT_LGKM0();
    if (lane == 0) *(volatile LDSP unsigned*)(lds + K2_FLAG + aw * 4) = 1u;
    for (int spin = 0; spin < (1 << 22); ++spin) {
      const u32x4 f = *(volatile LDSP u32x4*)(lds + K2_FLAG);
      if (__builtin_amdgcn_readfirstlane(f[0] & f[1] & f[2] & f[3])) break;
      __builtin_amdgcn_s_sleep(1);
    }
    asm volatile("" ::: "memory");
  }
  DG(T[2] = stamp();)
  {
    LDSP char* kl = lds + (((m >> 2) == g) ? K2_KVT + (m & 3) * 80 : K2_ZERO);
    f32x4 nd[6];
#pragma unroll
    for (int t = 0; t < 6; ++t) {
      f32x4 c = {0.f, 0.f, 0.f, 0.f};
#pragma unroll
      for (int s = 0; s < 5; ++s)
        c = mfma(*(LDSP u32x4*)(kl + t * 320 + s * 16), u32x4{ph[4 * s], ph[4 * s + 1], ph[4 * s + 2], ph[4 * s + 3]}, c);
      nd[t] = c;
    }
    const float rd = __builtin_amdgcn_rcpf(nd[5][0] + 1e-6f);
    unsigned ow[10];
#pragma unroll
    for (int t = 0; t < 5; ++t) {
      ow[2 * t] = pk2(nd[t][0] * rd, nd[t][1] * rd);
      ow[2 * t + 1] = pk2(nd[t][2] * rd, nd[t][3] * rd);
    }
    const int e0 = (5 * h + 1) >> 1, e8 = (h & 1) ? e0 - 1 : e0 + 2;
    const unsigned vo = (unsigned)(tok >> 4) * 20480 + m * 16;
    const bool odd = h & 1;
    st16(rs, odd ? u32x4{ow[2], ow[3], ow[4], ow[5]} : u32x4{ow[0], ow[1], ow[2], ow[3]}, vo, OFF_OT + e0 * 256);
    st16(rs, odd ? u32x4{ow[6], ow[7], ow[8], ow[9]} : u32x4{ow[4], ow[5], ow[6], ow[7]}, vo, OFF_OT + (e0 + 1) * 256);
    st8(rs, odd ? u32x2{ow[0], ow[1]} : u32x2{ow[8], ow[9]}, vo, OFF_OT + e8 * 256 + (h & 1) * 8);
    WAIT_VM(0);
    if (lane == 0) __builtin_amdgcn_raw_buffer_store_b64(tag, rs, OFF_FO + ((sb * 4 + aw) * 32 + h) * 64, 0, 16);
  }
  DG(T[3] = stamp();)
  const unsigned ring = K2_SD + aw * 20480;
  u32x2 xv[3];
#pragma unroll
  for (int c = 0; c < 2; ++c) xv[c] = *(const u32x2*)(xt + 48 + 16 * c);
  xv[2] = xv[1];
  {
    u32x4 A[5][4];
    auto loadb = [&](int i) {
#pragma unroll
      for (int c = 0; c < 5; ++c) B[i][c] = bld(rs, l16, wt + c * (74 * 1024) + (54 + 5 * aw + i) * 1024);
    };
#pragma unroll
    for (int i = K2_BPRE; i < 5; ++i) loadb(i);
    const unsigned ob = OFF_OT + (4 * rb2 * 20 + 5 * aw) * 1024;
    poll_tags<1>(rs, OFF_FO + (rb2 * 32 + 8 * aw) * 64, 8, tag);
    DG(T[4] = stamp();)
#pragma unroll
    for (int i = 0; i < 5; ++i)
#pragma unroll
      for (int l = 0; l < 4; ++l) A[i][l] = bld(rs, l16, ob + l * 20480 + i * 1024);
#pragma unroll
    for (int i = 0; i < 5; ++i) {
      __builtin_amdgcn_sched_barrier(0);
#pragma unroll
      for (int l = 0; l < 4; ++l)
#pragma unroll
        for (int c = 0; c < 5; ++c) acc[l][c] = mfma(B[i][c], A[i][l], acc[l][c]);
      __builtin_amdgcn_sched_barrier(0);
    }
  }
#pragma unroll
  for (int l = 0; l < 4; ++l)
#pragma unroll
    for (int c = 0; c < 5; ++c) *(LDSP f32x4*)(lds + ring + (l * 5 + c) * 1024 + l16) = acc[l][c];
  DG(T[6] = stamp();)
  combine<3, 5>(lds, xv, out, aw, rb2, cb, acc[0]);
  DG_OUT2();
}

static_assert(K1_LDS + 1024 <= F_TAG && K2_LDS <= F_TAG, "LDS map");

__global__ __launch_bounds__(512, 2) void hedgehog_fused(const u16* __restrict__ x, const u16* __restrict__ wqkv,
                                                         const u16* __restrict__ wphi, const u16* __restrict__ wo,
                                                         const u16* __restrict__ w1, const u16* __restrict__ b1,
                                                         const u16* __restrict__ w2, char* __restrict__ ws,
                                                         u16* __restrict__ out) {
  extern __shared__ __attribute__((aligned(16))) char smem[];
  LDSP char* lds = (LDSP char*)smem;
  phase1(lds, __builtin_amdgcn_s_getpc(), x, wqkv, wphi, wo, w1, b1, w2, ws);
  int tid = threadIdx.x;
  asm volatile("" : "+v"(tid));
  const __amdgpu_buffer_rsrc_t rs = __builtin_amdgcn_make_buffer_rsrc((void*)ws, 0, WS_END, 0x00020000);
  const u32x2 tag = *(LDSP u32x2*)(lds + F_TAG);
  bool seen = false;
  if (tid < 64) {
    const u32x2 a = __builtin_amdgcn_raw_buffer_load_b64(rs, (tid & 7) * 64, OFF_FL1 + (128 + (4 * ((blockIdx.x & 7) >> 1) + (((blockIdx.x >> 3) & 7) >> 1)) * 8) * 64, 16);
    poll_tags<4>(rs, OFF_FLR, 64, tag);
    seen = __all(a[0] == tag[0] && a[1] == tag[1]);
    asm volatile("" ::: "memory");
    if (seen) go_set(lds, 0);
  }
  WAIT_VM(0);
  WAIT_LGKM0();
  __builtin_amdgcn_s_barrier();
  asm volatile("" ::: "memory");
  if (tid == 0) {
    const int b = blockIdx.x, xcd = b & 7, jx = b >> 3;
    const int rb = 8 * (xcd >> 2) + (jx & 7), grp = 2 * (xcd & 3) + ((jx >> 3) & 1);
    __builtin_amdgcn_raw_buffer_store_b64(tag, rs, OFF_FL1 + (jx < 16 ? grp * 16 + rb : 128 + rb * 8 + grp) * 64, 0, 16);
  }
  phase2(lds, tag, seen, x, wphi, ws, out);
}

extern "C" void kernel_launch(void* const* d_in, const int* in_sizes, int n_in, void* d_out, int out_size, void* d_ws,
                              size_t ws_size, hipStream_t stream) {
  const u16* x = (const u16*)d_in[0];
  const u16* wqkv = (const u16*)d_in[1];
  const u16* wphi = (const u16*)d_in[2];
  const u16* wo = (const u16*)d_in[3];
  const u16* w1 = (const u16*)d_in[5];
  const u16* b1 = (const u16*)d_in[6];
  const u16* w2 = (const u16*)d_in[7];
  (void)hipFuncSetAttribute((const void*)hedgehog_fused, hipFuncAttributeMaxDynamicSharedMemorySize, F_LDS);
  hedgehog_fused<<<256, 512, F_LDS, stream>>>(x, wqkv, wphi, wo, w1, b1, w2, (char*)d_ws, (u16*)d_out);
}
```

```cpp
#include <hip/hip_runtime.h>
#include <stdint.h>

typedef unsigned short u16;
typedef __attribute__((ext_vector_type(2))) __bf16 bf16x2;
typedef __attribute__((ext_vector_type(8))) __bf16 bf16x8;
typedef __attribute__((ext_vector_type(4))) short s16x4;
typedef __attribute__((ext_vector_type(2))) float f32x2;
typedef __attribute__((ext_vector_type(4))) float f32x4;
typedef __attribute__((ext_vector_type(2))) unsigned u32x2;
typedef __attribute__((ext_vector_type(4))) unsigned u32x4;
#define LDSP __attribute__((address_space(3)))
#define DEVI __device__ __forceinline__

constexpr unsigned OFF_ACT = 0;
constexpr unsigned OFF_WT = OFF_ACT + 128u * 54u * 1024u;
constexpr unsigned OFF_OT = OFF_WT + 40u * 74u * 1024u;
constexpr unsigned OFF_QT = OFF_OT + 128u * 20u * 1024u;
constexpr unsigned OFF_KVP = OFF_QT + 32u * 2048u * 48u;
constexpr unsigned KVP_B = 3 * 1344;
constexpr unsigned OFF_SYNC = OFF_KVP + 32u * 16u * KVP_B;
constexpr unsigned OFF_FL1 = OFF_SYNC + 128;
constexpr unsigned OFF_FLR = OFF_FL1 + 256 * 64;
constexpr unsigned OFF_FO = OFF_FLR + 256 * 64;
constexpr unsigned WS_END = OFF_FO + 1024 * 64;

constexpr int CODE_BYTES = 48 * 1024;
constexpr int CODE2_OFF = 34 * 1024;
constexpr int K2_BPRE = 4;
constexpr float L2E = 1.4426950408889634f, LN2 = 0.6931471805599453f;

DEVI unsigned pk2(float a, float b) {
  bf16x2 v = {(__bf16)a, (__bf16)b};
  return __builtin_bit_cast(unsigned, v);
}
DEVI float bflo(unsigned u) { return __uint_as_float(u << 16); }
DEVI float bfhi(unsigned u) { return __uint_as_float(u & 0xffff0000u); }
DEVI f32x4 mfma(u32x4 a, u32x4 b, f32x4 c) {
  return __builtin_amdgcn_mfma_f32_16x16x32_bf16(__builtin_bit_cast(bf16x8, a), __builtin_bit_cast(bf16x8, b), c, 0, 0, 0);
}
DEVI u32x2 trread(LDSP char* p) {
  return __builtin_bit_cast(u32x2, __builtin_amdgcn_ds_read_tr16_b64_v4i16((LDSP s16x4*)p));
}
DEVI u32x4 trread2(LDSP char* p0, LDSP char* p1) {
  u32x2 a = trread(p0), b = trread(p1);
  return u32x4{a[0], a[1], b[0], b[1]};
}
#define WAIT_VM(n) asm volatile("s_waitcnt vmcnt(" #n ")" ::: "memory")
#define WAIT_LGKM0() asm volatile("s_waitcnt lgkmcnt(0)" ::: "memory")

DEVI void feature_map(const f32x4 (&p)[5], unsigned (&o)[20]) {
  float mx = p[0][0], mn = p[0][0];
#pragma unroll
  for (int t = 0; t < 5; ++t)
#pragma unroll
    for (int r = 0; r < 4; ++r) { mx = fmaxf(mx, p[t][r]); mn = fminf(mn, p[t][r]); }
  const float a = -mx * L2E, b = mn * L2E;
  float e1[20], e2[20], s1 = 0.f, s2 = 0.f;
#pragma unroll
  for (int t = 0; t < 5; ++t)
#pragma unroll
    for (int r = 0; r < 4; ++r) {
      e1[4 * t + r] = __builtin_amdgcn_exp2f(fmaf(p[t][r], L2E, a));
      e2[4 * t + r] = __builtin_amdgcn_exp2f(fmaf(p[t][r], -L2E, b));
      s1 += e1[4 * t + r]; s2 += e2[4 * t + r];
    }
  const float r1 = __builtin_amdgcn_rcpf(s1), r2 = __builtin_amdgcn_rcpf(s2);
#pragma unroll
  for (int i = 0; i < 10; ++i) {
    o[i] = pk2(e1[2 * i] * r1, e1[2 * i + 1] * r1);
    o[10 + i] = pk2(e2[2 * i] * r2, e2[2 * i + 1] * r2);
  }
}
DEVI void head_proj(LDSP char* wl, const u32x4 (&b)[3], f32x4 (&p)[5]) {
#pragma unroll
  for (int t = 0; t < 5; ++t) {
    u32x4 a0 = trread2(wl + 0 * 40 + t * 8, wl + 4 * 40 + t * 8);
    u32x4 a1 = trread2(wl + 8 * 40 + t * 8, wl + 12 * 40 + t * 8);
    u32x2 h2 = trread(wl + 16 * 40 + t * 8);
    u32x4 a2 = {h2[0], h2[1], 0u, 0u};
    f32x4 c = {0.f, 0.f, 0.f, 0.f};
    c = mfma(a0, b[0], c);
    c = mfma(a1, b[1], c);
    c = mfma(a2, b[2], c);
    p[t] = c;
  }
}

constexpr unsigned XSLOT = 16384, WSUB = 5120, WSLOT = 3 * WSUB;
constexpr unsigned K1_W = 4 * XSLOT;
constexpr unsigned K1_WPHI = K1_W + 5 * WSLOT;
constexpr unsigned K1_ZERO = K1_WPHI + 3200;
constexpr unsigned K1_STAT = K1_ZERO + 768;
constexpr unsigned K1_CS = K1_STAT + 1024;
constexpr unsigned K1_BL = K1_CS + 960;
constexpr unsigned K1_SCR = K1_BL + 960;
constexpr unsigned K1_MB = K1_SCR + 256;
constexpr unsigned K1_RT = K1_MB + 16;
constexpr unsigned K1_LDS = K1_RT + 10240;
constexpr unsigned F_TAG = 160832;
constexpr unsigned F_LDS = F_TAG + 16;
constexpr unsigned PK_PITCH = 352, V_PITCH = 224, K1_VIMG = 128 * PK_PITCH;
constexpr unsigned K1_HO = K1_VIMG + 128 * V_PITCH;

DEVI void glds16n(unsigned voff, const void* sbase, unsigned lds_dst) {
  asm volatile("s_mov_b32 m0, %2\n\ts_nop 0\n\tglobal_load_lds_dwordx4 %0, %1" ::"v"(voff), "s"(sbase), "s"(lds_dst) : "memory");
}
DEVI void st16(__amdgpu_buffer_rsrc_t rs, u32x4 v, unsigned voff, unsigned soff) {
  __builtin_amdgcn_raw_buffer_store_b128(v, rs, voff + soff, 0, 16);
}
DEVI void st8(__amdgpu_buffer_rsrc_t rs, u32x2 v, unsigned voff, unsigned soff) {
  __builtin_amdgcn_raw_buffer_store_b64(v, rs, voff + soff, 0, 16);
}
#define STEP_SYNC() do { __builtin_amdgcn_s_waitcnt(0xC07F); __builtin_amdgcn_s_barrier(); __builtin_amdgcn_sched_barrier(0); } while (0)

DEVI void mb_arrive(LDSP char* lds, int i) {
  WAIT_LGKM0();
  if ((threadIdx.x & 63) == 0) __hip_atomic_fetch_add((LDSP unsigned*)(lds + K1_MB + 4 * i), 1u, __ATOMIC_RELAXED, __HIP_MEMORY_SCOPE_WORKGROUP);
}
DEVI void mb_wait(LDSP char* lds, int i) {
  for (int spin = 0; spin < (1 << 20); ++spin) {
    if (__builtin_amdgcn_readfirstlane(*(volatile LDSP unsigned*)(lds + K1_MB + 4 * i)) >= 6u) break;
    __builtin_amdgcn_s_sleep(1);
  }
  asm volatile("" ::: "memory");
  __builtin_amdgcn_sched_barrier(0);
}
struct K1C {
  LDSP char* lds;
  unsigned xa, wa[5], wr;
};
template <int NC>
DEVI void c_read(K1C& S, int s, u32x4 (&xf)[4], u32x4 (&wf)[5]) {
  LDSP char* px = S.lds + ((s >> 1) & 3) * XSLOT + (S.xa ^ ((s & 1) << 6));
  LDSP char* pw = S.lds + S.wr;
  xf[0] = *(LDSP u32x4*)(px);
  wf[0] = trread2(pw + S.wa[0], pw + S.wa[0] + 1280);
#pragma unroll
  for (int r = 1; r < 4; ++r) xf[r] = *(LDSP u32x4*)(px + r * 2048);
#pragma unroll
  for (int c = 1; c < NC; ++c) wf[c] = trread2(pw + S.wa[c], pw + S.wa[c] + 1280);
  S.wr = S.wr == 4 * WSLOT ? 0 : S.wr + WSLOT;
}
template <int NC, int ROLE, bool READ, bool MINI = false>
DEVI void c_step(K1C& S, int s, const u32x4 (&xc)[4], const u32x4 (&wc)[5], u32x4 (&xn)[4], u32x4 (&wn)[5], f32x4 (&acc)[4][5],
                 f32x4 (&cs)[3], f32x4 (&s1)[4], f32x4 (&s2)[4], const u32x4& ones) {
  if (READ) {
    if (MINI) { WAIT_VM(0); mb_arrive(S.lds, 1); mb_wait(S.lds, 1); } else STEP_SYNC();
    c_read<NC>(S, s + 1, xn, wn);
  }
#pragma unroll
  for (int c = 0; c < NC; ++c)
#pragma unroll
    for (int r = 0; r < 4; ++r) acc[r][c] = mfma(wc[c], xc[r], acc[r][c]);
  if (ROLE) {
    constexpr int C0 = ROLE == 1 ? 0 : NC - 2, C1 = ROLE == 1 ? NC - 2 : NC;
#pragma unroll
    for (int r = 0; r < 4; ++r) {
      s1[r] = mfma(ones, xc[r], s1[r]);
      s2[r] = mfma(xc[r], xc[r], s2[r]);
    }
#pragma unroll
    for (int c = C0; c < C1; ++c) cs[c - C0] = mfma(wc[c], ones, cs[c - C0]);
  }
  if (READ) {
#pragma unroll
    for (int i = 0; i < 4 + 2 * NC; ++i) {
      __builtin_amdgcn_sched_group_barrier(0x8, 1, 0);
      __builtin_amdgcn_sched_group_barrier(0x100, 1, 0);
    }
  }
  __builtin_amdgcn_sched_barrier(0);
}
template <int NC, int ROLE>
DEVI void c_main(K1C& S, f32x4 (&acc)[4][5], f32x4 (&cs)[3], f32x4 (&s1)[4], f32x4 (&s2)[4]) {
  u32x4 xa[4], wa[5], xb[4], wb[5];
  u32x4 ones = {0x3f803f80u, 0x3f803f80u, 0x3f803f80u, 0x3f803f80u};
  asm volatile("" : "+v"(ones));
  c_read<NC>(S, 0, xa, wa);
  c_step<NC, ROLE, true, true>(S, 0, xa, wa, xb, wb, acc, cs, s1, s2, ones);
#pragma unroll 1
  for (int s = 1; s < 19; s += 2) {
    c_step<NC, ROLE, true>(S, s, xb, wb, xa, wa, acc, cs, s1, s2, ones);
    c_step<NC, ROLE, true>(S, s + 1, xa, wa, xb, wb, acc, cs, s1, s2, ones);
  }
  c_step<NC, ROLE, false>(S, 19, xb, wb, xa, wa, acc, cs, s1, s2, ones);
}

struct K1H {
  LDSP char* lds;
  unsigned ldsbase;
  unsigned vw[8], vx[4];
  const char* pw; const char* px;
  unsigned incw, wrapw, wrapx;
  unsigned dw, dx;
  unsigned wa[5], wr;
  __amdgpu_buffer_rsrc_t rs;
  const char* w2; const char* wo;
  unsigned rv2, rvo, rrd, l16;
  int gi;
  const char* pc2;
  const char* touch;
};
template <int HP, int ODD, bool SKIP = false>
DEVI void h_issue(K1H& S) {
  if (!SKIP) {
#pragma unroll
    for (int i = 0; i < 8 - HP; ++i) glds16n(S.vw[i], S.pw, S.ldsbase + K1_W + HP * 8192 + i * 1024 + S.dw);
#pragma unroll
    for (int i = 0; i < 4; ++i) glds16n(S.vx[i], S.px, S.ldsbase + HP * 4096 + i * 1024 + S.dx);
  }
  S.pw += S.incw;
  S.pw -= (unsigned)(size_t)S.pw == S.wrapw ? 18 * S.incw : 0u;
  S.px += ODD ? 128 - 64 * 1280 : 64 * 1280;
  S.px -= (unsigned)(size_t)S.px == S.wrapx ? 1152u : 0u;
  S.dw = S.dw == 4 * WSLOT ? 0 : S.dw + WSLOT;
  S.dx = (S.dx + 8192) & 0xffff;
}
template <int HP>
DEVI void h_retile(K1H& S, int s) {
  if (s >= 11) {
    const int i = s - 11, gi = S.gi + i, cq = (gi >> 2) / 74, ks = (gi >> 2) - 74 * cq;
    LDSP char* pr = S.lds + S.rrd + (i == 5 ? 0 : i) * 1024;
    st16(S.rs, trread2(pr, pr + 512), S.l16, gi < 2960 ? OFF_WT + ((4 * cq + (gi & 3)) * 74 + ks) * 1024 : WS_END);
  }
  if (s < 12) {
    const int i = s - 6, gi = S.gi + i, fid = gi < 2960 ? gi : 2959, cq = (fid >> 2) / 74, ks = (fid >> 2) - 74 * cq;
    const char* src = (ks < 54 ? S.w2 + ks * 40960 : S.wo + (ks - 54) * 40960) + (4 * cq + (fid & 3)) * 32;
    glds16n(ks < 54 ? S.rv2 : S.rvo, src, S.ldsbase + K1_RT + HP * 5120 + (i == 5 ? 0 : i) * 1024);
  }
  __builtin_amdgcn_sched_barrier(0);
}
template <int HP, int G> DEVI void h_wait() {
  constexpr int N = G * (12 - HP);
  asm volatile("s_waitcnt vmcnt(%0)" ::"i"(N) : "memory");
}
DEVI void h_read(K1H& S, u32x4 (&wf)[5]) {
  LDSP char* pw = S.lds + S.wr;
#pragma unroll
  for (int c = 0; c < 5; ++c) wf[c] = trread2(pw + S.wa[c], pw + S.wa[c] + 1280);
  S.wr = S.wr == 4 * WSLOT ? 0 : S.wr + WSLOT;
}
template <int HP, int G, int ISSUE>
DEVI void h_step(K1H& S, const u32x4 (&wc)[5], u32x4 (&wn)[5], f32x4 (&cs)[5], const u32x4& ones) {
  if (G >= 0 && G < 9) h_wait<HP, (G >= 0 && G < 9 ? G : 0)>();
  if (G >= 0) STEP_SYNC();
#pragma unroll
  for (int c = 0; c < 5; ++c) cs[c] = mfma(wc[c], ones, cs[c]);
  h_read(S, wn);
  __builtin_amdgcn_sched_barrier(0);
  if (ISSUE) h_issue<HP, ISSUE - 1>(S);
  __builtin_amdgcn_sched_barrier(0);
}
template <int HP>
DEVI void h_main(K1H& S, f32x4 (&cs)[5]) {
  u32x4 wa[5], wb[5];
  u32x4 ones = {0x3f803f80u, 0x3f803f80u, 0x3f803f80u, 0x3f803f80u};
  asm volatile("" : "+v"(ones));
  h_issue<HP, 1, true>(S);
  h_issue<HP, 0>(S);
  h_issue<HP, 1>(S);
  if (HP == 0) {
    if ((threadIdx.x & 63) < 16) asm volatile("s_mov_b32 m0, %1\n\ts_nop 0\n\tglobal_load_lds_dword %0, off" ::"v"(S.touch), "s"(S.ldsbase + K1_SCR) : "memory");
  }
  mb_wait(S.lds, 0);
  h_read(S, wa);
  mb_wait(S.lds, 1);
  h_step<HP, -1, 1>(S, wa, wb, cs, ones);
  h_step<HP, 3, 2>(S, wb, wa, cs, ones);
#pragma unroll 1
  for (int s = 2; s < 14; s += 2) {
    h_step<HP, 3, 1>(S, wa, wb, cs, ones);
    if (s >= 6) h_retile<HP>(S, s);
    h_step<HP, 3, 2>(S, wb, wa, cs, ones);
    if (s >= 6) h_retile<HP>(S, s + 1);
  }
  h_step<HP, 3, 1>(S, wa, wb, cs, ones);
  h_retile<HP>(S, 14);
  const int lane = threadIdx.x & 63;
  h_step<HP, 3, 0>(S, wb, wa, cs, ones);
  h_retile<HP>(S, 15);
  if (S.pc2) {
    asm volatile("s_mov_b32 m0, %1\n\ts_nop 0\n\tglobal_load_lds_dword %0, off" ::"v"(S.pc2 + min(CODE2_OFF + HP * 8192 + lane * 128, CODE_BYTES)), "s"(S.ldsbase + K1_SCR) : "memory");
  }
  h_step<HP, 2, 0>(S, wa, wb, cs, ones);
  h_retile<HP>(S, 16);
  h_step<HP, 1, 0>(S, wb, wa, cs, ones);
  h_step<HP, 0, 0>(S, wa, wb, cs, ones);
  if (lane == 0) {
    if (__hip_atomic_fetch_add((LDSP unsigned*)(S.lds + K1_MB + 8), 1u, __ATOMIC_RELAXED, __HIP_MEMORY_SCOPE_WORKGROUP) == 1u)
      __builtin_amdgcn_raw_buffer_store_b64(*(LDSP u32x2*)(S.lds + F_TAG), S.rs, OFF_FLR + blockIdx.x * 64, 0, 16);
  }
#pragma unroll
  for (int c = 0; c < 5; ++c) cs[c] = mfma(wb[c], ones, cs[c]);
  __builtin_amdgcn_s_setprio(0);
}

DEVI void phase1(LDSP char* lds, unsigned long long kpc, const u16* __restrict__ x, const u16* __restrict__ wqkv,
                 const u16* __restrict__ wphi, const u16* __restrict__ wo, const u16* __restrict__ w1,
                 const u16* __restrict__ b1, const u16* __restrict__ w2, char* __restrict__ ws) {
  const int tid = threadIdx.x, lane = tid & 63;
  const int wave = __builtin_amdgcn_readfirstlane(tid >> 6);
  const int b = blockIdx.x, xcd = b & 7, jx = b >> 3;
  const unsigned ldsbase = (unsigned)(size_t)lds;
  const bool helper = wave >= 6;
  if (helper) {
    const char* pc = (const char*)(kpc & ~(unsigned long long)127) + min(((jx & 3) * 2 + (wave & 1)) * 8192 + lane * 128, CODE_BYTES);
    asm volatile("s_mov_b32 m0, %1\n\ts_nop 0\n\tglobal_load_lds_dword %0, off" ::"v"(pc), "s"(ldsbase + K1_SCR) : "memory");
  }
  if (tid == 0) { *(LDSP u32x4*)(lds + K1_MB) = u32x4{0u, 0u, 0u, 0u}; *(LDSP u32x2*)(lds + F_TAG + 8) = u32x2{0u, 0u}; }
  WAIT_LGKM0();
  asm volatile("" ::"s"(x), "s"(wqkv), "s"(wphi), "s"(wo), "s"(w1), "s"(b1), "s"(w2), "s"(ws));
  const int rb = 8 * (xcd >> 2) + (jx & 7);
  const char* xrow = (const char*)x + (size_t)rb * 128 * 1280;
  auto xvoff = [&](int pc) {
    const int row = pc * 8 + (lane >> 3);
    return (unsigned)(row * 1280 + (((lane & 7) ^ ((row >> 1) & 7)) << 4));
  };
  const unsigned x0 = xvoff(wave), x1 = x0 + 48 * 1280;
  if (!helper) {
    glds16n(x0, xrow, ldsbase + wave * 1024);
    glds16n(x0, xrow + 64 * 1280, ldsbase + 8192 + wave * 1024);
    if (wave < 2) {
      glds16n(x1, xrow, ldsbase + wave * 1024 + 6144);
      glds16n(x1, xrow + 64 * 1280, ldsbase + 8192 + wave * 1024 + 6144);
    }
  }
  __builtin_amdgcn_s_barrier();
  const int mh = wave & 1;
  const int nj = wave < 4 ? wave >> 1 : 2;
  const int cj = helper ? 2 * mh : nj;
  const int g = lane >> 4, m = lane & 15, q4 = m >> 2, p4 = lane & 3;
  const int cgi = jx >> 3;
  const bool atype = cgi < 2;
  const int grp = 2 * (xcd & 3) + (cgi & 1);
  const u16* W = atype ? wqkv : w1;
  const int ldw = atype ? 1920 : 1728;
  const int c0 = atype ? 80 * grp : (grp < 6 ? 224 * grp : 1344 + 192 * (grp - 6));
  const int sub = atype ? 640 : (grp < 6 ? 80 : 64);
  const unsigned incw = 64 * ldw;
  const int t1 = 1 + 3 * (((jx & 7) + cgi) % 3);
  auto wvoff = [&](int id) {
    const int sj = id / 5, ss = id - 5 * sj;
    const int c = 64 * ss + lane, rho = c / 10, cc = c - 10 * rho;
    const int k = (rho & 0x13) | ((rho & 4) << 1) | ((rho & 8) >> 1);
    const bool narrow = !atype && (grp >= 6 || sj == 2);
    return (unsigned)(k * ldw + c0 + sub * sj + 8 * ((narrow && cc >= 8) ? cc - 2 : cc)) * 2;
  };
  const unsigned wa0 = K1_W + cj * WSUB + (16 * (g >> 1) + 4 * (g & 1) + q4) * 160 + p4 * (atype ? 40 : 8);
  const unsigned cstr = atype ? 8 : 32;
  const __amdgpu_buffer_rsrc_t rs = __builtin_amdgcn_make_buffer_rsrc((void*)ws, 0, WS_END, 0x00020000);
  f32x4 acc[4][5];

  if (!helper) {
    auto xgrp = [&](int u, int xt) {
      const char* px = xrow + xt * 128 + (u & 1) * (64 * 1280);
      const unsigned d = ldsbase + u * 8192 + wave * 1024;
      glds16n(x0, px, d);
      if (wave < 2) glds16n(x1, px, d + 6144);
    };
    const unsigned dw = ldsbase + K1_W + wave * 1024;
    const unsigned v0 = wvoff(wave);
    glds16n(v0, W, dw);
    const unsigned v1 = wvoff(wave + 6);
    glds16n(v1, W, dw + 6144);
    const unsigned v2 = wvoff(wave < 3 ? wave + 12 : 14);
    if (wave < 3) glds16n(v2, W, dw + 12288);
    glds16n(v0, (const char*)W + incw, dw + WSLOT);
    glds16n(v1, (const char*)W + incw, dw + WSLOT + 6144);
    if (wave < 3) glds16n(v2, (const char*)W + incw, dw + WSLOT + 12288);
    xgrp(2, t1);
    K1C S;
    S.lds = lds;
    S.xa = (mh * 64 + m) * 128 + ((g ^ (m >> 1)) << 4);
#pragma unroll
    for (int c = 0; c < 5; ++c) S.wa[c] = wa0 + c * cstr;
    S.wr = 0;
    f32x4 cs[3], s1[4], s2[4];
#pragma unroll
    for (int i = 0; i < 4; ++i) {
      s1[i] = f32x4{0.f, 0.f, 0.f, 0.f}; s2[i] = s1[i]; cs[i % 3] = s1[i];
#pragma unroll
      for (int c = 0; c < 5; ++c) acc[i][c] = s1[i];
    }
    if (wave == 4 && lane < 48) *(LDSP u32x4*)(lds + K1_ZERO + lane * 16) = u32x4{0u, 0u, 0u, 0u};
    const bool five = atype || (grp < 6 && nj < 2);
    if (wave < 2) WAIT_VM(5); else if (wave == 2) WAIT_VM(4); else WAIT_VM(3);
    mb_arrive(lds, 0);
    {
      const int col = c0 + sub * (lane / 10) + 8 * (lane % 10);
      const unsigned vo = atype ? tid * 16 : (col > 1720 ? 1720 : col) * 2;
      if (tid < (atype ? 200 : 30)) glds16n(vo, atype ? (const char*)wphi + grp * 3200 : (const char*)b1, ldsbase + K1_WPHI + wave * 1024);
    }
    if (tid == 320) {
      const u32x2 e = __builtin_amdgcn_raw_buffer_load_b64(rs, OFF_SYNC, 0, 16);
      const unsigned long long t = (((unsigned long long)e[1] << 32) | e[0]) * 6364136223846793005ull + 1442695040888963407ull;
      *(LDSP u32x2*)(lds + F_TAG) = u32x2{(unsigned)t, (unsigned)(t >> 32)};
    }
    mb_wait(lds, 0);
    if (nj == 1) {
      if (five) { if (mh == 0) c_main<5, 1>(S, acc, cs, s1, s2); else c_main<5, 2>(S, acc, cs, s1, s2); }
      else { if (mh == 0) c_main<4, 1>(S, acc, cs, s1, s2); else c_main<4, 2>(S, acc, cs, s1, s2); }
      const int cb = mh == 0 ? 0 : (five ? 3 : 2), cn = mh == 0 ? (five ? 3 : 2) : 2;
      if (m == 0) {
#pragma unroll
        for (int c = 0; c < 3; ++c)
          if (c < cn) *(LDSP f32x4*)(lds + K1_CS + (80 + (cb + c) * 16 + 4 * g) * 4) = cs[c];
      }
      if (g == q4) {
#pragma unroll
        for (int r = 0; r < 4; ++r) {
          const float sq = (m & 2) ? ((m & 1) ? s2[r][3] : s2[r][2]) : ((m & 1) ? s2[r][1] : s2[r][0]);
          const float mean = s1[r][0] * (1.0f / 640.0f);
          const float var = fmaxf(sq * (1.0f / 640.0f) - mean * mean, 0.f);
          float rsd = __builtin_amdgcn_rsqf(var + 1e-5f);
          if (!atype) rsd *= L2E;
          *(LDSP f32x2*)(lds + K1_STAT + (mh * 64 + r * 16 + m) * 8) = f32x2{rsd, -mean * rsd};
        }
      }
      if (atype) {
#pragma unroll
        for (int r = 2; r < 4; ++r)
#pragma unroll
          for (int c = 0; c < 5; ++c) *(LDSP f32x4*)(lds + K1_HO + (mh * 2 + r - 2) * 5120 + c * 1024 + lane * 16) = acc[r][c];
      }
    } else {
      if (five) c_main<5, 0>(S, acc, cs, s1, s2); else c_main<4, 0>(S, acc, cs, s1, s2);
      if (!atype) {
#pragma unroll
        for (int c = 0; c < 5; ++c) *(LDSP f32x4*)(lds + K1_HO + (mh * 2 + (nj >> 1)) * 5120 + c * 1024 + lane * 16) = acc[3][c];
        if (nj == 0 && tid < 120) {
          const unsigned w = *(LDSP unsigned*)(lds + K1_WPHI + tid * 4);
          *(LDSP f32x2*)(lds + K1_BL + tid * 8) = f32x2{bflo(w) * L2E, bfhi(w) * L2E};
        }
      }
    }
  } else {
    __builtin_amdgcn_s_setprio(3);
    K1H S;
    S.lds = lds; S.ldsbase = ldsbase;
    S.incw = incw;
    S.pw = (const char*)W + 2 * t1 * incw;
    S.wrapw = (unsigned)(size_t)W + 20 * incw;
    S.px = xrow + t1 * 128 + 64 * 1280;
    S.wrapx = (unsigned)(size_t)xrow + 1280;
    S.dw = 2 * WSLOT; S.dx = 3 * 8192; S.wr = 0;
#pragma unroll
    for (int i = 0; i < 4; ++i) {
      S.vx[i] = xvoff(4 * mh + i);
      glds16n(S.vx[i], S.px, ldsbase + mh * 4096 + i * 1024 + 3 * 8192);
    }
#pragma unroll
    for (int i = 0; i < 8; ++i) {
      S.vw[i] = wvoff(8 * mh + i < 15 ? 8 * mh + i : 14);
      if (8 * mh + i < 15) glds16n(S.vw[i], S.pw, ldsbase + K1_W + mh * 8192 + i * 1024 + 2 * WSLOT);
    }
    S.touch = lane < 12 ? ws + (size_t)lane * (2u << 20) : lane == 12 ? (const char*)w2 : lane == 13 ? (const char*)w2 + (2u << 20) : lane == 14 ? (const char*)wo : (const char*)wphi;
#pragma unroll
    for (int c = 0; c < 5; ++c) { S.wa[c] = wa0 + c * cstr; acc[0][c] = f32x4{0.f, 0.f, 0.f, 0.f}; }
    S.rs = rs; S.l16 = lane * 16; S.w2 = (const char*)w2; S.wo = (const char*)wo;
    S.rrd = K1_RT + mh * 5120 + (lane >> 2) * 32 + (lane & 3) * 8;
    S.gi = b * 12 + mh * 6;
    S.rv2 = (lane >> 1) * 1280 + (lane & 1) * 16;
    S.rvo = ((((lane >> 1) & 12) << 1) | (((lane >> 1) & 16) >> 2) | ((lane >> 1) & 3)) * 1280 + (lane & 1) * 16;
    S.pc2 = jx < 2 ? (const char*)(kpc & ~(unsigned long long)127) : nullptr;
    if (mh == 0) h_main<0>(S, acc[0]); else h_main<1>(S, acc[0]);
    if (m == 0) {
#pragma unroll
      for (int c = 0; c < 5; ++c) *(LDSP f32x4*)(lds + K1_CS + (cj * 80 + c * 16 + 4 * g) * 4) = acc[0][c];
    }
  }
  WAIT_LGKM0();
  __builtin_amdgcn_s_barrier();
  asm volatile("" ::: "memory");

  if (!atype) {
    auto round = [&](const f32x4 (&a)[5], int ej, int rt) {
      const bool five = grp < 6 && ej < 2;
      const f32x2 st = *(LDSP f32x2*)(lds + K1_STAT + (rt * 16 + m) * 8);
      u32x2 pk[5];
#pragma unroll
      for (int c = 0; c < 5; ++c) {
        if (c == 4 && !five) { pk[4] = u32x2{0u, 0u}; break; }
        const f32x4 cs = *(LDSP f32x4*)(lds + K1_CS + (ej * 80 + c * 16 + 4 * g) * 4);
        const f32x4 bl = *(LDSP f32x4*)(lds + K1_BL + (ej * 80 + c * 16 + 4 * g) * 4);
        float res[4];
#pragma unroll
        for (int e = 0; e < 4; ++e) {
          const float hl = fmaf(a[c][e], st[0], fmaf(st[1], cs[e], bl[e]));
          const float ex = __builtin_amdgcn_exp2f(hl);
          const float rc = __builtin_amdgcn_rcpf(fmaf(ex, ex + 2.f, 2.f));
          res[e] = hl * fmaf(rc, -2.f * LN2, LN2);
        }
        pk[c] = u32x2{pk2(res[0], res[1]), pk2(res[2], res[3])};
      }
      const int T0 = (c0 + sub * ej) >> 4;
      const unsigned base = OFF_ACT + ((rb * 8 + rt) * 54 + (T0 >> 1)) * 1024;
      if (!(T0 & 1)) {
        st16(rs, u32x4{pk[0][0], pk[0][1], pk[1][0], pk[1][1]}, lane * 16, base);
        st16(rs, u32x4{pk[2][0], pk[2][1], pk[3][0], pk[3][1]}, lane * 16, base + 1024);
        if (five) st8(rs, pk[4], lane * 16, base + 2048);
      } else {
        st8(rs, pk[0], lane * 16, base + 8);
        st16(rs, u32x4{pk[1][0], pk[1][1], pk[2][0], pk[2][1]}, lane * 16, base + 1024);
        st16(rs, u32x4{pk[3][0], pk[3][1], pk[4][0], pk[4][1]}, lane * 16, base + 2048);
      }
    };
    if (helper) {
#pragma unroll
      for (int r = 0; r < 2; ++r)
#pragma unroll
        for (int c = 0; c < 5; ++c) acc[r][c] = *(LDSP f32x4*)(lds + K1_HO + (mh * 2 + r) * 5120 + c * 1024 + lane * 16);
    }
#pragma unroll 1
    for (int it = 0; it < (helper ? 1 : 2); ++it) {
      round(acc[0], helper ? 0 : nj, mh * 4 + (helper ? 3 : 2 * it));
      if (it == 0 || nj == 1) round(acc[1], helper ? 2 : nj, mh * 4 + (helper ? 3 : 2 * it + 1));
#pragma unroll
      for (int c = 0; c < 5; ++c) { acc[0][c] = acc[2][c]; acc[1][c] = acc[3][c]; }
    }
    return;
  }

  auto normalise = [&](const f32x4 (&a)[5], const f32x4 (&cs)[5], int rt, u32x4 (&bq)[3], unsigned one) {
    const f32x2 st = *(LDSP f32x2*)(lds + K1_STAT + (rt * 16 + m) * 8);
#pragma unroll
    for (int c = 0; c < 5; ++c) {
      float v[4];
#pragma unroll
      for (int e = 0; e < 4; ++e) v[e] = fmaf(a[c][e], st[0], st[1] * cs[c][e]);
      bq[c >> 1][(c & 1) * 2] = pk2(v[0], v[1]);
      bq[c >> 1][(c & 1) * 2 + 1] = pk2(v[2], v[3]);
    }
    bq[2][2] = one;
    bq[2][3] = 0u;
  };
  f32x4 csv[5];
  if (!helper && nj != 1) {
#pragma unroll
    for (int c = 0; c < 5; ++c) csv[c] = *(LDSP f32x4*)(lds + K1_CS + (nj * 80 + c * 16 + 4 * g) * 4);
#pragma unroll
    for (int r = 0; r < 4; ++r) {
      u32x4 bq[3];
      normalise(acc[r], csv, mh * 4 + r, bq, nj == 2 ? 0x00003f80u : 0u);
      if (nj == 0) {
        const unsigned so = OFF_QT + (4 * grp * 128 + rb * 8 + mh * 4 + r) * 768;
#pragma unroll
        for (int i = 0; i < 3; ++i) st16(rs, bq[i], g * (128 * 768) + m * 16, so + i * 256);
      } else {
#pragma unroll
        for (int i = 0; i < 3; ++i) *(LDSP u32x4*)(lds + K1_VIMG + (mh * 64 + r * 16 + m) * V_PITCH + g * 48 + i * 16) = bq[i];
      }
    }
  }
  if (helper || nj != 2) {
    const int kr0 = helper ? 3 : (nj == 1 ? 0 : 2);
    if (kr0) {
#pragma unroll
      for (int c = 0; c < 5; ++c) acc[0][c] = *(LDSP f32x4*)(lds + K1_HO + (mh * 2 + kr0 - 2) * 5120 + c * 1024 + lane * 16);
    }
#pragma unroll
    for (int c = 0; c < 5; ++c) csv[c] = *(LDSP f32x4*)(lds + K1_CS + (80 + c * 16 + 4 * g) * 4);
    LDSP char* wl = lds + ((p4 == g) ? K1_WPHI + g * 800 + q4 * 40 : K1_ZERO);
#pragma unroll 1
    for (int kr = kr0; kr < (kr0 ? kr0 + 1 : 2); ++kr) {
      u32x4 bq[3];
      normalise(acc[0], csv, mh * 4 + kr, bq, 0u);
      f32x4 pk[5];
      head_proj(wl, bq, pk);
      unsigned ph[20];
      feature_map(pk, ph);
      LDSP char* dst = lds + (mh * 64 + kr * 16 + m) * PK_PITCH + g * 80;
#pragma unroll
      for (int i = 0; i < 5; ++i) *(LDSP u32x4*)(dst + i * 16) = u32x4{ph[4 * i], ph[4 * i + 1], ph[4 * i + 2], ph[4 * i + 3]};
#pragma unroll
      for (int c = 0; c < 5; ++c) acc[0][c] = acc[1][c];
    }
  }
  WAIT_LGKM0();
  __builtin_amdgcn_s_barrier();
  asm volatile("" ::: "memory");
  {
#pragma unroll 1
    for (int job = wave; job < 12; job += 8) {
      const int hh = job & 3, ft = job >> 2;
      LDSP char* pa = lds + (4 * g + q4) * PK_PITCH + hh * 80 + ft * 32 + p4 * 8;
      LDSP char* pb = lds + K1_VIMG + (4 * g + q4) * V_PITCH + hh * 48 + p4 * 8;
      f32x4 d0 = {0.f, 0.f, 0.f, 0.f}, d1 = {0.f, 0.f, 0.f, 0.f};
#pragma unroll
      for (int kk = 0; kk < 4; ++kk) {
        u32x4 a = trread2(pa + (32 * kk) * PK_PITCH, pa + (32 * kk + 16) * PK_PITCH);
        u32x4 v0 = trread2(pb + (32 * kk) * V_PITCH, pb + (32 * kk + 16) * V_PITCH);
        u32x4 v1 = trread2(pb + (32 * kk) * V_PITCH + 32, pb + (32 * kk + 16) * V_PITCH + 32);
        d0 = mfma(a, v0, d0);
        d1 = mfma(a, v1, d1);
      }
      const unsigned so = OFF_KVP + ((4 * grp + hh) * 16 + rb) * KVP_B + 1344 * ft;
      st16(rs, __builtin_bit_cast(u32x4, d0), m * 64 + g * 16, so);
      if (m < 5) st16(rs, __builtin_bit_cast(u32x4, d1), (16 + m) * 64 + g * 16, so);
    }
  }
}

constexpr unsigned K2_YD = 0;
constexpr unsigned K2_SD = 4 * 15360;
constexpr unsigned K2_KVT = K2_SD + 4 * 20480;
constexpr unsigned K2_ZERO = K2_KVT + 1920;
constexpr unsigned K2_WPHI = K2_ZERO + 1792;
constexpr unsigned K2_FLAG = K2_WPHI + 3200;
constexpr unsigned K2_OWN = K2_FLAG + 16;
constexpr unsigned K2_LDS = K2_OWN + 8192;

DEVI u32x4 bld(__amdgpu_buffer_rsrc_t rs, unsigned voff, unsigned soff) {
  return __builtin_amdgcn_raw_buffer_load_b128(rs, voff, soff, 16);
}
template <int N, class F>
DEVI void y_stream(__amdgpu_buffer_rsrc_t rs, unsigned act, unsigned wt, int rot, bool more, f32x4 (&acc)[4][5], F ready) {
  const unsigned l16 = (threadIdx.x & 63) * 16;
  unsigned ab[4];
#pragma unroll
  for (int l = 0; l < 4; ++l) ab[l] = act + ((l + rot) & 3) * (54 * 1024);
  u32x4 A[3][4], B[3][5];
  auto loada = [&](int i) {
#pragma unroll
    for (int l = 0; l < 4; ++l) A[i % 3][l] = bld(rs, l16, ab[l] + i * 1024);
  };
  auto loadb = [&](int i) {
#pragma unroll
    for (int c = 0; c < 5; ++c) B[i % 3][c] = bld(rs, l16, wt + c * (74 * 1024) + i * 1024);
  };
  auto comp = [&](int i) {
    __builtin_amdgcn_sched_barrier(0);
#pragma unroll
    for (int l = 0; l < 4; ++l)
#pragma unroll
      for (int c = 0; c < 5; ++c) acc[l][c] = mfma(B[i % 3][c], A[i % 3][l], acc[l][c]);
    __builtin_amdgcn_sched_barrier(0);
  };
  loadb(0);
  loadb(1);
  loadb(2);
  ready();
  loada(0);
  loada(1);
#pragma unroll
  for (int i = 0; i < N; ++i) {
    if (i + 2 < N) loada(i + 2);
    else if (i + 2 == N) { if (more) loada(N); }
    comp(i);
    if (i + 3 < N) loadb(i + 3);
    else if (i + 3 == N) { if (more) loadb(N); }
  }
  if (more) comp(N);
}

template <int C0, int C1>
DEVI void combine(LDSP char* lds, const u32x2 (&xv)[3], u16* out, int rt, int rb2, int cb, const f32x4 (&own)[5]) {
  const int lane = threadIdx.x & 63;
  const unsigned l16 = lane * 16;
  const size_t orow = (size_t)(64 * rb2 + 16 * rt + (lane & 15)) * 640 + 80 * cb + 4 * (lane >> 4);
  WAIT_LGKM0();
  __builtin_amdgcn_s_barrier();
  asm volatile("" ::: "memory");
#pragma unroll
  for (int c = C0; c < C1; ++c) {
    f32x4 y = C0 == 0 ? own[c] : *(LDSP f32x4*)(lds + K2_OWN + (rt * 2 + c - 3) * 1024 + l16);
#pragma unroll
    for (int l = 1; l < 4; ++l)
      y += *(LDSP f32x4*)(lds + K2_YD + ((rt - l) & 3) * 15360 + ((l - 1) * 5 + c) * 1024 + l16);
    LDSP f32x4* sp = (LDSP f32x4*)(lds + K2_SD + (rt * 5 + c) * 1024 + l16);
    const f32x4 s = (sp[0] + sp[1280]) + (sp[2560] + sp[3840]);
    float o[4];
#pragma unroll
    for (int e = 0; e < 4; ++e) {
      const float gate = y[e] * __builtin_amdgcn_rcpf(1.f + __builtin_amdgcn_exp2f(-y[e] * L2E));
      const unsigned xw = xv[c - C0][e >> 1];
      o[e] = fmaf(s[e], gate, (e & 1) ? bfhi(xw) : bflo(xw));
    }
    *(u32x2*)(out + orow + 16 * c) = u32x2{pk2(o[0], o[1]), pk2(o[2], o[3])};
  }
}

template <int NW>
DEVI bool poll_tags(__amdgpu_buffer_rsrc_t rs, unsigned off, int cnt, u32x2 tag) {
  const int lane = threadIdx.x & 63;
  const unsigned vo = (lane < cnt ? lane : 0) * 64;
  for (int spin = 0; spin < (1 << 18); ++spin) {
    asm volatile("" ::: "memory");
    bool ok = true;
#pragma unroll
    for (int i = 0; i < NW; ++i) {
      const u32x2 w = __builtin_amdgcn_raw_buffer_load_b64(rs, vo, off + i * 4096, 16);
      ok = ok && w[0] == tag[0] && w[1] == tag[1];
    }
    if (__all(ok)) { asm volatile("" ::: "memory"); return true; }
    __builtin_amdgcn_s_sleep(4);
  }
  return false;
}

DEVI void go_set(LDSP char* lds, int i) {
  if ((threadIdx.x & 63) == 0) *(volatile LDSP unsigned*)(lds + F_TAG + 8 + 4 * i) = 1u;
}
DEVI void go_wait(LDSP char* lds, int i) {
  for (int spin = 0; spin < (1 << 21); ++spin) {
    if (__builtin_amdgcn_readfirstlane(*(volatile LDSP unsigned*)(lds + F_TAG + 8 + 4 * i))) break;
    __builtin_amdgcn_s_sleep(2);
  }
  asm volatile("" ::: "memory");
}

DEVI void phase2(LDSP char* lds, u32x2 tag, bool seen, const u16* __restrict__ x, const u16* __restrict__ wphi, char* __restrict__ ws,
                 u16* __restrict__ out) {
  const int tid = threadIdx.x, lane = tid & 63;
  const int wave = __builtin_amdgcn_readfirstlane(tid >> 6);
  const int g = lane >> 4, m = lane & 15, q4 = m >> 2, p4 = lane & 3;
  const int b = blockIdx.x, xcd = b & 7, jx = b >> 3;
  const int rb2 = 8 * (xcd >> 1) + (jx & 7), cb = 4 * (xcd & 1) + (jx >> 3);
  const __amdgpu_buffer_rsrc_t rs = __builtin_amdgcn_make_buffer_rsrc((void*)ws, 0, WS_END, 0x00020000);
  const unsigned l16 = lane * 16;
  asm volatile("" ::"s"(x), "s"(wphi), "s"(out));
  if (wave == 4) {
    *(LDSP u32x4*)(lds + K2_ZERO + l16) = u32x4{0u, 0u, 0u, 0u};
    if (lane < 48) *(LDSP u32x4*)(lds + K2_ZERO + 1024 + l16) = u32x4{0u, 0u, 0u, 0u};
    if (lane == 0) *(LDSP u32x4*)(lds + K2_FLAG) = u32x4{0u, 0u, 0u, 0u};
  }
  const unsigned act = OFF_ACT + 4 * rb2 * (54 * 1024), wt = OFF_WT + 5 * cb * (74 * 1024);
  const u16* xt = x + (size_t)(64 * rb2 + 16 * (wave & 3) + m) * 640 + 80 * cb + 4 * g;
  f32x4 acc[4][5];
#pragma unroll
  for (int r = 0; r < 4; ++r)
#pragma unroll
    for (int c = 0; c < 5; ++c) acc[r][c] = f32x4{0.f, 0.f, 0.f, 0.f};

  if (wave < 4) {
    u32x2 xv[3];
#pragma unroll
    for (int c = 0; c < 3; ++c) xv[c] = *(const u32x2*)(xt + 16 * c);
    const unsigned k0 = (wave < 2 ? 14 * wave : 2 + 13 * wave) * 1024;
    y_stream<13>(rs, act + k0, wt + k0, wave, wave < 2, acc, [&]() {
      if (wave == 0 && !seen) {
        poll_tags<1>(rs, OFF_FL1 + (128 + (rb2 >> 1) * 8) * 64, 8, tag);
        go_set(lds, 0);
      } else {
        go_wait(lds, 0);
      }
    });
#pragma unroll
    for (int l = 1; l < 4; ++l)
#pragma unroll
      for (int c = 0; c < 5; ++c)
        *(LDSP f32x4*)(lds + K2_YD + wave * 15360 + ((l - 1) * 5 + c) * 1024 + l16) = acc[l][c];
#pragma unroll
    for (int c = 3; c < 5; ++c) *(LDSP f32x4*)(lds + K2_OWN + (wave * 2 + c - 3) * 1024 + l16) = acc[0][c];
    combine<0, 3>(lds, xv, out, wave, rb2, cb, acc[0]);
    if (b == 0 && wave == 0 && poll_tags<4>(rs, OFF_FLR, 64, tag) && lane == 0) __builtin_amdgcn_raw_buffer_store_b64(tag, rs, OFF_SYNC, 0, 16);
    return;
  }

  const int aw = wave - 4;
  const int h = 4 * xcd + (jx & 3), sb = jx >> 2;
  const int tok = sb * 256 + aw * 64 + lane;
  __builtin_amdgcn_s_setprio(2);
  u32x4 wv = *(const u32x4*)(wphi + h * 400 + (lane < 50 ? lane : 49) * 8);
  u32x4 B[5][5];
#pragma unroll
  for (int i = 0; i < K2_BPRE; ++i)
#pragma unroll
    for (int c = 0; c < 5; ++c) B[i][c] = bld(rs, l16, wt + c * (74 * 1024) + (54 + 5 * aw + i) * 1024);
  if (aw == 0) {
    poll_tags<1>(rs, OFF_FL1 + (h >> 2) * 16 * 64, 16, tag);
    WAIT_LGKM0();
    go_set(lds, 1);
  } else {
    go_wait(lds, 1);
  }
  u32x4 bq[3];
#pragma unroll
  for (int s = 0; s < 3; ++s) bq[s] = bld(rs, (unsigned)(tok >> 4) * 768 + m * 16, OFF_QT + h * (128 * 768) + s * 256);
  int item = aw * 53 + (lane < 53 ? lane : 52);
  item = item < 210 ? item : 209;
  const int d = item / 10, fq = item - 10 * d;
  u32x4 part[16];
#pragma unroll
  for (int r = 0; r < 16; ++r) part[r] = bld(rs, (unsigned)((fq >> 2) * 1344 + d * 64 + (fq & 3) * 16), OFF_KVP + (h * 16 + r) * KVP_B);
  *(LDSP u32x4*)(lds + K2_WPHI + aw * 800 + (lane < 50 ? lane : 49) * 16) = wv;
  unsigned ph[20];
  {
    LDSP char* wl = lds + ((p4 == g) ? K2_WPHI + aw * 800 + q4 * 40 : K2_ZERO);
    f32x4 pq[5];
    head_proj(wl, bq, pq);
    feature_map(pq, ph);
#pragma unroll
    for (int i = 0; i < 20; ++i) asm volatile("" : "+v"(ph[i]));
  }
  {
    f32x4 sum = __builtin_bit_cast(f32x4, part[0]);
#pragma unroll
    for (int r = 1; r < 16; ++r) sum += __builtin_bit_cast(f32x4, part[r]);
    *(LDSP u32x2*)(lds + K2_KVT + d * 80 + fq * 8) = u32x2{pk2(sum[0], sum[1]), pk2(sum[2], sum[3])};
    WAIT_LGKM0();
    if (lane == 0) *(volatile LDSP unsigned*)(lds + K2_FLAG + aw * 4) = 1u;
    for (int spin = 0; spin < (1 << 20); ++spin) {
      const u32x4 f = *(volatile LDSP u32x4*)(lds + K2_FLAG);
      if (__builtin_amdgcn_readfirstlane(f[0] & f[1] & f[2] & f[3])) break;
      __builtin_amdgcn_s_sleep(1);
    }
    asm volatile("" ::: "memory");
  }
  {
    LDSP char* kl = lds + (((m >> 2) == g) ? K2_KVT + (m & 3) * 80 : K2_ZERO);
    f32x4 nd[6];
#pragma unroll
    for (int t = 0; t < 6; ++t) {
      f32x4 c = {0.f, 0.f, 0.f, 0.f};
#pragma unroll
      for (int s = 0; s < 5; ++s)
        c = mfma(*(LDSP u32x4*)(kl + t * 320 + s * 16), u32x4{ph[4 * s], ph[4 * s + 1], ph[4 * s + 2], ph[4 * s + 3]}, c);
      nd[t] = c;
    }
    const float rd = __builtin_amdgcn_rcpf(nd[5][0] + 1e-6f);
    unsigned ow[10];
#pragma unroll
    for (int t = 0; t < 5; ++t) {
      ow[2 * t] = pk2(nd[t][0] * rd, nd[t][1] * rd);
      ow[2 * t + 1] = pk2(nd[t][2] * rd, nd[t][3] * rd);
    }
    const int e0 = (5 * h + 1) >> 1, e8 = (h & 1) ? e0 - 1 : e0 + 2;
    const unsigned vo = (unsigned)(tok >> 4) * 20480 + m * 16;
    const bool odd = h & 1;
    st16(rs, odd ? u32x4{ow[2], ow[3], ow[4], ow[5]} : u32x4{ow[0], ow[1], ow[2], ow[3]}, vo, OFF_OT + e0 * 256);
    st16(rs, odd ? u32x4{ow[6], ow[7], ow[8], ow[9]} : u32x4{ow[4], ow[5], ow[6], ow[7]}, vo, OFF_OT + (e0 + 1) * 256);
    st8(rs, odd ? u32x2{ow[0], ow[1]} : u32x2{ow[8], ow[9]}, vo, OFF_OT + e8 * 256 + (h & 1) * 8);
    WAIT_VM(0);
    if (lane == 0) __builtin_amdgcn_raw_buffer_store_b64(tag, rs, OFF_FO + ((sb * 4 + aw) * 32 + h) * 64, 0, 16);
  }
  const unsigned ring = K2_SD + aw * 20480;
  u32x2 xv[3];
#pragma unroll
  for (int c = 0; c < 2; ++c) xv[c] = *(const u32x2*)(xt + 48 + 16 * c);
  xv[2] = xv[1];
  {
    u32x4 A[5][4];
    auto loadb = [&](int i) {
#pragma unroll
      for (int c = 0; c < 5; ++c) B[i][c] = bld(rs, l16, wt + c * (74 * 1024) + (54 + 5 * aw + i) * 1024);
    };
#pragma unroll
    for (int i = K2_BPRE; i < 5; ++i) loadb(i);
    const unsigned ob = OFF_OT + (4 * rb2 * 20 + 5 * aw) * 1024;
    poll_tags<1>(rs, OFF_FO + (rb2 * 32 + 8 * aw) * 64, 8, tag);
#pragma unroll
    for (int i = 0; i < 5; ++i)
#pragma unroll
      for (int l = 0; l < 4; ++l) A[i][l] = bld(rs, l16, ob + l * 20480 + i * 1024);
#pragma unroll
    for (int i = 0; i < 5; ++i) {
      __builtin_amdgcn_sched_barrier(0);
#pragma unroll
      for (int l = 0; l < 4; ++l)
#pragma unroll
        for (int c = 0; c < 5; ++c) acc[l][c] = mfma(B[i][c], A[i][l], acc[l][c]);
      __builtin_amdgcn_sched_barrier(0);
    }
  }
#pragma unroll
  for (int l = 0; l < 4; ++l)
#pragma unroll
    for (int c = 0; c < 5; ++c) *(LDSP f32x4*)(lds + ring + (l * 5 + c) * 1024 + l16) = acc[l][c];
  combine<3, 5>(lds, xv, out, aw, rb2, cb, acc[0]);
}

__global__ __launch_bounds__(512, 2) void hedgehog_fused(const u16* __restrict__ x, const u16* __restrict__ wqkv,
                                                         const u16* __restrict__ wphi, const u16* __restrict__ wo,
                                                         const u16* __restrict__ w1, const u16* __restrict__ b1,
                                                         const u16* __restrict__ w2, char* __restrict__ ws,
                                                         u16* __restrict__ out) {
  extern __shared__ __attribute__((aligned(16))) char smem[];
  LDSP char* lds = (LDSP char*)smem;
  phase1(lds, __builtin_amdgcn_s_getpc(), x, wqkv, wphi, wo, w1, b1, w2, ws);
  int tid = threadIdx.x;
  asm volatile("" : "+v"(tid));
  const __amdgpu_buffer_rsrc_t rs = __builtin_amdgcn_make_buffer_rsrc((void*)ws, 0, WS_END, 0x00020000);
  const u32x2 tag = *(LDSP u32x2*)(lds + F_TAG);
  bool seen = false;
  if (tid < 64) {
    const u32x2 a = __builtin_amdgcn_raw_buffer_load_b64(rs, (tid & 7) * 64, OFF_FL1 + (128 + (4 * ((blockIdx.x & 7) >> 1) + (((blockIdx.x >> 3) & 7) >> 1)) * 8) * 64, 16);
    poll_tags<4>(rs, OFF_FLR, 64, tag);
    seen = __all(a[0] == tag[0] && a[1] == tag[1]);
    asm volatile("" ::: "memory");
    if (seen) go_set(lds, 0);
  }
  WAIT_VM(0);
  WAIT_LGKM0();
  __builtin_amdgcn_s_barrier();
  asm volatile("" ::: "memory");
  if (tid == 0) {
    const int b = blockIdx.x, xcd = b & 7, jx = b >> 3;
    const int rb = 8 * (xcd >> 2) + (jx & 7), grp = 2 * (xcd & 3) + ((jx >> 3) & 1);
    __builtin_amdgcn_raw_buffer_store_b64(tag, rs, OFF_FL1 + (jx < 16 ? grp * 16 + rb : 128 + rb * 8 + grp) * 64, 0, 16);
  }
  phase2(lds, tag, seen, x, wphi, ws, out);
}

extern "C" void kernel_launch(void* const* d_in, const int* in_sizes, int n_in, void* d_out, int out_size, void* d_ws,
                              size_t ws_size, hipStream_t stream) {
  const u16* x = (const u16*)d_in[0];
  const u16* wqkv = (const u16*)d_in[1];
  const u16* wphi = (const u16*)d_in[2];
  const u16* wo = (const u16*)d_in[3];
  const u16* w1 = (const u16*)d_in[5];
  const u16* b1 = (const u16*)d_in[6];
  const u16* w2 = (const u16*)d_in[7];
  (void)hipFuncSetAttribute((const void*)hedgehog_fused, hipFuncAttributeMaxDynamicSharedMemorySize, F_LDS);
  hedgehog_fused<<<256, 512, F_LDS, stream>>>(x, wqkv, wphi, wo, w1, b1, w2, (char*)d_ws, (u16*)d_out);
}
```
